```python
import torch

def _softmax(x, axis=-1):
    return torch.softmax(x, dim=axis)
def _gelu(x):
    return torch.nn.functional.gelu(x, approximate="tanh")
def _silu(x):
    return torch.nn.functional.silu(x)
def _sigmoid(x):
    return torch.sigmoid(x)
def _layernorm(x, axis=-1, eps=1e-5):
    m = x.mean(dim=axis, keepdim=True)
    v = x.var(dim=axis, keepdim=True, unbiased=False)
    return (x - m) / torch.sqrt(v + eps)
def _scatter_add(tgt, idx, src, axis=0):
    return tgt.clone().index_add_(axis, idx, src)
def _segment_sum(src, seg, n):
    out = torch.zeros((n,) + src.shape[1:], dtype=src.dtype)
    return out.index_add_(0, seg, src)
def _pad(x, pw):
    flat = tuple(v for pair in reversed(pw) for v in pair)
    return torch.nn.functional.pad(x, flat)

kernel_name = 'parallel_sum__ssm_conv_s5__glu_gated_swiglu__prompt'

def setup_inputs():

    _w = (lambda a: (lambda t: t.to(torch.bfloat16) if t.is_floating_point() else t)(torch.as_tensor(a)))
    def _A_setup():
        B, S, D = 1, 2048, 576
        P = D
        Lambda_re = -0.5 * (lambda _x: torch.exp(torch.as_tensor(_x)))((lambda *s: torch.randn(*s, dtype=torch.bfloat16) * 0.2)(P))
        Lambda_im = (lambda *s: torch.randn(*s, dtype=torch.bfloat16) * 0.8)(P) * 1.0
        B_re = (lambda *s: torch.randn(*s, dtype=torch.bfloat16) * 0.2)(P, D) * (D ** -0.5)
        B_im = (lambda *s: torch.randn(*s, dtype=torch.bfloat16) * 0.2)(P, D) * (D ** -0.5)
        C_re = (lambda *s: torch.randn(*s, dtype=torch.bfloat16) * 0.2)(D, P) * (P ** -0.5)
        C_im = (lambda *s: torch.randn(*s, dtype=torch.bfloat16) * 0.2)(D, P) * (P ** -0.5)
        log_dt = (lambda *s: torch.randn(*s, dtype=torch.bfloat16) * 0.2)(P) * 0.3 - 2.0
        Dskip = (lambda *s: torch.randn(*s, dtype=torch.bfloat16) * 0.05)(D)
        return {
            "x": _w((lambda *s: torch.randn(*s, dtype=torch.bfloat16) * 0.05)(B, S, D)),
            "Lambda_re": _w(Lambda_re),
            "Lambda_im": _w(Lambda_im),
            "B_re": _w(B_re),
            "B_im": _w(B_im),
            "C_re": _w(C_re),
            "C_im": _w(C_im),
            "log_dt": _w(log_dt),
            "Dskip": _w(Dskip),
        }
    _A_in = _A_setup()

    def _B_setup():
        B, S, D, F = 1, 2048, 576, 4608
        return {
            "x": _w((lambda *s: torch.randn(*s, dtype=torch.bfloat16) * 0.05)(B, S, D)),
            "w1": _w((lambda *s: torch.randn(*s, dtype=torch.bfloat16) * 0.4)(D, F) * (2.0 / D) ** 0.5),
            "w3": _w((lambda *s: torch.randn(*s, dtype=torch.bfloat16) * 0.4)(D, F) * (2.0 / D) ** 0.5),
            "w2": _w((lambda *s: torch.randn(*s, dtype=torch.bfloat16) * 0.4)(F, D) * (2.0 / F) ** 0.5),
        }
    _B_in = _B_setup()

    return {
        "A_x": _A_in["x"],
        "A_Lambda_re": _A_in["Lambda_re"],
        "A_Lambda_im": _A_in["Lambda_im"],
        "A_B_re": _A_in["B_re"],
        "A_B_im": _A_in["B_im"],
        "A_C_re": _A_in["C_re"],
        "A_C_im": _A_in["C_im"],
        "A_log_dt": _A_in["log_dt"],
        "A_Dskip": _A_in["Dskip"],
        "B_x": _B_in["x"],
        "B_w1": _B_in["w1"],
        "B_w3": _B_in["w3"],
        "B_w2": _B_in["w2"],
    }

def reference(A_x, A_Lambda_re, A_Lambda_im, A_B_re, A_B_im, A_C_re, A_C_im, A_log_dt, A_Dskip, B_x, B_w1, B_w3, B_w2):
    def _A_ref(x, Lambda_re, Lambda_im, B_re, B_im, C_re, C_im, log_dt, Dskip):
        Bsz, L, H = x.shape
        Lam = Lambda_re + 1j * Lambda_im
        Bmat = B_re + 1j * B_im
        Cmat = C_re + 1j * C_im
        dt = torch.exp(log_dt)
        Lam_bar = torch.exp(dt * Lam)
        B_bar = ((Lam_bar - 1.0) / Lam)[:, None] * Bmat
        Bu = torch.einsum("bld,pd->blp", x + 0j, B_bar)
        h = torch.zeros_like(Bu[:, 0, :])
        hs = []
        for t in range(L):
            h = Lam_bar[None, :] * h + Bu[:, t, :]
            hs.append(h)
        hseq = torch.stack(hs, 1)
        y = 2.0 * torch.einsum("blp,dp->bld", hseq, Cmat).real + x * Dskip[None, None, :]
        return y

    def _B_ref(x, w1, w3, w2):
        gate = torch.einsum("bsd,df->bsf", x, w1)
        up = torch.einsum("bsd,df->bsf", x, w3)
        h = _silu(gate) * up
        out = torch.einsum("bsf,fd->bsd", h, w2)
        return out

    x = A_x
    xn = _layernorm(x, axis=-1)
    return x + _A_ref(xn, A_Lambda_re, A_Lambda_im, A_B_re, A_B_im, A_C_re, A_C_im, A_log_dt, A_Dskip) + _B_ref(xn, B_w1, B_w3, B_w2)
```

```cpp
#include <hip/hip_runtime.h>
#include <hip/hip_bf16.h>
#include <stdint.h>

#define L_ 2048
#define D_ 576
#define P_ 576
#define F_ 4608

typedef unsigned short u16;
__device__ __forceinline__ float bf2f(u16 v) { return __uint_as_float(((uint32_t)v) << 16); }
__device__ __forceinline__ u16 f2bf(float f) {
    uint32_t u = __float_as_uint(f);
    u += 0x7FFF + ((u >> 16) & 1);
    return (u16)(u >> 16);
}
__device__ __forceinline__ float rbf(float f) { return bf2f(f2bf(f)); }

__global__ void ln_kernel(const u16* x, u16* xn) {
    int row = blockIdx.x, lane = threadIdx.x;
    float v[9], s = 0.f;
    for (int i = 0; i < 9; i++) { v[i] = bf2f(x[row * D_ + lane + 64 * i]); s += v[i]; }
    for (int o = 32; o; o >>= 1) s += __shfl_xor(s, o);
    float mean = s / D_;
    float q = 0.f;
    for (int i = 0; i < 9; i++) { float d = v[i] - mean; q += d * d; }
    for (int o = 32; o; o >>= 1) q += __shfl_xor(q, o);
    float m = rbf(mean), var = rbf(q / D_);
    float sd = rbf(sqrtf(rbf(var + 1e-5f)));
    for (int i = 0; i < 9; i++) xn[row * D_ + lane + 64 * i] = f2bf(rbf(v[i] - m) / sd);
}

template <int TB, typename TA>
__global__ void gemm_kernel(const TA* A, const u16* B, float* C, int M, int N, int K) {
    __shared__ float As[16][65], Bs[16][65];
    int tx = threadIdx.x & 15, ty = threadIdx.x >> 4;
    int m0 = blockIdx.y * 64, n0 = blockIdx.x * 64;
    float acc[4][4] = {};
    for (int k0 = 0; k0 < K; k0 += 16) {
        for (int i = threadIdx.x; i < 1024; i += 256) {
            int kk = i & 15, mm = i >> 4;
            TA a = A[(size_t)(m0 + mm) * K + k0 + kk];
            if constexpr (sizeof(TA) == 2) As[kk][mm] = bf2f(a); else As[kk][mm] = a;
            if (TB) Bs[kk][mm] = bf2f(B[(size_t)(n0 + mm) * K + k0 + kk]);
            else { int nn = i & 63, k2 = i >> 6; Bs[k2][nn] = bf2f(B[(size_t)(k0 + k2) * N + n0 + nn]); }
        }
        __syncthreads();
        for (int kk = 0; kk < 16; kk++) {
            float a[4], b[4];
            for (int i = 0; i < 4; i++) { a[i] = As[kk][ty * 4 + i]; b[i] = Bs[kk][tx * 4 + i]; }
            for (int i = 0; i < 4; i++) for (int j = 0; j < 4; j++) acc[i][j] += a[i] * b[j];
        }
        __syncthreads();
    }
    for (int i = 0; i < 4; i++) for (int j = 0; j < 4; j++)
        C[(size_t)(m0 + ty * 4 + i) * N + n0 + tx * 4 + j] = acc[i][j];
}

__global__ void swiglu_kernel(const float* g, const float* u, u16* h, int n) {
    int i = blockIdx.x * 256 + threadIdx.x;
    if (i >= n) return;
    float gg = rbf(g[i]), uu = rbf(u[i]);
    float s = rbf(gg / (1.f + __expf(-gg)));
    h[i] = f2bf(s * uu);
}

__global__ void scan_kernel(float* ure, float* uim, const u16* lre, const u16* lim, const u16* ldt) {
    int p = blockIdx.x * 64 + threadIdx.x;
    float a = bf2f(lre[p]), b = bf2f(lim[p]);
    float dt = rbf(expf(bf2f(ldt[p])));
    float e = expf(dt * a), lr = e * cosf(dt * b), li = e * sinf(dt * b);
    float den = a * a + b * b;
    float cr = ((lr - 1.f) * a + li * b) / den, ci = (li * a - (lr - 1.f) * b) / den;
    float hr = 0.f, hi = 0.f;
    for (int t = 0; t < L_; t++) {
        float xr = ure[t * P_ + p], xi = uim[t * P_ + p];
        float br = cr * xr - ci * xi, bi = cr * xi + ci * xr;
        float nr = lr * hr - li * hi + br, ni = lr * hi + li * hr + bi;
        hr = nr; hi = ni;
        ure[t * P_ + p] = hr; uim[t * P_ + p] = hi;
    }
}

__global__ void final_kernel(const u16* x, const u16* xn, const u16* dsk, const float* yr, const float* yi,
                             const float* ffn, float* out, int n) {
    int i = blockIdx.x * 256 + threadIdx.x;
    if (i >= n) return;
    float y = 2.f * (yr[i] - yi[i]) + rbf(bf2f(xn[i]) * bf2f(dsk[i % D_]));
    out[i] = (bf2f(x[i]) + y) + rbf(ffn[i]);
}

extern "C" void kernel_launch(void* const* d_in, const int* in_sizes, int n_in, void* d_out, int out_size,
                              void* d_ws, size_t ws_size, hipStream_t stream) {
    const u16* x = (const u16*)d_in[0];
    char* ws = (char*)d_ws;
    u16* xn = (u16*)ws; ws += (size_t)L_ * D_ * 2;
    float* g = (float*)ws; ws += (size_t)L_ * F_ * 4;
    float* u = (float*)ws; ws += (size_t)L_ * F_ * 4;
    u16* h = (u16*)ws; ws += (size_t)L_ * F_ * 2;
    float* ure = (float*)ws; ws += (size_t)L_ * P_ * 4;
    float* uim = (float*)ws; ws += (size_t)L_ * P_ * 4;
    float* yr = (float*)ws; ws += (size_t)L_ * D_ * 4;
    float* yi = (float*)ws; ws += (size_t)L_ * D_ * 4;
    float* ffn = (float*)ws;
    ln_kernel<<<L_, 64, 0, stream>>>(x, xn);
    gemm_kernel<0, u16><<<dim3(F_ / 64, L_ / 64), 256, 0, stream>>>(xn, (const u16*)d_in[10], g, L_, F_, D_);
    gemm_kernel<0, u16><<<dim3(F_ / 64, L_ / 64), 256, 0, stream>>>(xn, (const u16*)d_in[11], u, L_, F_, D_);
    swiglu_kernel<<<L_ * F_ / 256, 256, 0, stream>>>(g, u, h, L_ * F_);
    gemm_kernel<0, u16><<<dim3(D_ / 64, L_ / 64), 256, 0, stream>>>(h, (const u16*)d_in[12], ffn, L_, D_, F_);
    gemm_kernel<1, u16><<<dim3(P_ / 64, L_ / 64), 256, 0, stream>>>(xn, (const u16*)d_in[3], ure, L_, P_, D_);
    gemm_kernel<1, u16><<<dim3(P_ / 64, L_ / 64), 256, 0, stream>>>(xn, (const u16*)d_in[4], uim, L_, P_, D_);
    scan_kernel<<<P_ / 64, 64, 0, stream>>>(ure, uim, (const u16*)d_in[1], (const u16*)d_in[2], (const u16*)d_in[7]);
    gemm_kernel<1, float><<<dim3(D_ / 64, L_ / 64), 256, 0, stream>>>(ure, (const u16*)d_in[5], yr, L_, D_, P_);
    gemm_kernel<1, float><<<dim3(D_ / 64, L_ / 64), 256, 0, stream>>>(uim, (const u16*)d_in[6], yi, L_, D_, P_);
    final_kernel<<<L_ * D_ / 256, 256, 0, stream>>>(x, xn, (const u16*)d_in[8], yr, yi, ffn, (float*)d_out, L_ * D_);
}
```

```cpp
#include <hip/hip_runtime.h>
#include <stdint.h>

__global__ __launch_bounds__(256, 1) void fused_main(const void* x, const void* lre, const void* lim, const void* ldt,
    const void* bre, const void* bim, const void* cre, const void* cim, const void* w1, const void* w3, const void* w2, void* ws,
    const void* dsk, void* out) {
  __shared__ char smem[163840];
  asm volatile(
R"(.macro Mvv d,a,b
v_mfma_f32_16x16x32_bf16 v[\d:\d+3], v[\a:\a+3], v[\b:\b+3], v[\d:\d+3]
.endm
.macro Zvv d,a,b
v_mfma_f32_16x16x32_bf16 v[\d:\d+3], v[\a:\a+3], v[\b:\b+3], 0
.endm
.macro Mva d,a,b
v_mfma_f32_16x16x32_bf16 v[\d:\d+3], v[\a:\a+3], a[\b:\b+3], v[\d:\d+3]
.endm
.macro Zva d,a,b
v_mfma_f32_16x16x32_bf16 v[\d:\d+3], v[\a:\a+3], a[\b:\b+3], 0
.endm
.macro Mav d,a,b
v_mfma_f32_16x16x32_bf16 v[\d:\d+3], a[\a:\a+3], v[\b:\b+3], v[\d:\d+3]
.endm
.macro Zav d,a,b
v_mfma_f32_16x16x32_bf16 v[\d:\d+3], a[\a:\a+3], v[\b:\b+3], 0
.endm
.macro Maa d,a,b
v_mfma_f32_16x16x32_bf16 v[\d:\d+3], a[\a:\a+3], a[\b:\b+3], v[\d:\d+3]
.endm
.macro Zaa d,a,b
v_mfma_f32_16x16x32_bf16 v[\d:\d+3], a[\a:\a+3], a[\b:\b+3], 0
.endm
.macro TR d,a,o
ds_read_b64_tr_b16 v[\d:\d+1], v\a offset:\o
.endm
.macro VM d,a,b
v_mul_f32 v\d, v\a, v\b
.endm
.macro VC d,a,b
v_cvt_pk_bf16_f32 v\d, v\a, v\b
.endm
.macro VF d,a,b,c
v_fma_f32 v\d, v\a, v\b, v\c
.endm
s_mov_b64 s[8:9], %0
s_mov_b32 s10, %1
v_mov_b32 v0, %2
s_load_dwordx16 s[76:91], s[8:9], 0
s_load_dwordx8 s[92:99], s[8:9], 64
v_lshrrev_b32 v1, 6, v0
s_and_b32 s13, s10, 7
s_lshr_b32 s14, s10, 3
s_and_b32 s12, s14, 1
v_readfirstlane_b32 s11, v1
s_lshl_b32 s13, s13, 1
s_add_u32 s13, s13, s12
s_lshr_b32 s14, s14, 1
s_mov_b32 s4, 0xb321a09f
s_mov_b32 s5, 0xe876d5c4
s_lshl_b32 s13, s13, 2
s_lshr_b64 s[4:5], s[4:5], s13
s_and_b32 s13, s4, 15
s_cmp_eq_u32 s13, 15
s_cbranch_scc1 PB6
s_cmp_gt_u32 s13, 8
s_cbranch_scc1 PB5
PA:
v_and_b32 v0, 63, v0
s_mov_b32 m0, 161792
s_and_b32 s72, s14, 3
s_lshl_b32 s72, s72, 2
s_add_u32 s72, s72, s11
s_mov_b32 s7, 0x20000
s_getpc_b64 s[4:5]
LPCA:
s_mov_b32 s6, LENDA-LPCA
s_lshl_b32 s73, s72, 13
v_lshl_add_u32 v47, v0, 7, s73
buffer_load_dword v47, s[4:7], 0 offen lds
v_and_b32 v32, 15, v0
v_lshrrev_b32 v33, 4, v0
v_lshrrev_b32 v34, 2, v32
v_and_b32 v35, 3, v0
s_and_b32 s64, s11, 1
s_lshr_b32 s65, s11, 1
s_lshl_b32 s66, s64, 6
s_lshl_b32 s67, s65, 3
s_add_u32 s66, s66, s67
v_lshl_add_u32 v36, v33, 4, v34
v_add_u32 v36, s66, v36
s_lshl_b32 s67, s14, 7
v_add_u32 v37, s67, v36
v_mul_u32_u24 v38, 1152, v37
v_lshl_add_u32 v38, v35, 4, v38
v_add_u32 v39, 4608, v38
s_waitcnt lgkmcnt(0)
s_mov_b32 s48, s76
s_mov_b32 s49, s77
s_mov_b32 s50, 2359296
s_mov_b32 s51, 0x20000
buffer_load_dwordx4 a[0:3], v38, s[48:51], 0 offen offset:0
buffer_load_dwordx4 a[4:7], v39, s[48:51], 0 offen offset:0
buffer_load_dwordx4 a[16:19], v38, s[48:51], 0 offen offset:64
buffer_load_dwordx4 a[20:23], v39, s[48:51], 0 offen offset:64
buffer_load_dwordx4 a[32:35], v38, s[48:51], 0 offen offset:128
buffer_load_dwordx4 a[36:39], v39, s[48:51], 0 offen offset:128
buffer_load_dwordx4 a[48:51], v38, s[48:51], 0 offen offset:192
buffer_load_dwordx4 a[52:55], v39, s[48:51], 0 offen offset:192
buffer_load_dwordx4 a[64:67], v38, s[48:51], 0 offen offset:256
buffer_load_dwordx4 a[68:71], v39, s[48:51], 0 offen offset:256
buffer_load_dwordx4 a[80:83], v38, s[48:51], 0 offen offset:320
buffer_load_dwordx4 a[84:87], v39, s[48:51], 0 offen offset:320
s_lshl_b32 s74, s13, 2
s_lshl_b32 s75, s74, 7
s_add_u32 s16, s92, s75
s_addc_u32 s17, s93, 0
s_sub_u32 s16, s16, 0x1000
s_subb_u32 s17, s17, 0
s_sub_u32 s18, 5312512, s75
s_mov_b32 s19, 0x20000
s_add_u32 s20, s94, s75
s_addc_u32 s21, s95, 0
s_sub_u32 s20, s20, 0x1000
s_subb_u32 s21, s21, 0
s_sub_u32 s22, 5312512, s75
s_mov_b32 s23, 0x20000
s_mul_i32 s75, s74, 73728
s_add_u32 s24, s96, s75
s_addc_u32 s25, s97, 0
s_sub_u32 s24, s24, 0x1000
s_subb_u32 s25, s25, 0
s_sub_u32 s26, 5312512, s75
s_mov_b32 s27, 0x20000
s_mul_i32 s75, s13, 73728
s_add_u32 s28, s84, s75
s_addc_u32 s29, s85, 0
s_sub_u32 s28, s28, 0x1000
s_subb_u32 s29, s29, 0
s_sub_u32 s30, 667648, s75
s_mov_b32 s31, 0x20000
s_add_u32 s36, s86, s75
s_addc_u32 s37, s87, 0
s_sub_u32 s36, s36, 0x1000
s_subb_u32 s37, s37, 0
s_sub_u32 s38, 667648, s75
s_mov_b32 s39, 0x20000
s_lshl_b32 s75, s13, 7
s_add_u32 s40, s88, s75
s_addc_u32 s41, s89, 0
s_sub_u32 s40, s40, 0x1000
s_subb_u32 s41, s41, 0
s_sub_u32 s42, 667648, s75
s_mov_b32 s43, 0x20000
s_add_u32 s44, s90, s75
s_addc_u32 s45, s91, 0
s_sub_u32 s44, s44, 0x1000
s_subb_u32 s45, s45, 0
s_sub_u32 s46, 667648, s75
s_mov_b32 s47, 0x20000
v_lshrrev_b32 v40, 3, v0
v_and_b32 v41, 7, v0
v_and_b32 v42, 1, v0
v_lshlrev_b32 v42, 4, v42
v_lshrrev_b32 v43, 1, v41
s_lshl_b32 s67, s11, 3
v_add_u32 v44, s67, v40
s_lshl_b32 s68, s64, 1
v_and_b32 v45, 1, v33
v_or_b32 v46, s68, v45
v_xor_b32 v46, v46, v43
v_lshl_add_u32 v46, v46, 5, v42
v_mul_u32_u24 v1, 9216, v44
v_add_u32 v1, v1, v46
v_xor_b32 v46, v33, v43
v_lshl_add_u32 v46, v46, 5, v42
v_mul_u32_u24 v18, 1152, v44
v_add_u32 v18, v18, v46
v_add_u32 v44, s67, v44
v_xor_b32 v46, v33, v41
v_mul_u32_u24 v14, 1152, v44
v_lshl_add_u32 v14, v46, 4, v14
v_xor_b32 v15, 64, v14
v_add_u32 v15, 9216, v15
s_lshl_b32 s68, s11, 12
s_add_u32 s69, s68, 0x4000
s_add_u32 s70, s68, 0x8000
s_add_u32 s71, s68, 0xc000
v_lshrrev_b32 v46, 1, v34
v_lshl_or_b32 v46, v45, 1, v46
s_lshl_b32 s67, s65, 1
v_xor_b32 v46, s67, v46
v_lshlrev_b32 v46, 5, v46
v_lshl_add_u32 v46, v35, 3, v46
v_lshl_add_u32 v46, v34, 7, v46
v_lshl_add_u32 v2, v33, 12, v46
v_xor_b32 v3, 32, v2
v_lshl_add_u32 v19, v45, 9, v46
v_lshrrev_b32 v47, 1, v33
v_lshl_add_u32 v19, v47, 12, v19
v_xor_b32 v20, 32, v19
v_lshrrev_b32 v46, 1, v32
v_xor_b32 v46, v46, v33
v_lshlrev_b32 v46, 4, v46
v_and_b32 v47, 7, v32
v_lshl_add_u32 v46, v47, 7, v46
v_lshrrev_b32 v47, 3, v32
v_lshl_add_u32 v46, v47, 11, v46
s_lshl_b32 s67, s65, 13
v_add_u32 v16, s67, v46
v_xor_b32 v17, 64, v16
s_lshl_b32 s67, s64, 13
s_add_u32 s67, s67, 0x10000
v_lshl_add_u32 v4, v0, 4, s67
s_lshl_b32 s67, s65, 12
v_add_u32 v5, s67, v4
s_lshl_b32 s67, s64, 13
s_add_u32 s67, s67, 0x10000
v_and_b32 v46, 2, v33
v_lshl_or_b32 v46, v33, 2, v46
v_xor_b32 v46, v46, v0
v_lshl_add_u32 v157, v46, 4, s67
v_and_b32 v46, 2, v35
v_lshl_or_b32 v46, v35, 2, v46
v_lshrrev_b32 v47, 2, v0
v_xor_b32 v46, v46, v47
v_lshl_add_u32 v46, v35, 4, v46
s_lshl_b32 s72, s65, 11
s_add_u32 s67, s67, s72
v_lshl_add_u32 v156, v46, 4, s67
s_lshl_b32 s67, s14, 4
s_add_u32 s67, s67, s13
s_lshl_b32 s67, s67, 2
s_add_u32 s67, s67, s11
s_mul_i32 s67, s67, 36864
v_lshl_add_u32 v21, v0, 4, s67
s_add_u32 s52, s98, s67
s_addc_u32 s53, s99, 0x800000
s_mov_b32 s54, 64
s_mov_b32 s55, 0x20000
s_add_u32 s56, s98, 42991616
s_addc_u32 s57, s99, 0
s_mov_b32 s58, 262144
s_mov_b32 s59, 0x20000
s_lshl_b32 s67, s10, 10
s_add_u32 s67, s67, 41943040
s_add_u32 s60, s98, s67
s_addc_u32 s61, s99, 0
s_mov_b32 s62, 1024
s_mov_b32 s63, 0x20000
s_lshl_b32 s67, s14, 7
s_add_u32 s67, s67, 196608
s_load_dword s100, s[56:57], s67
s_lshl_b32 s67, s13, 7
s_add_u32 s67, s67, 200704
s_load_dword s101, s[56:57], s67
v_mov_b32 v96, 0x3f803f80
v_mov_b32 v97, 0x3f803f80
v_mov_b32 v98, 0x3f803f80
v_mov_b32 v99, 0x3f803f80
v_lshlrev_b32 v46, v42, v96
v_and_b32 v47, 0xffff, v96
v_cmp_eq_u32 vcc, 0, v42
s_nop 1
v_cndmask_b32 v46, v46, v47, vcc
v_cmp_eq_u32 vcc, v33, v34
s_nop 1
v_cndmask_b32 v46, 0, v46, vcc
v_cmp_gt_u32 vcc, 2, v35
v_mov_b32 v100, 0
v_mov_b32 v101, 0
v_cndmask_b32 v102, 0, v46, vcc
v_cndmask_b32 v103, v46, 0, vcc
v_mov_b32 v104, 0
v_mov_b32 v105, 0
v_cmp_eq_u32 s[72:73], 1, v35
v_cmp_eq_u32 s[74:75], 2, v35
v_cmp_eq_u32 s[76:77], 3, v35
s_lshl_b32 s67, s13, 6
s_lshl_b32 s84, s65, 5
s_add_u32 s67, s67, s84
v_add_lshl_u32 v48, v32, s67, 1
global_load_ushort v176, v48, s[78:79]
global_load_ushort v177, v48, s[78:79] offset:32
global_load_ushort v178, v48, s[80:81]
global_load_ushort v179, v48, s[80:81] offset:32
global_load_ushort v180, v48, s[82:83]
global_load_ushort v181, v48, s[82:83] offset:32
s_lshl_b32 s67, s65, 8
s_add_u32 s67, s67, 148480
v_lshl_add_u32 v30, v32, 4, s67
s_lshl_b32 s67, s64, 2
v_add_u32 v49, s67, v33
v_lshl_add_u32 v31, v49, 9, v30
v_lshl_add_u32 v49, v49, 1, s14
v_cmp_le_u32 s[84:85], 16, v49
v_cmp_le_u32 s[86:87], 15, v49
s_lshl_b32 s67, s11, 10
s_add_u32 s67, s67, 148480
v_lshl_add_u32 v49, v0, 4, s67
v_mov_b32 v106, 0
v_mov_b32 v107, 0
ds_write_b128 v49, v[104:107]
s_mov_b32 m0, 161792
s_lshl_b32 s90, s14, 2
s_add_u32 s90, s90, s11
s_mov_b32 s7, 0x20000
s_add_u32 s91, s90, 72
s_mul_i32 s92, s91, 3641
s_lshr_b32 s92, s92, 15
s_mul_i32 s93, s92, 9
s_sub_u32 s93, s91, s93
s_bitcmp1_b32 s92, 0
s_cselect_b32 s4, s20, s16
s_cselect_b32 s5, s21, s17
s_cselect_b32 s32, s44, s40
s_cselect_b32 s33, s45, s41
s_lshr_b32 s92, s92, 1
s_lshl_b32 s92, s92, 7
s_mul_i32 s34, s93, 589824
s_add_u32 s92, s92, s34
s_or_b32 s5, s5, 603979776
s_or_b32 s33, s33, 75497472
s_mul_i32 s34, s93, 73728
s_sub_u32 s93, s91, 72
s_lshl_b32 s93, s93, 13
s_or_b32 s35, s25, 0x800000
s_cmp_ge_u32 s91, 72
s_cselect_b32 s4, s24, s4
s_cselect_b32 s5, s35, s5
s_cselect_b32 s92, s93, s92
s_cmp_ge_u32 s91, 108
s_cselect_b32 s4, s32, s4
s_cselect_b32 s5, s33, s5
s_cselect_b32 s92, s34, s92
s_add_u32 s92, s92, 0x1000
s_cmp_ge_u32 s91, 126
s_cselect_b32 s6, 0, 64
s_cselect_b32 s92, 0, s92
buffer_load_dwordx4 a[96:99], v38, s[48:51], 0 offen offset:384
buffer_load_dwordx4 a[100:103], v39, s[48:51], 0 offen offset:384
s_waitcnt vmcnt(19)
v_mfma_f32_4x4x4_16b_bf16 v[108:111], v[96:97], a[0:1], 0
v_mfma_f32_4x4x4_16b_bf16 v[116:119], a[0:1], a[0:1], 0
v_mfma_f32_4x4x4_16b_bf16 v[108:111], v[96:97], a[2:3], v[108:111]
v_mfma_f32_4x4x4_16b_bf16 v[116:119], a[2:3], a[2:3], v[116:119]
s_waitcnt vmcnt(18)
v_mfma_f32_4x4x4_16b_bf16 v[112:115], v[96:97], a[4:5], 0
v_mfma_f32_4x4x4_16b_bf16 v[120:123], a[4:5], a[4:5], 0
v_mfma_f32_4x4x4_16b_bf16 v[112:115], v[96:97], a[6:7], v[112:115]
v_mfma_f32_4x4x4_16b_bf16 v[120:123], a[6:7], a[6:7], v[120:123]
buffer_load_dwordx4 a[112:115], v38, s[48:51], 0 offen offset:448
buffer_load_dwordx4 a[116:119], v39, s[48:51], 0 offen offset:448
s_waitcnt vmcnt(19)
v_mfma_f32_4x4x4_16b_bf16 v[108:111], v[96:97], a[16:17], v[108:111]
v_mfma_f32_4x4x4_16b_bf16 v[116:119], a[16:17], a[16:17], v[116:119]
v_mfma_f32_4x4x4_16b_bf16 v[108:111], v[96:97], a[18:19], v[108:111]
v_mfma_f32_4x4x4_16b_bf16 v[116:119], a[18:19], a[18:19], v[116:119]
s_waitcnt vmcnt(18)
v_mfma_f32_4x4x4_16b_bf16 v[112:115], v[96:97], a[20:21], v[112:115]
v_mfma_f32_4x4x4_16b_bf16 v[120:123], a[20:21], a[20:21], v[120:123]
v_mfma_f32_4x4x4_16b_bf16 v[112:115], v[96:97], a[22:23], v[112:115]
v_mfma_f32_4x4x4_16b_bf16 v[120:123], a[22:23], a[22:23], v[120:123]
buffer_load_dwordx4 a[128:131], v38, s[48:51], 0 offen offset:512
buffer_load_dwordx4 a[132:135], v39, s[48:51], 0 offen offset:512
s_waitcnt vmcnt(19)
v_mfma_f32_4x4x4_16b_bf16 v[108:111], v[96:97], a[32:33], v[108:111]
v_mfma_f32_4x4x4_16b_bf16 v[116:119], a[32:33], a[32:33], v[116:119]
v_mfma_f32_4x4x4_16b_bf16 v[108:111], v[96:97], a[34:35], v[108:111]
v_mfma_f32_4x4x4_16b_bf16 v[116:119], a[34:35], a[34:35], v[116:119]
s_waitcnt vmcnt(18)
v_mfma_f32_4x4x4_16b_bf16 v[112:115], v[96:97], a[36:37], v[112:115]
v_mfma_f32_4x4x4_16b_bf16 v[120:123], a[36:37], a[36:37], v[120:123]
v_mfma_f32_4x4x4_16b_bf16 v[112:115], v[96:97], a[38:39], v[112:115]
v_mfma_f32_4x4x4_16b_bf16 v[120:123], a[38:39], a[38:39], v[120:123]
buffer_load_dwordx4 a[144:147], v38, s[48:51], 0 offen offset:576
buffer_load_dwordx4 a[148:151], v39, s[48:51], 0 offen offset:576
s_waitcnt vmcnt(19)
v_mfma_f32_4x4x4_16b_bf16 v[108:111], v[96:97], a[48:49], v[108:111]
v_mfma_f32_4x4x4_16b_bf16 v[116:119], a[48:49], a[48:49], v[116:119]
v_mfma_f32_4x4x4_16b_bf16 v[108:111], v[96:97], a[50:51], v[108:111]
v_mfma_f32_4x4x4_16b_bf16 v[116:119], a[50:51], a[50:51], v[116:119]
s_waitcnt vmcnt(18)
v_mfma_f32_4x4x4_16b_bf16 v[112:115], v[96:97], a[52:53], v[112:115]
v_mfma_f32_4x4x4_16b_bf16 v[120:123], a[52:53], a[52:53], v[120:123]
v_mfma_f32_4x4x4_16b_bf16 v[112:115], v[96:97], a[54:55], v[112:115]
v_mfma_f32_4x4x4_16b_bf16 v[120:123], a[54:55], a[54:55], v[120:123]
buffer_load_dwordx4 a[160:163], v38, s[48:51], 0 offen offset:640
buffer_load_dwordx4 a[164:167], v39, s[48:51], 0 offen offset:640
s_waitcnt vmcnt(19)
v_mfma_f32_4x4x4_16b_bf16 v[108:111], v[96:97], a[64:65], v[108:111]
v_mfma_f32_4x4x4_16b_bf16 v[116:119], a[64:65], a[64:65], v[116:119]
v_mfma_f32_4x4x4_16b_bf16 v[108:111], v[96:97], a[66:67], v[108:111]
v_mfma_f32_4x4x4_16b_bf16 v[116:119], a[66:67], a[66:67], v[116:119]
s_waitcnt vmcnt(18)
v_mfma_f32_4x4x4_16b_bf16 v[112:115], v[96:97], a[68:69], v[112:115]
v_mfma_f32_4x4x4_16b_bf16 v[120:123], a[68:69], a[68:69], v[120:123]
v_mfma_f32_4x4x4_16b_bf16 v[112:115], v[96:97], a[70:71], v[112:115]
v_mfma_f32_4x4x4_16b_bf16 v[120:123], a[70:71], a[70:71], v[120:123]
buffer_load_dwordx4 a[176:179], v38, s[48:51], 0 offen offset:704
buffer_load_dwordx4 a[180:183], v39, s[48:51], 0 offen offset:704
s_waitcnt vmcnt(19)
v_mfma_f32_4x4x4_16b_bf16 v[108:111], v[96:97], a[80:81], v[108:111]
v_mfma_f32_4x4x4_16b_bf16 v[116:119], a[80:81], a[80:81], v[116:119]
v_mfma_f32_4x4x4_16b_bf16 v[108:111], v[96:97], a[82:83], v[108:111]
v_mfma_f32_4x4x4_16b_bf16 v[116:119], a[82:83], a[82:83], v[116:119]
s_waitcnt vmcnt(18)
v_mfma_f32_4x4x4_16b_bf16 v[112:115], v[96:97], a[84:85], v[112:115]
v_mfma_f32_4x4x4_16b_bf16 v[120:123], a[84:85], a[84:85], v[120:123]
v_mfma_f32_4x4x4_16b_bf16 v[112:115], v[96:97], a[86:87], v[112:115]
v_mfma_f32_4x4x4_16b_bf16 v[120:123], a[86:87], a[86:87], v[120:123]
buffer_load_dwordx4 a[192:195], v38, s[48:51], 0 offen offset:768
buffer_load_dwordx4 a[196:199], v39, s[48:51], 0 offen offset:768
s_waitcnt vmcnt(13)
v_mfma_f32_4x4x4_16b_bf16 v[108:111], v[96:97], a[96:97], v[108:111]
v_mfma_f32_4x4x4_16b_bf16 v[116:119], a[96:97], a[96:97], v[116:119]
v_mfma_f32_4x4x4_16b_bf16 v[108:111], v[96:97], a[98:99], v[108:111]
v_mfma_f32_4x4x4_16b_bf16 v[116:119], a[98:99], a[98:99], v[116:119]
s_waitcnt vmcnt(12)
v_mfma_f32_4x4x4_16b_bf16 v[112:115], v[96:97], a[100:101], v[112:115]
v_mfma_f32_4x4x4_16b_bf16 v[120:123], a[100:101], a[100:101], v[120:123]
v_mfma_f32_4x4x4_16b_bf16 v[112:115], v[96:97], a[102:103], v[112:115]
v_mfma_f32_4x4x4_16b_bf16 v[120:123], a[102:103], a[102:103], v[120:123]
buffer_load_dwordx4 a[208:211], v38, s[48:51], 0 offen offset:832
buffer_load_dwordx4 a[212:215], v39, s[48:51], 0 offen offset:832
s_waitcnt vmcnt(13)
v_mfma_f32_4x4x4_16b_bf16 v[108:111], v[96:97], a[112:113], v[108:111]
v_mfma_f32_4x4x4_16b_bf16 v[116:119], a[112:113], a[112:113], v[116:119]
v_mfma_f32_4x4x4_16b_bf16 v[108:111], v[96:97], a[114:115], v[108:111]
v_mfma_f32_4x4x4_16b_bf16 v[116:119], a[114:115], a[114:115], v[116:119]
s_waitcnt vmcnt(12)
v_mfma_f32_4x4x4_16b_bf16 v[112:115], v[96:97], a[116:117], v[112:115]
v_mfma_f32_4x4x4_16b_bf16 v[120:123], a[116:117], a[116:117], v[120:123]
v_mfma_f32_4x4x4_16b_bf16 v[112:115], v[96:97], a[118:119], v[112:115]
v_mfma_f32_4x4x4_16b_bf16 v[120:123], a[118:119], a[118:119], v[120:123]
buffer_load_dwordx4 a[224:227], v38, s[48:51], 0 offen offset:896
buffer_load_dwordx4 a[228:231], v39, s[48:51], 0 offen offset:896
s_waitcnt vmcnt(13)
v_mfma_f32_4x4x4_16b_bf16 v[108:111], v[96:97], a[128:129], v[108:111]
v_mfma_f32_4x4x4_16b_bf16 v[116:119], a[128:129], a[128:129], v[116:119]
v_mfma_f32_4x4x4_16b_bf16 v[108:111], v[96:97], a[130:131], v[108:111]
v_mfma_f32_4x4x4_16b_bf16 v[116:119], a[130:131], a[130:131], v[116:119]
s_waitcnt vmcnt(12)
v_mfma_f32_4x4x4_16b_bf16 v[112:115], v[96:97], a[132:133], v[112:115]
v_mfma_f32_4x4x4_16b_bf16 v[120:123], a[132:133], a[132:133], v[120:123]
v_mfma_f32_4x4x4_16b_bf16 v[112:115], v[96:97], a[134:135], v[112:115]
v_mfma_f32_4x4x4_16b_bf16 v[120:123], a[134:135], a[134:135], v[120:123]
buffer_load_dwordx4 a[240:243], v38, s[48:51], 0 offen offset:960
buffer_load_dwordx4 a[244:247], v39, s[48:51], 0 offen offset:960
s_waitcnt vmcnt(13)
v_mfma_f32_4x4x4_16b_bf16 v[108:111], v[96:97], a[144:145], v[108:111]
v_mfma_f32_4x4x4_16b_bf16 v[116:119], a[144:145], a[144:145], v[116:119]
v_mfma_f32_4x4x4_16b_bf16 v[108:111], v[96:97], a[146:147], v[108:111]
v_mfma_f32_4x4x4_16b_bf16 v[116:119], a[146:147], a[146:147], v[116:119]
s_waitcnt vmcnt(12)
v_mfma_f32_4x4x4_16b_bf16 v[112:115], v[96:97], a[148:149], v[112:115]
v_mfma_f32_4x4x4_16b_bf16 v[120:123], a[148:149], a[148:149], v[120:123]
v_mfma_f32_4x4x4_16b_bf16 v[112:115], v[96:97], a[150:151], v[112:115]
v_mfma_f32_4x4x4_16b_bf16 v[120:123], a[150:151], a[150:151], v[120:123]
buffer_load_dwordx4 v[224:227], v38, s[48:51], 0 offen offset:1024
buffer_load_dwordx4 v[228:231], v39, s[48:51], 0 offen offset:1024
s_waitcnt vmcnt(13)
v_mfma_f32_4x4x4_16b_bf16 v[108:111], v[96:97], a[160:161], v[108:111]
v_mfma_f32_4x4x4_16b_bf16 v[116:119], a[160:161], a[160:161], v[116:119]
v_mfma_f32_4x4x4_16b_bf16 v[108:111], v[96:97], a[162:163], v[108:111]
v_mfma_f32_4x4x4_16b_bf16 v[116:119], a[162:163], a[162:163], v[116:119]
s_waitcnt vmcnt(12)
v_mfma_f32_4x4x4_16b_bf16 v[112:115], v[96:97], a[164:165], v[112:115]
v_mfma_f32_4x4x4_16b_bf16 v[120:123], a[164:165], a[164:165], v[120:123]
v_mfma_f32_4x4x4_16b_bf16 v[112:115], v[96:97], a[166:167], v[112:115]
v_mfma_f32_4x4x4_16b_bf16 v[120:123], a[166:167], a[166:167], v[120:123]
buffer_load_dwordx4 v[240:243], v38, s[48:51], 0 offen offset:1088
buffer_load_dwordx4 v[244:247], v39, s[48:51], 0 offen offset:1088
s_waitcnt vmcnt(13)
v_mfma_f32_4x4x4_16b_bf16 v[108:111], v[96:97], a[176:177], v[108:111]
v_mfma_f32_4x4x4_16b_bf16 v[116:119], a[176:177], a[176:177], v[116:119]
v_mfma_f32_4x4x4_16b_bf16 v[108:111], v[96:97], a[178:179], v[108:111]
v_mfma_f32_4x4x4_16b_bf16 v[116:119], a[178:179], a[178:179], v[116:119]
s_waitcnt vmcnt(12)
v_mfma_f32_4x4x4_16b_bf16 v[112:115], v[96:97], a[180:181], v[112:115]
v_mfma_f32_4x4x4_16b_bf16 v[120:123], a[180:181], a[180:181], v[120:123]
v_mfma_f32_4x4x4_16b_bf16 v[112:115], v[96:97], a[182:183], v[112:115]
v_mfma_f32_4x4x4_16b_bf16 v[120:123], a[182:183], a[182:183], v[120:123]
s_mov_b32 m0, s68
s_mov_b32 s15, 4096
buffer_load_dwordx4 v14, s[28:31], s15 offen offset:0 lds
s_mov_b32 s15, 3072
buffer_load_dwordx4 v14, s[36:39], s15 offen offset:1024 lds
s_mov_b32 s15, 2048
buffer_load_dwordx4 v15, s[28:31], s15 offen offset:2048 lds
s_mov_b32 s15, 1024
buffer_load_dwordx4 v15, s[36:39], s15 offen offset:3072 lds
s_mov_b32 m0, s69
s_mov_b32 s15, 4224
buffer_load_dwordx4 v14, s[28:31], s15 offen offset:0 lds
s_mov_b32 s15, 3200
buffer_load_dwordx4 v14, s[36:39], s15 offen offset:1024 lds
s_mov_b32 s15, 2176
buffer_load_dwordx4 v15, s[28:31], s15 offen offset:2048 lds
s_mov_b32 s15, 1152
buffer_load_dwordx4 v15, s[36:39], s15 offen offset:3072 lds
s_mov_b32 m0, s70
s_mov_b32 s15, 4352
buffer_load_dwordx4 v14, s[28:31], s15 offen offset:0 lds
s_mov_b32 s15, 3328
buffer_load_dwordx4 v14, s[36:39], s15 offen offset:1024 lds
s_mov_b32 s15, 2304
buffer_load_dwordx4 v15, s[28:31], s15 offen offset:2048 lds
s_mov_b32 s15, 1280
buffer_load_dwordx4 v15, s[36:39], s15 offen offset:3072 lds
s_mov_b32 m0, 161792
s_waitcnt vmcnt(23)
v_mfma_f32_4x4x4_16b_bf16 v[108:111], v[96:97], a[192:193], v[108:111]
v_mfma_f32_4x4x4_16b_bf16 v[116:119], a[192:193], a[192:193], v[116:119]
v_mfma_f32_4x4x4_16b_bf16 v[108:111], v[96:97], a[194:195], v[108:111]
v_mfma_f32_4x4x4_16b_bf16 v[116:119], a[194:195], a[194:195], v[116:119]
s_waitcnt vmcnt(22)
v_mfma_f32_4x4x4_16b_bf16 v[112:115], v[96:97], a[196:197], v[112:115]
v_mfma_f32_4x4x4_16b_bf16 v[120:123], a[196:197], a[196:197], v[120:123]
v_mfma_f32_4x4x4_16b_bf16 v[112:115], v[96:97], a[198:199], v[112:115]
v_mfma_f32_4x4x4_16b_bf16 v[120:123], a[198:199], a[198:199], v[120:123]
s_waitcnt vmcnt(21)
v_mfma_f32_4x4x4_16b_bf16 v[108:111], v[96:97], a[208:209], v[108:111]
v_mfma_f32_4x4x4_16b_bf16 v[116:119], a[208:209], a[208:209], v[116:119]
v_mfma_f32_4x4x4_16b_bf16 v[108:111], v[96:97], a[210:211], v[108:111]
v_mfma_f32_4x4x4_16b_bf16 v[116:119], a[210:211], a[210:211], v[116:119]
s_waitcnt vmcnt(20)
v_mfma_f32_4x4x4_16b_bf16 v[112:115], v[96:97], a[212:213], v[112:115]
v_mfma_f32_4x4x4_16b_bf16 v[120:123], a[212:213], a[212:213], v[120:123]
v_mfma_f32_4x4x4_16b_bf16 v[112:115], v[96:97], a[214:215], v[112:115]
v_mfma_f32_4x4x4_16b_bf16 v[120:123], a[214:215], a[214:215], v[120:123]
s_waitcnt vmcnt(19)
v_mfma_f32_4x4x4_16b_bf16 v[108:111], v[96:97], a[224:225], v[108:111]
v_mfma_f32_4x4x4_16b_bf16 v[116:119], a[224:225], a[224:225], v[116:119]
v_mfma_f32_4x4x4_16b_bf16 v[108:111], v[96:97], a[226:227], v[108:111]
v_mfma_f32_4x4x4_16b_bf16 v[116:119], a[226:227], a[226:227], v[116:119]
s_waitcnt vmcnt(18)
v_mfma_f32_4x4x4_16b_bf16 v[112:115], v[96:97], a[228:229], v[112:115]
v_mfma_f32_4x4x4_16b_bf16 v[120:123], a[228:229], a[228:229], v[120:123]
v_mfma_f32_4x4x4_16b_bf16 v[112:115], v[96:97], a[230:231], v[112:115]
v_mfma_f32_4x4x4_16b_bf16 v[120:123], a[230:231], a[230:231], v[120:123]
s_waitcnt vmcnt(17)
v_mfma_f32_4x4x4_16b_bf16 v[108:111], v[96:97], a[240:241], v[108:111]
v_mfma_f32_4x4x4_16b_bf16 v[116:119], a[240:241], a[240:241], v[116:119]
v_mfma_f32_4x4x4_16b_bf16 v[108:111], v[96:97], a[242:243], v[108:111]
v_mfma_f32_4x4x4_16b_bf16 v[116:119], a[242:243], a[242:243], v[116:119]
s_waitcnt vmcnt(16)
v_mfma_f32_4x4x4_16b_bf16 v[112:115], v[96:97], a[244:245], v[112:115]
v_mfma_f32_4x4x4_16b_bf16 v[120:123], a[244:245], a[244:245], v[120:123]
v_mfma_f32_4x4x4_16b_bf16 v[112:115], v[96:97], a[246:247], v[112:115]
v_mfma_f32_4x4x4_16b_bf16 v[120:123], a[246:247], a[246:247], v[120:123]
s_waitcnt vmcnt(15)
v_mfma_f32_4x4x4_16b_bf16 v[108:111], v[96:97], v[224:225], v[108:111]
v_mfma_f32_4x4x4_16b_bf16 v[116:119], v[224:225], v[224:225], v[116:119]
v_mfma_f32_4x4x4_16b_bf16 v[108:111], v[96:97], v[226:227], v[108:111]
v_mfma_f32_4x4x4_16b_bf16 v[116:119], v[226:227], v[226:227], v[116:119]
s_waitcnt vmcnt(14)
v_mfma_f32_4x4x4_16b_bf16 v[112:115], v[96:97], v[228:229], v[112:115]
v_mfma_f32_4x4x4_16b_bf16 v[120:123], v[228:229], v[228:229], v[120:123]
v_mfma_f32_4x4x4_16b_bf16 v[112:115], v[96:97], v[230:231], v[112:115]
v_mfma_f32_4x4x4_16b_bf16 v[120:123], v[230:231], v[230:231], v[120:123]
s_waitcnt vmcnt(13)
v_mfma_f32_4x4x4_16b_bf16 v[108:111], v[96:97], v[240:241], v[108:111]
v_mfma_f32_4x4x4_16b_bf16 v[116:119], v[240:241], v[240:241], v[116:119]
v_mfma_f32_4x4x4_16b_bf16 v[108:111], v[96:97], v[242:243], v[108:111]
v_mfma_f32_4x4x4_16b_bf16 v[116:119], v[242:243], v[242:243], v[116:119]
s_waitcnt vmcnt(12)
v_mfma_f32_4x4x4_16b_bf16 v[112:115], v[96:97], v[244:245], v[112:115]
v_mfma_f32_4x4x4_16b_bf16 v[120:123], v[244:245], v[244:245], v[120:123]
v_mfma_f32_4x4x4_16b_bf16 v[112:115], v[96:97], v[246:247], v[112:115]
v_mfma_f32_4x4x4_16b_bf16 v[120:123], v[246:247], v[246:247], v[120:123]
buffer_load_dword v0, s[4:7], s92 idxen  lds
s_nop 1
v_cndmask_b32 v116, v116, v117, s[72:73]
s_nop 3
v_cndmask_b32 v120, v120, v121, s[72:73]
v_cndmask_b32 v116, v116, v118, s[74:75]
v_cndmask_b32 v120, v120, v122, s[74:75]
v_cndmask_b32 v116, v116, v119, s[76:77]
v_cndmask_b32 v120, v120, v123, s[76:77]
v_add_f32_dpp v108, v108, v108 quad_perm:[1,0,3,2] row_mask:0xf bank_mask:0xf
v_add_f32_dpp v112, v112, v112 quad_perm:[1,0,3,2] row_mask:0xf bank_mask:0xf
v_add_f32_dpp v116, v116, v116 quad_perm:[1,0,3,2] row_mask:0xf bank_mask:0xf
v_add_f32_dpp v120, v120, v120 quad_perm:[1,0,3,2] row_mask:0xf bank_mask:0xf
v_add_f32_dpp v108, v108, v108 quad_perm:[2,3,0,1] row_mask:0xf bank_mask:0xf
v_add_f32_dpp v112, v112, v112 quad_perm:[2,3,0,1] row_mask:0xf bank_mask:0xf
v_add_f32_dpp v116, v116, v116 quad_perm:[2,3,0,1] row_mask:0xf bank_mask:0xf
v_add_f32_dpp v120, v120, v120 quad_perm:[2,3,0,1] row_mask:0xf bank_mask:0xf
v_mul_f32 v109, 0x3ae38e39, v108
v_mul_f32 v113, 0x3ae38e39, v112
VC 108,109,109
VC 112,113,113
v_and_b32 v50, 0xffff0000, v108
v_and_b32 v52, 0xffff0000, v112
VM 109,109,109
VM 113,113,113
v_lshlrev_b32 v54, 2, v36
v_lshlrev_b32 v55, 2, v36
v_add_u32 v54, 147456, v54
v_add_u32 v55, 147472, v55
v_lshl_add_u32 v56, v36, 3, 0
v_lshl_add_u32 v57, v36, 3, 32
v_mov_b32 v117, 0x3ae38e39
v_mov_b32 v121, 0x3ae38e39
v_fma_f32 v116, v116, v117, -v109
v_fma_f32 v120, v120, v121, -v113
v_max_f32 v116, 0, v116
v_max_f32 v120, 0, v120
VC 116,116,116
)"
R"(VC 120,120,120
v_and_b32 v116, 0xffff0000, v116
v_and_b32 v120, 0xffff0000, v120
v_add_f32 v116, 0x3727c5ac, v116
v_add_f32 v120, 0x3727c5ac, v120
VC 116,116,116
VC 120,120,120
v_and_b32 v116, 0xffff0000, v116
v_and_b32 v120, 0xffff0000, v120
v_sqrt_f32 v116, v116
v_sqrt_f32 v120, v120
v_xor_b32 v108, 0x80000000, v50
v_xor_b32 v112, 0x80000000, v52
VC 116,116,116
VC 120,120,120
v_and_b32 v51, 0xffff0000, v116
v_and_b32 v53, 0xffff0000, v120
v_rcp_f32 v116, v51
v_rcp_f32 v120, v53
ds_write_b32 v54, v51
ds_write_b32 v55, v53
v_mov_b32 v109, v108
v_mov_b32 v113, v112
v_mov_b32 v110, v108
v_mov_b32 v114, v112
v_mov_b32 v111, v108
v_mov_b32 v115, v112
ds_write_b32 v54, v116 offset:512
ds_write_b32 v55, v120 offset:512
buffer_store_dwordx2 v[50:51], v56, s[60:63], 0 offen sc1
buffer_store_dwordx2 v[52:53], v57, s[60:63], 0 offen sc1
s_waitcnt lgkmcnt(0)
s_add_u32 s100, s100, 1
s_add_u32 s101, s101, 1
v_mfma_f32_16x16x32_bf16 v[124:127], v[102:105], a[0:3], v[108:111]
v_mfma_f32_16x16x32_bf16 v[128:131], v[100:103], a[0:3], v[108:111]
v_mfma_f32_16x16x32_bf16 v[132:135], v[102:105], a[4:7], v[112:115]
v_mfma_f32_16x16x32_bf16 v[136:139], v[100:103], a[4:7], v[112:115]
s_nop 4
VC 140,124,125
VC 141,126,127
VC 142,128,129
VC 143,130,131
ds_write_b128 v156, v[140:143] offset:0
v_mfma_f32_16x16x32_bf16 v[124:127], v[102:105], a[16:19], v[108:111]
v_mfma_f32_16x16x32_bf16 v[128:131], v[100:103], a[16:19], v[108:111]
VC 144,132,133
VC 145,134,135
VC 146,136,137
VC 147,138,139
ds_write_b128 v156, v[144:147] offset:1024
v_mfma_f32_16x16x32_bf16 v[132:135], v[102:105], a[20:23], v[112:115]
v_mfma_f32_16x16x32_bf16 v[136:139], v[100:103], a[20:23], v[112:115]
VC 148,124,125
VC 149,126,127
VC 150,128,129
VC 151,130,131
ds_write_b128 v156, v[148:151] offset:4096
v_mfma_f32_16x16x32_bf16 v[124:127], v[102:105], a[32:35], v[108:111]
v_mfma_f32_16x16x32_bf16 v[128:131], v[100:103], a[32:35], v[108:111]
VC 152,132,133
VC 153,134,135
VC 154,136,137
VC 155,138,139
ds_write_b128 v156, v[152:155] offset:5120
v_mfma_f32_16x16x32_bf16 v[132:135], v[102:105], a[36:39], v[112:115]
v_mfma_f32_16x16x32_bf16 v[136:139], v[100:103], a[36:39], v[112:115]
VC 140,124,125
VC 141,126,127
VC 142,128,129
VC 143,130,131
ds_write_b128 v156, v[140:143] offset:16384
v_mfma_f32_16x16x32_bf16 v[124:127], v[102:105], a[48:51], v[108:111]
v_mfma_f32_16x16x32_bf16 v[128:131], v[100:103], a[48:51], v[108:111]
VC 144,132,133
VC 145,134,135
VC 146,136,137
VC 147,138,139
ds_write_b128 v156, v[144:147] offset:17408
v_mfma_f32_16x16x32_bf16 v[132:135], v[102:105], a[52:55], v[112:115]
v_mfma_f32_16x16x32_bf16 v[136:139], v[100:103], a[52:55], v[112:115]
VC 148,124,125
VC 149,126,127
VC 150,128,129
VC 151,130,131
ds_write_b128 v156, v[148:151] offset:20480
s_nop 1
VC 152,132,133
VC 153,134,135
VC 154,136,137
VC 155,138,139
ds_write_b128 v156, v[152:155] offset:21504
s_waitcnt lgkmcnt(0)
s_waitcnt vmcnt(11)
s_barrier
ds_read_b128 a[0:3], v157 offset:0
ds_read_b128 a[4:7], v157 offset:1024
ds_read_b128 a[8:11], v157 offset:2048
ds_read_b128 a[12:15], v157 offset:3072
ds_read_b128 a[16:19], v157 offset:4096
ds_read_b128 a[20:23], v157 offset:5120
ds_read_b128 a[24:27], v157 offset:6144
ds_read_b128 a[28:31], v157 offset:7168
v_lshl_add_u32 v54, v34, 4, v35
s_lshl_b32 s67, s64, 6
v_add_u32 v54, s67, v54
v_lshlrev_b32 v54, 2, v54
v_add_u32 v54, 147456, v54
ds_read_b32 v10, v54 offset:0
ds_read_b32 v6, v54 offset:512
ds_read_b32 v11, v54 offset:16
ds_read_b32 v7, v54 offset:528
ds_read_b32 v12, v54 offset:32
ds_read_b32 v8, v54 offset:544
ds_read_b32 v13, v54 offset:48
ds_read_b32 v9, v54 offset:560
s_lshl_b32 s67, s64, 8
s_add_u32 s67, s67, 147968
v_lshl_add_u32 v55, v33, 6, s67
ds_read_b128 v[160:163], v55 offset:0
ds_read_b128 v[164:167], v55 offset:16
ds_read_b128 v[168:171], v55 offset:32
ds_read_b128 v[172:175], v55 offset:48
ds_read_b128 v[192:195], v16 offset:0
ds_read_b128 v[196:199], v16 offset:1024
s_waitcnt lgkmcnt(13)
VM 10,10,10
s_waitcnt lgkmcnt(12)
v_mul_f32 v6, 0xbfb8aa3b, v6
s_waitcnt lgkmcnt(11)
VM 11,11,11
s_waitcnt lgkmcnt(10)
v_mul_f32 v7, 0xbfb8aa3b, v7
s_waitcnt lgkmcnt(9)
VM 12,12,12
s_waitcnt lgkmcnt(8)
v_mul_f32 v8, 0xbfb8aa3b, v8
s_waitcnt lgkmcnt(7)
VM 13,13,13
s_waitcnt lgkmcnt(6)
v_mul_f32 v9, 0xbfb8aa3b, v9
s_waitcnt vmcnt(7)
s_barrier
s_waitcnt lgkmcnt(1)
Zav 32,0,192
s_mov_b32 m0, s71
ds_read_b128 a[32:35], v157 offset:16384
Zav 36,4,192
s_mov_b32 s15, 4480
buffer_load_dwordx4 v14, s[28:31], s15 offen offset:0 lds
Zav 40,8,192
ds_read_b128 v[200:203], v16 offset:4096
Zav 44,12,192
ds_read_b128 a[36:39], v157 offset:17408
v_mfma_f32_16x16x32_bf16 v[124:127], v[102:105], a[64:67], v[108:111]
s_mov_b32 s15, 3456
buffer_load_dwordx4 v14, s[36:39], s15 offen offset:1024 lds
v_mfma_f32_16x16x32_bf16 v[128:131], v[100:103], a[64:67], v[108:111]
ds_read_b128 v[204:207], v16 offset:5120
s_waitcnt lgkmcnt(4)
Zav 48,0,196
ds_read_b128 a[40:43], v157 offset:18432
Zav 52,4,196
s_mov_b32 s15, 2432
buffer_load_dwordx4 v15, s[28:31], s15 offen offset:2048 lds
Zav 56,8,196
VC 140,124,125
VC 141,126,127
Zav 60,12,196
VC 142,128,129
VC 143,130,131
v_mfma_f32_16x16x32_bf16 v[132:135], v[102:105], a[68:71], v[112:115]
s_mov_b32 s15, 1408
buffer_load_dwordx4 v15, s[36:39], s15 offen offset:3072 lds
v_mfma_f32_16x16x32_bf16 v[136:139], v[100:103], a[68:71], v[112:115]
ds_read_b128 v[208:211], v17 offset:0
s_waitcnt lgkmcnt(4)
Zav 64,0,200
ds_write_b128 v156, v[140:143] offset:32768
Zav 68,4,200
ds_read_b128 a[44:47], v157 offset:19456
Zav 72,8,200
VC 144,132,133
VC 145,134,135
Zav 76,12,200
VC 146,136,137
VC 147,138,139
s_waitcnt lgkmcnt(4)
Zav 80,0,204
ds_read_b128 v[192:195], v17 offset:1024
Zav 84,4,204
ds_read_b128 a[48:51], v157 offset:20480
Zav 88,8,204
ds_write_b128 v156, v[144:147] offset:33792
Zav 92,12,204
ds_read_b128 v[196:199], v17 offset:4096
v_mfma_f32_16x16x32_bf16 v[124:127], v[102:105], a[80:83], v[108:111]
ds_read_b128 a[52:55], v157 offset:21504
v_mfma_f32_16x16x32_bf16 v[128:131], v[100:103], a[80:83], v[108:111]
ds_read_b128 a[56:59], v157 offset:22528
s_waitcnt lgkmcnt(8)
Mav 32,16,208
ds_read_b128 v[200:203], v17 offset:5120
Mav 36,20,208
ds_read_b128 a[60:63], v157 offset:23552
Mav 40,24,208
VC 148,124,125
VC 149,126,127
Mav 44,28,208
VC 150,128,129
VC 151,130,131
v_mfma_f32_16x16x32_bf16 v[132:135], v[102:105], a[84:87], v[112:115]
ds_read_b128 v[204:207], v16 offset:16384
v_mfma_f32_16x16x32_bf16 v[136:139], v[100:103], a[84:87], v[112:115]
ds_write_b128 v156, v[148:151] offset:36864
s_waitcnt lgkmcnt(9)
Mav 48,16,192
Mav 52,20,192
Mav 56,24,192
s_nop 0
VC 152,132,133
VC 153,134,135
Mav 60,28,192
VC 154,136,137
VC 155,138,139
s_waitcnt lgkmcnt(6)
Mav 64,16,196
ds_read_b128 v[208:211], v16 offset:17408
Mav 68,20,196
ds_write_b128 v156, v[152:155] offset:37888
Mav 72,24,196
Mav 76,28,196
s_waitcnt lgkmcnt(5)
Mav 80,16,200
Mav 84,20,200
Mav 88,24,200
ds_read_b128 v[192:195], v16 offset:20480
Mav 92,28,200
s_waitcnt lgkmcnt(1)
s_waitcnt vmcnt(7)
s_barrier
Mav 32,32,204
s_mov_b32 m0, s68
v_lshlrev_b32 v180, 16, v180
Mav 36,36,204
s_mov_b32 s15, 4608
buffer_load_dwordx4 v14, s[28:31], s15 offen offset:0 lds
Mav 40,40,204
ds_read_b128 a[64:67], v157 offset:32768
v_lshlrev_b32 v181, 16, v181
Mav 44,44,204
v_lshlrev_b32 v176, 16, v176
ds_read_b128 a[68:71], v157 offset:33792
v_mfma_f32_16x16x32_bf16 v[124:127], v[102:105], a[96:99], v[108:111]
s_mov_b32 s15, 3584
buffer_load_dwordx4 v14, s[36:39], s15 offen offset:1024 lds
v_mfma_f32_16x16x32_bf16 v[128:131], v[100:103], a[96:99], v[108:111]
v_lshlrev_b32 v177, 16, v177
ds_read_b128 v[196:199], v16 offset:21504
Mav 48,32,208
v_lshlrev_b32 v178, 16, v178
ds_read_b128 a[72:75], v157 offset:34816
Mav 52,36,208
s_mov_b32 s15, 2560
buffer_load_dwordx4 v15, s[28:31], s15 offen offset:2048 lds
Mav 56,40,208
v_lshlrev_b32 v179, 16, v179
VC 140,124,125
Mav 60,44,208
VC 141,126,127
VC 142,128,129
v_mfma_f32_16x16x32_bf16 v[132:135], v[102:105], a[100:103], v[112:115]
s_mov_b32 s15, 1536
buffer_load_dwordx4 v15, s[36:39], s15 offen offset:3072 lds
v_mfma_f32_16x16x32_bf16 v[136:139], v[100:103], a[100:103], v[112:115]
VC 143,130,131
ds_read_b128 v[200:203], v17 offset:16384
s_waitcnt lgkmcnt(5)
Mav 64,32,192
v_mul_f32 v180, 0x3fb8aa3b, v180
ds_read_b128 a[76:79], v157 offset:35840
Mav 68,36,192
ds_write_b128 v156, v[140:143] offset:0
Mav 72,40,192
v_mul_f32 v181, 0x3fb8aa3b, v181
VC 144,132,133
Mav 76,44,192
VC 145,134,135
VC 146,136,137
s_waitcnt lgkmcnt(4)
Mav 80,32,196
VC 147,138,139
ds_read_b128 v[204:207], v17 offset:17408
Mav 84,36,196
ds_read_b128 a[80:83], v157 offset:36864
Mav 88,40,196
v_exp_f32 v180, v180
Mav 92,44,196
ds_write_b128 v156, v[144:147] offset:1024
v_mfma_f32_16x16x32_bf16 v[124:127], v[102:105], a[112:115], v[108:111]
ds_read_b128 v[208:211], v17 offset:20480
v_mfma_f32_16x16x32_bf16 v[128:131], v[100:103], a[112:115], v[108:111]
ds_read_b128 a[84:87], v157 offset:37888
s_waitcnt lgkmcnt(7)
Mav 32,48,200
v_exp_f32 v181, v181
Mav 36,52,200
VM 186,176,176
ds_read_b128 v[192:195], v17 offset:21504
Mav 40,56,200
ds_read_b128 a[88:91], v157 offset:38912
VC 148,124,125
Mav 44,60,200
VC 149,126,127
VC 150,128,129
v_mfma_f32_16x16x32_bf16 v[132:135], v[102:105], a[116:119], v[112:115]
VC 151,130,131
VM 187,177,177
v_mfma_f32_16x16x32_bf16 v[136:139], v[100:103], a[116:119], v[112:115]
VC 180,180,180
ds_read_b128 v[196:199], v16 offset:32768
s_waitcnt lgkmcnt(7)
Mav 48,48,204
ds_write_b128 v156, v[148:151] offset:4096
Mav 52,52,204
ds_read_b128 a[92:95], v157 offset:39936
VC 181,181,181
Mav 56,56,204
VC 152,132,133
VC 153,134,135
Mav 60,60,204
VC 154,136,137
VC 155,138,139
s_waitcnt lgkmcnt(6)
Mav 64,48,208
v_and_b32 v180, 0xffff0000, v180
ds_read_b128 v[200:203], v16 offset:33792
Mav 68,52,208
ds_write_b128 v156, v[152:155] offset:5120
Mav 72,56,208
v_and_b32 v181, 0xffff0000, v181
VF 186,178,178,186
Mav 76,60,208
VF 187,179,179,187
VM 182,180,176
s_waitcnt lgkmcnt(6)
Mav 80,48,192
VM 183,181,177
VM 184,180,178
Mav 84,52,192
VM 185,181,179
Mav 88,56,192
ds_read_b128 v[204:207], v16 offset:36864
Mav 92,60,192
v_rcp_f32 v186, v186
s_waitcnt lgkmcnt(1)
s_waitcnt vmcnt(4)
s_barrier
Mav 32,64,196
s_mov_b32 m0, s69
ds_read_b128 a[96:99], v157 offset:0
Mav 36,68,196
s_mov_b32 s15, 4736
buffer_load_dwordx4 v14, s[28:31], s15 offen offset:0 lds
Mav 40,72,196
ds_read_b128 a[100:103], v157 offset:1024
Mav 44,76,196
ds_read_b128 v[208:211], v16 offset:37888
v_mfma_f32_16x16x32_bf16 v[124:127], v[102:105], a[128:131], v[108:111]
s_mov_b32 s15, 3712
buffer_load_dwordx4 v14, s[36:39], s15 offen offset:1024 lds
v_mfma_f32_16x16x32_bf16 v[128:131], v[100:103], a[128:131], v[108:111]
ds_read_b128 a[104:107], v157 offset:2048
Mav 48,64,200
ds_read_b128 a[108:111], v157 offset:3072
Mav 52,68,200
s_mov_b32 s15, 2688
buffer_load_dwordx4 v15, s[28:31], s15 offen offset:2048 lds
Mav 56,72,200
VC 140,124,125
VC 141,126,127
Mav 60,76,200
VC 142,128,129
VC 143,130,131
v_mfma_f32_16x16x32_bf16 v[132:135], v[102:105], a[132:135], v[112:115]
s_mov_b32 s15, 1664
buffer_load_dwordx4 v15, s[36:39], s15 offen offset:3072 lds
v_mfma_f32_16x16x32_bf16 v[136:139], v[100:103], a[132:135], v[112:115]
ds_read_b128 v[192:195], v17 offset:32768
s_waitcnt lgkmcnt(6)
Mav 64,64,204
ds_write_b128 v156, v[140:143] offset:16384
Mav 68,68,204
v_rcp_f32 v187, v187
Mav 72,72,204
VC 144,132,133
VC 145,134,135
Mav 76,76,204
VC 146,136,137
VC 147,138,139
s_waitcnt lgkmcnt(4)
Mav 80,64,208
ds_read_b128 v[196:199], v17 offset:33792
v_mul_f32 v182, 0x3fb8aa3b, v182
Mav 84,68,208
ds_read_b128 a[112:115], v157 offset:4096
v_mul_f32 v183, 0x3fb8aa3b, v183
Mav 88,72,208
ds_write_b128 v156, v[144:147] offset:17408
Mav 92,76,208
ds_read_b128 v[200:203], v17 offset:36864
v_mul_f32 v184, 0x3e22f983, v184
v_mfma_f32_16x16x32_bf16 v[124:127], v[102:105], a[144:147], v[108:111]
ds_read_b128 a[116:119], v157 offset:5120
v_mul_f32 v185, 0x3e22f983, v185
v_mfma_f32_16x16x32_bf16 v[128:131], v[100:103], a[144:147], v[108:111]
ds_read_b128 a[120:123], v157 offset:6144
s_waitcnt lgkmcnt(7)
Mav 32,80,192
ds_read_b128 v[204:207], v17 offset:37888
Mav 36,84,192
v_exp_f32 v182, v182
Mav 40,88,192
VC 148,124,125
VC 149,126,127
Mav 44,92,192
VC 150,128,129
VC 151,130,131
v_mfma_f32_16x16x32_bf16 v[132:135], v[102:105], a[148:151], v[112:115]
ds_read_b128 v[208:211], v16 offset:49152
v_mfma_f32_16x16x32_bf16 v[136:139], v[100:103], a[148:151], v[112:115]
ds_write_b128 v156, v[148:151] offset:20480
s_waitcnt lgkmcnt(8)
Mav 48,80,196
ds_read_b128 a[124:127], v157 offset:7168
Mav 52,84,196
v_exp_f32 v183, v183
Mav 56,88,196
VC 152,132,133
VC 153,134,135
Mav 60,92,196
VC 154,136,137
VC 155,138,139
s_waitcnt lgkmcnt(6)
Mav 64,80,200
ds_read_b128 v[192:195], v16 offset:50176
v_rndne_f32 v188, v184
Mav 68,84,200
ds_write_b128 v156, v[152:155] offset:21504
Mav 72,88,200
v_rndne_f32 v189, v185
v_sub_f32 v184, v184, v188
Mav 76,92,200
v_sub_f32 v185, v185, v189
s_waitcnt lgkmcnt(5)
Mav 80,80,204
v_cos_f32 v188, v184
Mav 84,84,204
v_cos_f32 v189, v185
Mav 88,88,204
ds_read_b128 v[196:199], v16 offset:53248
Mav 92,92,204
v_sin_f32 v184, v184
s_waitcnt lgkmcnt(1)
s_waitcnt vmcnt(4)
s_barrier
Mav 32,96,208
s_mov_b32 m0, s70
ds_read_b128 a[128:131], v157 offset:16384
Mav 36,100,208
s_mov_b32 s15, 4864
buffer_load_dwordx4 v14, s[28:31], s15 offen offset:0 lds
Mav 40,104,208
ds_read_b128 a[132:135], v157 offset:17408
Mav 44,108,208
ds_read_b128 v[200:203], v16 offset:54272
v_mfma_f32_16x16x32_bf16 v[124:127], v[102:105], a[160:163], v[108:111]
s_mov_b32 s15, 3840
buffer_load_dwordx4 v14, s[36:39], s15 offen offset:1024 lds
v_mfma_f32_16x16x32_bf16 v[128:131], v[100:103], a[160:163], v[108:111]
ds_read_b128 a[136:139], v157 offset:18432
Mav 48,96,192
ds_read_b128 a[140:143], v157 offset:19456
Mav 52,100,192
s_mov_b32 s15, 2816
buffer_load_dwordx4 v15, s[28:31], s15 offen offset:2048 lds
Mav 56,104,192
VC 140,124,125
VC 141,126,127
Mav 60,108,192
VC 142,128,129
VC 143,130,131
v_mfma_f32_16x16x32_bf16 v[132:135], v[102:105], a[164:167], v[112:115]
s_mov_b32 s15, 1792
buffer_load_dwordx4 v15, s[36:39], s15 offen offset:3072 lds
v_mfma_f32_16x16x32_bf16 v[136:139], v[100:103], a[164:167], v[112:115]
ds_read_b128 v[204:207], v17 offset:49152
s_waitcnt lgkmcnt(6)
Mav 64,96,196
ds_write_b128 v156, v[140:143] offset:32768
Mav 68,100,196
ds_read_b128 a[144:147], v157 offset:20480
Mav 72,104,196
VC 144,132,133
VC 145,134,135
Mav 76,108,196
VC 146,136,137
VC 147,138,139
s_waitcnt lgkmcnt(5)
Mav 80,96,200
ds_read_b128 v[208:211], v17 offset:50176
Mav 84,100,200
ds_write_b128 v156, v[144:147] offset:33792
Mav 88,104,200
v_sin_f32 v185, v185
Mav 92,108,200
ds_read_b128 v[192:195], v17 offset:53248
v_add_f32 v186, v186, v186
v_mfma_f32_16x16x32_bf16 v[124:127], v[102:105], a[176:179], v[108:111]
ds_read_b128 a[148:151], v157 offset:21504
v_add_f32 v187, v187, v187
v_mfma_f32_16x16x32_bf16 v[128:131], v[100:103], a[176:179], v[108:111]
ds_read_b128 a[152:155], v157 offset:22528
VM 22,182,188
s_waitcnt lgkmcnt(7)
Mav 32,112,204
ds_read_b128 v[196:199], v17 offset:54272
VM 24,183,189
Mav 36,116,204
ds_read_b128 a[156:159], v157 offset:23552
v_mul_f32 v23, v182, -v184
Mav 40,120,204
VC 148,124,125
VC 149,126,127
Mav 44,124,204
VC 150,128,129
VC 151,130,131
v_mfma_f32_16x16x32_bf16 v[132:135], v[102:105], a[180:183], v[112:115]
ds_read_b128 v[200:203], v16 offset:0
v_mul_f32 v25, v183, -v185
v_mfma_f32_16x16x32_bf16 v[136:139], v[100:103], a[180:183], v[112:115]
ds_write_b128 v156, v[148:151] offset:36864
s_waitcnt lgkmcnt(8)
Mav 48,112,208
v_add_f32 v188, -1.0, v22
v_add_f32 v189, -1.0, v24
Mav 52,116,208
VM 182,188,176
VM 183,189,177
Mav 56,120,208
VC 152,132,133
VC 153,134,135
Mav 60,124,208
VC 154,136,137
VC 155,138,139
s_waitcnt lgkmcnt(6)
Mav 64,112,192
ds_read_b128 v[204:207], v16 offset:1024
VM 184,188,178
Mav 68,116,192
ds_write_b128 v156, v[152:155] offset:37888
Mav 72,120,192
VM 185,189,179
v_fma_f32 v182, -v23, v178, v182
Mav 76,124,192
v_fma_f32 v183, -v25, v179, v183
VF 184,23,176,184
s_waitcnt lgkmcnt(5)
Mav 80,112,196
VF 185,25,177,185
VM 212,182,186
Mav 84,116,196
VM 214,183,187
VM 213,184,186
Mav 88,120,196
ds_read_b128 v[208:211], v16 offset:4096
VM 215,185,187
Mav 92,124,196
VM 186,23,23
VM 187,25,25
s_waitcnt lgkmcnt(1)
s_waitcnt vmcnt(4)
s_barrier
Mav 32,128,200
s_mov_b32 m0, s71
ds_read_b128 a[160:163], v157 offset:32768
Mav 36,132,200
s_mov_b32 s15, 4992
buffer_load_dwordx4 v14, s[28:31], s15 offen offset:0 lds
Mav 40,136,200
ds_read_b128 a[164:167], v157 offset:33792
v_add_f32 v188, v22, v22
Mav 44,140,200
ds_read_b128 v[192:195], v16 offset:5120
v_add_f32 v189, v24, v24
v_mfma_f32_16x16x32_bf16 v[124:127], v[102:105], a[192:195], v[108:111]
s_mov_b32 s15, 3968
buffer_load_dwordx4 v14, s[36:39], s15 offen offset:1024 lds
v_mfma_f32_16x16x32_bf16 v[128:131], v[100:103], a[192:195], v[108:111]
ds_read_b128 a[168:171], v157 offset:34816
)"
R"(v_fma_f32 v182, v22, v22, -v186
Mav 48,128,204
ds_read_b128 a[172:175], v157 offset:35840
v_fma_f32 v183, v24, v24, -v187
Mav 52,132,204
s_mov_b32 s15, 2944
buffer_load_dwordx4 v15, s[28:31], s15 offen offset:2048 lds
Mav 56,136,204
VC 140,124,125
VC 141,126,127
Mav 60,140,204
VC 142,128,129
VC 143,130,131
v_mfma_f32_16x16x32_bf16 v[132:135], v[102:105], a[196:199], v[112:115]
s_mov_b32 s15, 1920
buffer_load_dwordx4 v15, s[36:39], s15 offen offset:3072 lds
v_mfma_f32_16x16x32_bf16 v[136:139], v[100:103], a[196:199], v[112:115]
ds_read_b128 v[196:199], v17 offset:0
VM 184,188,23
s_waitcnt lgkmcnt(6)
Mav 64,128,208
ds_write_b128 v156, v[140:143] offset:0
Mav 68,132,208
ds_read_b128 a[176:179], v157 offset:36864
VM 185,189,25
Mav 72,136,208
VC 144,132,133
VC 145,134,135
Mav 76,140,208
VC 146,136,137
VC 147,138,139
s_waitcnt lgkmcnt(5)
Mav 80,128,192
ds_read_b128 v[200:203], v17 offset:1024
VM 186,184,184
Mav 84,132,192
ds_write_b128 v156, v[144:147] offset:1024
Mav 88,136,192
ds_read_b128 v[204:207], v17 offset:4096
VM 187,185,185
Mav 92,140,192
ds_read_b128 a[180:183], v157 offset:37888
v_add_f32 v188, v182, v182
v_mfma_f32_16x16x32_bf16 v[124:127], v[102:105], a[208:211], v[108:111]
ds_read_b128 a[184:187], v157 offset:38912
v_add_f32 v189, v183, v183
v_mfma_f32_16x16x32_bf16 v[128:131], v[100:103], a[208:211], v[108:111]
ds_read_b128 a[188:191], v157 offset:39936
v_fma_f32 v182, v182, v182, -v186
s_waitcnt lgkmcnt(8)
Mav 32,144,196
ds_read_b128 v[208:211], v17 offset:5120
v_fma_f32 v183, v183, v183, -v187
Mav 36,148,196
VM 184,188,184
VM 185,189,185
Mav 40,152,196
VC 148,124,125
VC 149,126,127
Mav 44,156,196
VC 150,128,129
VC 151,130,131
v_mfma_f32_16x16x32_bf16 v[132:135], v[102:105], a[212:215], v[112:115]
ds_read_b128 v[192:195], v16 offset:16384
VM 186,184,184
v_mfma_f32_16x16x32_bf16 v[136:139], v[100:103], a[212:215], v[112:115]
ds_write_b128 v156, v[148:151] offset:4096
s_waitcnt lgkmcnt(8)
Mav 48,144,200
VM 187,185,185
v_add_f32 v188, v182, v182
Mav 52,148,200
v_add_f32 v189, v183, v183
v_fma_f32 v182, v182, v182, -v186
Mav 56,152,200
VC 152,132,133
VC 153,134,135
Mav 60,156,200
VC 154,136,137
VC 155,138,139
s_waitcnt lgkmcnt(6)
Mav 64,144,204
ds_read_b128 v[196:199], v16 offset:17408
v_fma_f32 v183, v183, v183, -v187
Mav 68,148,204
ds_write_b128 v156, v[152:155] offset:5120
Mav 72,152,204
VM 184,188,184
VM 185,189,185
Mav 76,156,204
VM 186,184,184
VM 187,185,185
s_waitcnt lgkmcnt(4)
Mav 80,144,208
v_add_f32 v188, v182, v182
v_add_f32 v189, v183, v183
Mav 84,148,208
v_fma_f32 v26, v182, v182, -v186
v_fma_f32 v28, v183, v183, -v187
Mav 88,152,208
ds_read_b128 v[200:203], v16 offset:20480
VM 27,188,184
Mav 92,156,208
VM 29,189,185
VM 186,27,27
s_waitcnt lgkmcnt(1)
s_waitcnt vmcnt(4)
s_barrier
Mav 32,160,192
s_mov_b32 m0, s68
ds_read_b128 a[192:195], v157 offset:0
Mav 36,164,192
s_mov_b32 s15, 5120
buffer_load_dwordx4 v14, s[28:31], s15 offen offset:0 lds
Mav 40,168,192
ds_read_b128 a[196:199], v157 offset:1024
VM 187,29,29
Mav 44,172,192
ds_read_b128 v[204:207], v16 offset:21504
v_add_f32 v188, v26, v26
v_mfma_f32_16x16x32_bf16 v[124:127], v[102:105], a[224:227], v[108:111]
s_mov_b32 s15, 4096
buffer_load_dwordx4 v14, s[36:39], s15 offen offset:1024 lds
v_mfma_f32_16x16x32_bf16 v[128:131], v[100:103], a[224:227], v[108:111]
ds_read_b128 a[200:203], v157 offset:2048
v_add_f32 v189, v28, v28
Mav 48,160,196
ds_read_b128 a[204:207], v157 offset:3072
v_fma_f32 v182, v26, v26, -v186
Mav 52,164,196
s_mov_b32 s15, 3072
buffer_load_dwordx4 v15, s[28:31], s15 offen offset:2048 lds
Mav 56,168,196
VC 140,124,125
VC 141,126,127
Mav 60,172,196
VC 142,128,129
VC 143,130,131
v_mfma_f32_16x16x32_bf16 v[132:135], v[102:105], a[228:231], v[112:115]
s_mov_b32 s15, 2048
buffer_load_dwordx4 v15, s[36:39], s15 offen offset:3072 lds
v_mfma_f32_16x16x32_bf16 v[136:139], v[100:103], a[228:231], v[112:115]
ds_read_b128 v[208:211], v17 offset:16384
v_fma_f32 v183, v28, v28, -v187
s_waitcnt lgkmcnt(6)
Mav 64,160,200
ds_write_b128 v156, v[140:143] offset:16384
Mav 68,164,200
ds_read_b128 a[208:211], v157 offset:4096
VM 184,188,27
Mav 72,168,200
VC 144,132,133
VC 145,134,135
Mav 76,172,200
VC 146,136,137
VC 147,138,139
s_waitcnt lgkmcnt(5)
Mav 80,160,204
ds_read_b128 v[192:195], v17 offset:17408
VM 185,189,29
Mav 84,164,204
ds_write_b128 v156, v[144:147] offset:17408
Mav 88,168,204
ds_read_b128 v[196:199], v17 offset:20480
VM 186,184,184
Mav 92,172,204
ds_read_b128 a[212:215], v157 offset:5120
VM 187,185,185
v_mfma_f32_16x16x32_bf16 v[124:127], v[102:105], a[240:243], v[108:111]
ds_read_b128 a[216:219], v157 offset:6144
v_add_f32 v188, v182, v182
v_mfma_f32_16x16x32_bf16 v[128:131], v[100:103], a[240:243], v[108:111]
ds_read_b128 a[220:223], v157 offset:7168
v_add_f32 v189, v183, v183
s_waitcnt lgkmcnt(8)
Mav 32,176,208
ds_read_b128 v[200:203], v17 offset:21504
v_fma_f32 v182, v182, v182, -v186
Mav 36,180,208
v_fma_f32 v183, v183, v183, -v187
VM 184,188,184
Mav 40,184,208
VC 148,124,125
VC 149,126,127
Mav 44,188,208
VC 150,128,129
VC 151,130,131
v_mfma_f32_16x16x32_bf16 v[132:135], v[102:105], a[244:247], v[112:115]
ds_read_b128 v[204:207], v16 offset:32768
VM 185,189,185
v_mfma_f32_16x16x32_bf16 v[136:139], v[100:103], a[244:247], v[112:115]
ds_write_b128 v156, v[148:151] offset:20480
s_waitcnt lgkmcnt(8)
Mav 48,176,192
VM 186,184,184
VM 187,185,185
Mav 52,180,192
v_add_f32 v188, v182, v182
v_add_f32 v189, v183, v183
Mav 56,184,192
VC 152,132,133
VC 153,134,135
Mav 60,188,192
VC 154,136,137
VC 155,138,139
s_waitcnt lgkmcnt(6)
Mav 64,176,196
ds_read_b128 v[208:211], v16 offset:33792
v_fma_f32 v216, v182, v182, -v186
Mav 68,180,196
ds_write_b128 v156, v[152:155] offset:21504
Mav 72,184,196
v_fma_f32 v218, v183, v183, -v187
VM 217,188,184
Mav 76,188,196
VM 219,189,185
VM 186,217,217
s_waitcnt lgkmcnt(4)
Mav 80,176,200
VM 187,219,219
v_add_f32 v188, v216, v216
Mav 84,180,200
v_add_f32 v189, v218, v218
v_fma_f32 v182, v216, v216, -v186
Mav 88,184,200
ds_read_b128 v[192:195], v16 offset:36864
v_fma_f32 v183, v218, v218, -v187
Mav 92,188,200
VM 184,188,217
VM 185,189,219
s_waitcnt lgkmcnt(1)
s_waitcnt vmcnt(4)
s_barrier
Mav 32,192,204
s_mov_b32 m0, s69
ds_read_b128 a[224:227], v157 offset:16384
Mav 36,196,204
s_mov_b32 s15, 4096
buffer_load_dwordx4 v1, s[16:19], s15 offen offset:0 lds
Mav 40,200,204
ds_read_b128 a[228:231], v157 offset:17408
v_and_b32 v176, 16, v0
Mav 44,204,204
ds_read_b128 v[196:199], v16 offset:37888
v_cmp_ne_u32 s[88:89], 0, v176
v_mfma_f32_16x16x32_bf16 v[124:127], v[102:105], v[224:227], v[108:111]
s_mov_b32 s15, 3072
buffer_load_dwordx4 v1, s[20:23], s15 offen offset:1024 lds
v_mfma_f32_16x16x32_bf16 v[128:131], v[100:103], v[224:227], v[108:111]
ds_read_b128 a[232:235], v157 offset:18432
v_cndmask_b32 v220, v182, v183, s[88:89]
Mav 48,192,208
ds_read_b128 a[236:239], v157 offset:19456
v_cndmask_b32 v221, v184, v185, s[88:89]
Mav 52,196,208
s_mov_b32 s15, 296960
buffer_load_dwordx4 v1, s[16:19], s15 offen offset:2048 lds
Mav 56,200,208
VC 140,124,125
VC 141,126,127
Mav 60,204,208
VC 142,128,129
VC 143,130,131
v_mfma_f32_16x16x32_bf16 v[132:135], v[102:105], v[228:231], v[112:115]
s_mov_b32 s15, 295936
buffer_load_dwordx4 v1, s[20:23], s15 offen offset:3072 lds
v_mfma_f32_16x16x32_bf16 v[136:139], v[100:103], v[228:231], v[112:115]
ds_read_b128 v[200:203], v17 offset:32768
v_cndmask_b32 v222, v26, v28, s[88:89]
s_waitcnt lgkmcnt(6)
Mav 64,192,192
ds_write_b128 v156, v[140:143] offset:32768
Mav 68,196,192
ds_read_b128 a[240:243], v157 offset:20480
v_cndmask_b32 v223, v27, v29, s[88:89]
Mav 72,200,192
VC 144,132,133
VC 145,134,135
Mav 76,204,192
VC 146,136,137
VC 147,138,139
s_waitcnt lgkmcnt(5)
Mav 80,192,196
ds_read_b128 v[204:207], v17 offset:33792
Mav 84,196,196
ds_write_b128 v156, v[144:147] offset:33792
Mav 88,200,196
ds_read_b128 v[208:211], v17 offset:36864
Mav 92,204,196
ds_read_b128 a[244:247], v157 offset:21504
v_mfma_f32_16x16x32_bf16 v[124:127], v[102:105], v[240:243], v[108:111]
ds_read_b128 a[248:251], v157 offset:22528
v_mfma_f32_16x16x32_bf16 v[128:131], v[100:103], v[240:243], v[108:111]
ds_read_b128 a[252:255], v157 offset:23552
s_waitcnt lgkmcnt(8)
Mav 32,208,200
ds_read_b128 v[192:195], v17 offset:37888
Mav 36,212,200
Mav 40,216,200
VC 148,124,125
VC 149,126,127
Mav 44,220,200
VC 150,128,129
VC 151,130,131
v_mfma_f32_16x16x32_bf16 v[132:135], v[102:105], v[244:247], v[112:115]
ds_read_b128 v[196:199], v16 offset:49152
v_mfma_f32_16x16x32_bf16 v[136:139], v[100:103], v[244:247], v[112:115]
ds_write_b128 v156, v[148:151] offset:36864
s_waitcnt lgkmcnt(8)
Mav 48,208,204
Mav 52,212,204
Mav 56,216,204
s_nop 0
VC 152,132,133
VC 153,134,135
Mav 60,220,204
VC 154,136,137
VC 155,138,139
s_waitcnt lgkmcnt(6)
Mav 64,208,208
ds_read_b128 v[200:203], v16 offset:50176
Mav 68,212,208
ds_write_b128 v156, v[152:155] offset:37888
Mav 72,216,208
ds_read_b128 v[204:207], v16 offset:53248
Mav 76,220,208
s_waitcnt lgkmcnt(5)
Mav 80,208,192
Mav 84,212,192
Mav 88,216,192
ds_read_b128 v[208:211], v16 offset:54272
Mav 92,220,192
s_waitcnt lgkmcnt(2)
s_waitcnt vmcnt(4)
s_barrier
Mav 32,224,196
s_mov_b32 m0, s70
ds_read_b128 v[224:227], v157 offset:32768
Mav 36,228,196
s_mov_b32 s15, 593920
buffer_load_dwordx4 v1, s[16:19], s15 offen offset:0 lds
Mav 40,232,196
ds_read_b128 v[228:231], v157 offset:33792
Mav 44,236,196
ds_read_b128 v[192:195], v17 offset:49152
Mav 48,224,200
s_mov_b32 s15, 592896
buffer_load_dwordx4 v1, s[20:23], s15 offen offset:1024 lds
Mav 52,228,200
ds_read_b128 v[232:235], v157 offset:34816
Mav 56,232,200
ds_read_b128 v[196:199], v17 offset:50176
Mav 60,236,200
s_mov_b32 s15, 886784
buffer_load_dwordx4 v1, s[16:19], s15 offen offset:2048 lds
s_waitcnt lgkmcnt(6)
Mav 64,224,204
ds_read_b128 v[236:239], v157 offset:35840
Mav 68,228,204
ds_read_b128 v[240:243], v157 offset:36864
Mav 72,232,204
s_mov_b32 s15, 885760
buffer_load_dwordx4 v1, s[20:23], s15 offen offset:3072 lds
Mav 76,236,204
ds_read_b128 v[200:203], v17 offset:53248
s_waitcnt lgkmcnt(8)
Mav 80,224,208
ds_read_b128 v[244:247], v157 offset:37888
Mav 84,228,208
ds_read_b128 v[248:251], v157 offset:38912
Mav 88,232,208
ds_read_b128 v[204:207], v17 offset:54272
Mav 92,236,208
ds_read_b128 v[252:255], v157 offset:39936
s_waitcnt lgkmcnt(9)
Mav 32,240,192
Mav 36,244,192
Mav 40,248,192
ds_read_b128 v[208:211], v16 offset:0
Mav 44,252,192
s_waitcnt lgkmcnt(8)
Mav 48,240,196
Mav 52,244,196
Mav 56,248,196
ds_read_b128 v[192:195], v16 offset:1024
Mav 60,252,196
s_waitcnt lgkmcnt(6)
Mav 64,240,200
Mav 68,244,200
Mav 72,248,200
ds_read_b128 v[196:199], v16 offset:4096
Mav 76,252,200
s_waitcnt lgkmcnt(4)
Mav 80,240,204
Mav 84,244,204
Mav 88,248,204
ds_read_b128 v[200:203], v16 offset:5120
Mav 92,252,204
s_waitcnt vmcnt(4)
s_barrier
s_waitcnt lgkmcnt(3)
Mvv 32,224,208
s_mov_b32 m0, s71
Mvv 36,228,208
s_mov_b32 s15, 1183744
buffer_load_dwordx4 v1, s[16:19], s15 offen offset:0 lds
Mvv 40,232,208
ds_read_b128 v[204:207], v17 offset:0
Mvv 44,236,208
s_waitcnt lgkmcnt(3)
Mvv 48,224,192
s_mov_b32 s15, 1182720
buffer_load_dwordx4 v1, s[20:23], s15 offen offset:1024 lds
Mvv 52,228,192
Mvv 56,232,192
ds_read_b128 v[208:211], v17 offset:1024
Mvv 60,236,192
s_mov_b32 s15, 1476608
buffer_load_dwordx4 v1, s[16:19], s15 offen offset:2048 lds
s_waitcnt lgkmcnt(3)
Mvv 64,224,196
Mvv 68,228,196
Mvv 72,232,196
s_mov_b32 s15, 1475584
buffer_load_dwordx4 v1, s[20:23], s15 offen offset:3072 lds
Mvv 76,236,196
ds_read_b128 v[192:195], v17 offset:4096
s_waitcnt lgkmcnt(3)
Mvv 80,224,200
Mvv 84,228,200
Mvv 88,232,200
ds_read_b128 v[196:199], v17 offset:5120
Mvv 92,236,200
s_waitcnt lgkmcnt(3)
Mvv 32,240,204
Mvv 36,244,204
Mvv 40,248,204
TR 200,2,16384
Mvv 44,252,204
TR 202,2,16896
s_waitcnt lgkmcnt(4)
Mvv 48,240,208
Mvv 52,244,208
Mvv 56,248,208
TR 204,2,17408
Mvv 60,252,208
TR 206,2,17920
s_waitcnt lgkmcnt(5)
Mvv 64,240,192
Mvv 68,244,192
Mvv 72,248,192
TR 208,3,16384
Mvv 76,252,192
TR 210,3,16896
s_waitcnt lgkmcnt(6)
Mvv 80,240,196
Mvv 84,244,196
Mvv 88,248,196
TR 192,3,17408
Mvv 92,252,196
TR 194,3,17920
s_waitcnt vmcnt(4)
s_barrier
s_waitcnt lgkmcnt(6)
Zva 96,200,0
s_mov_b32 m0, s68
Zva 112,200,4
s_mov_b32 s15, 1773568
buffer_load_dwordx4 v1, s[16:19], s15 offen offset:0 lds
Zva 128,200,8
VM 32,160,32
VM 64,160,64
Zva 144,200,12
VM 48,160,48
VM 80,160,80
s_waitcnt lgkmcnt(4)
Zva 100,204,0
s_mov_b32 s15, 1772544
buffer_load_dwordx4 v1, s[20:23], s15 offen offset:1024 lds
Zva 116,204,4
VM 190,212,32
VM 191,214,64
Zva 132,204,8
VM 32,213,32
VM 64,215,64
Zva 148,204,12
s_mov_b32 s15, 2066432
buffer_load_dwordx4 v1, s[16:19], s15 offen offset:2048 lds
s_waitcnt lgkmcnt(2)
Zva 104,208,0
VF 190,213,48,190
TR 196,2,18432
Zva 120,208,4
VF 191,215,80,191
v_fma_f32 v48, -v212, v48, v32
Zva 136,208,8
TR 198,2,18944
v_fma_f32 v80, -v214, v80, v64
Zva 152,208,12
s_mov_b32 s15, 2065408
buffer_load_dwordx4 v1, s[20:23], s15 offen offset:3072 lds
VC 160,190,48
s_waitcnt lgkmcnt(2)
Zva 108,192,0
VC 176,191,80
v_mov_b32 v32, v190
Zva 124,192,4
TR 200,2,19456
v_mov_b32 v64, v191
Zva 140,192,8
TR 202,2,19968
VM 33,161,33
Zva 156,192,12
VM 65,161,65
VM 49,161,49
s_waitcnt lgkmcnt(2)
Mva 96,196,16
VM 81,161,81
TR 204,3,18432
Mva 112,196,20
VM 190,212,33
VM 191,214,65
Mva 128,196,24
TR 206,3,18944
VM 33,213,33
Mva 144,196,28
VM 65,215,65
VF 190,213,49,190
s_waitcnt lgkmcnt(2)
Mva 100,200,16
VF 191,215,81,191
TR 208,3,19456
Mva 116,200,20
v_fma_f32 v49, -v212, v49, v33
TR 210,3,19968
Mva 132,200,24
v_fma_f32 v81, -v214, v81, v65
VC 161,190,49
Mva 148,200,28
VC 177,191,81
VF 33,22,32,190
s_waitcnt lgkmcnt(2)
Mva 104,204,16
TR 192,2,32768
VF 65,24,64,191
Mva 120,204,20
VF 49,22,48,49
TR 194,2,33280
Mva 136,204,24
VF 81,24,80,81
v_fma_f32 v33, -v23, v48, v33
Mva 152,204,28
v_fma_f32 v65, -v25, v80, v65
VF 49,23,32,49
s_waitcnt lgkmcnt(2)
Mva 108,208,16
TR 196,2,33792
VF 81,25,64,81
Mva 124,208,20
TR 198,2,34304
VM 34,162,34
Mva 140,208,24
VM 66,162,66
VM 50,162,50
Mva 156,208,28
VM 82,162,82
TR 200,3,32768
s_waitcnt vmcnt(4)
s_barrier
s_waitcnt lgkmcnt(3)
Mva 96,192,32
s_mov_b32 m0, s69
VM 190,212,34
Mva 112,192,36
VM 191,214,66
TR 202,3,33280
Mva 128,192,40
s_mov_b32 s15, 2363392
buffer_load_dwordx4 v1, s[16:19], s15 offen offset:0 lds
VM 34,213,34
Mva 144,192,44
VM 66,215,66
VF 190,213,50,190
s_waitcnt lgkmcnt(2)
Mva 100,196,32
VF 191,215,82,191
TR 204,3,33792
Mva 116,196,36
s_mov_b32 s15, 2362368
buffer_load_dwordx4 v1, s[20:23], s15 offen offset:1024 lds
v_fma_f32 v50, -v212, v50, v34
Mva 132,196,40
TR 206,3,34304
v_fma_f32 v82, -v214, v82, v66
VC 162,190,50
Mva 148,196,44
VC 178,191,82
VF 34,22,33,190
s_waitcnt lgkmcnt(2)
Mva 104,200,32
)"
R"(s_mov_b32 s15, 2656256
buffer_load_dwordx4 v1, s[16:19], s15 offen offset:2048 lds
Mva 120,200,36
TR 208,2,34816
VF 66,24,65,191
VF 50,22,49,50
Mva 136,200,40
TR 210,2,35328
VF 82,24,81,82
Mva 152,200,44
s_mov_b32 s15, 2655232
buffer_load_dwordx4 v1, s[20:23], s15 offen offset:3072 lds
v_fma_f32 v34, -v23, v49, v34
s_waitcnt lgkmcnt(2)
Mva 108,204,32
v_fma_f32 v66, -v25, v81, v66
VF 50,23,33,50
Mva 124,204,36
TR 192,2,35840
VF 82,25,65,82
Mva 140,204,40
TR 194,2,36352
VM 35,163,35
VM 67,163,67
Mva 156,204,44
VM 51,163,51
VM 83,163,83
s_waitcnt lgkmcnt(2)
Mva 96,208,48
TR 196,3,34816
VM 190,212,35
Mva 112,208,52
VM 191,214,67
TR 198,3,35328
Mva 128,208,56
VM 35,213,35
VM 67,215,67
Mva 144,208,60
VF 190,213,51,190
VF 191,215,83,191
s_waitcnt lgkmcnt(2)
Mva 100,192,48
TR 200,3,35840
v_fma_f32 v51, -v212, v51, v35
Mva 116,192,52
TR 202,3,36352
v_fma_f32 v83, -v214, v83, v67
Mva 132,192,56
VC 163,190,51
VC 179,191,83
Mva 148,192,60
VF 35,22,34,190
TR 204,2,49152
s_waitcnt lgkmcnt(3)
Mva 104,196,48
VF 67,24,66,191
VF 51,22,50,51
Mva 120,196,52
TR 206,2,49664
VF 83,24,82,83
Mva 136,196,56
v_fma_f32 v35, -v23, v50, v35
v_fma_f32 v67, -v25, v82, v67
Mva 152,196,60
VF 51,23,34,51
TR 208,2,50176
s_waitcnt lgkmcnt(3)
Mva 108,200,48
VF 83,25,66,83
TR 210,2,50688
Mva 124,200,52
VM 36,164,36
VM 68,164,68
Mva 140,200,56
VM 52,164,52
VM 84,164,84
Mva 156,200,60
TR 192,3,49152
VM 190,212,36
s_waitcnt vmcnt(4)
s_barrier
s_waitcnt lgkmcnt(3)
Mva 96,204,64
s_mov_b32 m0, s70
VM 191,214,68
Mva 112,204,68
s_mov_b32 s15, 2953216
buffer_load_dwordx4 v1, s[16:19], s15 offen offset:0 lds
Mva 128,204,72
TR 194,3,49664
VM 36,213,36
Mva 144,204,76
VM 68,215,68
VF 190,213,52,190
s_waitcnt lgkmcnt(2)
Mva 100,208,64
VF 191,215,84,191
TR 196,3,50176
Mva 116,208,68
s_mov_b32 s15, 2952192
buffer_load_dwordx4 v1, s[20:23], s15 offen offset:1024 lds
v_fma_f32 v52, -v212, v52, v36
Mva 132,208,72
TR 198,3,50688
v_fma_f32 v84, -v214, v84, v68
VC 164,190,52
Mva 148,208,76
VC 180,191,84
VF 36,22,35,190
s_waitcnt lgkmcnt(2)
Mva 104,192,64
s_mov_b32 s15, 3246080
buffer_load_dwordx4 v1, s[16:19], s15 offen offset:2048 lds
Mva 120,192,68
TR 200,2,51200
VF 68,24,67,191
VF 52,22,51,52
Mva 136,192,72
TR 202,2,51712
VF 84,24,83,84
Mva 152,192,76
s_mov_b32 s15, 3245056
buffer_load_dwordx4 v1, s[20:23], s15 offen offset:3072 lds
v_fma_f32 v36, -v23, v51, v36
v_fma_f32 v68, -v25, v83, v68
s_waitcnt lgkmcnt(2)
Mva 108,196,64
VF 52,23,35,52
TR 204,2,52224
Mva 124,196,68
VF 84,25,67,84
TR 206,2,52736
Mva 140,196,72
VM 37,165,37
VM 69,165,69
Mva 156,196,76
VM 53,165,53
VM 85,165,85
s_waitcnt lgkmcnt(2)
Mva 96,200,80
TR 208,3,51200
VM 190,212,37
Mva 112,200,84
VM 191,214,69
TR 210,3,51712
Mva 128,200,88
VM 37,213,37
VM 69,215,69
Mva 144,200,92
VF 190,213,53,190
VF 191,215,85,191
s_waitcnt lgkmcnt(2)
Mva 100,204,80
TR 192,3,52224
v_fma_f32 v53, -v212, v53, v37
Mva 116,204,84
TR 194,3,52736
v_fma_f32 v85, -v214, v85, v69
Mva 132,204,88
VC 165,190,53
VC 181,191,85
Mva 148,204,92
VF 37,22,36,190
VF 69,24,68,191
s_waitcnt lgkmcnt(2)
Mva 104,208,80
TR 196,2,0
VF 53,22,52,53
Mva 120,208,84
TR 198,2,512
VF 85,24,84,85
Mva 136,208,88
v_fma_f32 v37, -v23, v52, v37
v_fma_f32 v69, -v25, v84, v69
Mva 152,208,92
VF 53,23,36,53
TR 200,2,1024
s_waitcnt lgkmcnt(3)
Mva 108,192,80
VF 85,25,68,85
TR 202,2,1536
Mva 124,192,84
VM 38,166,38
VM 70,166,70
Mva 140,192,88
VM 54,166,54
VM 86,166,86
Mva 156,192,92
VM 190,212,38
TR 204,3,0
s_waitcnt vmcnt(4)
s_barrier
s_waitcnt lgkmcnt(3)
Mva 96,196,96
s_mov_b32 m0, s71
VM 191,214,70
Mva 112,196,100
s_mov_b32 s15, 3543040
buffer_load_dwordx4 v1, s[16:19], s15 offen offset:0 lds
Mva 128,196,104
TR 206,3,512
VM 38,213,38
Mva 144,196,108
VM 70,215,70
VF 190,213,54,190
s_waitcnt lgkmcnt(2)
Mva 100,200,96
VF 191,215,86,191
TR 208,3,1024
Mva 116,200,100
s_mov_b32 s15, 3542016
buffer_load_dwordx4 v1, s[20:23], s15 offen offset:1024 lds
v_fma_f32 v54, -v212, v54, v38
Mva 132,200,104
TR 210,3,1536
v_fma_f32 v86, -v214, v86, v70
VC 166,190,54
Mva 148,200,108
VC 182,191,86
VF 38,22,37,190
s_waitcnt lgkmcnt(2)
Mva 104,204,96
s_mov_b32 s15, 3835904
buffer_load_dwordx4 v1, s[16:19], s15 offen offset:2048 lds
VF 70,24,69,191
Mva 120,204,100
TR 192,2,2048
VF 54,22,53,54
Mva 136,204,104
TR 194,2,2560
VF 86,24,85,86
Mva 152,204,108
s_mov_b32 s15, 3834880
buffer_load_dwordx4 v1, s[20:23], s15 offen offset:3072 lds
v_fma_f32 v38, -v23, v53, v38
v_fma_f32 v70, -v25, v85, v70
s_waitcnt lgkmcnt(2)
Mva 108,208,96
VF 54,23,37,54
TR 196,2,3072
Mva 124,208,100
VF 86,25,69,86
TR 198,2,3584
Mva 140,208,104
VM 39,167,39
VM 71,167,71
Mva 156,208,108
VM 55,167,55
VM 87,167,87
s_waitcnt lgkmcnt(2)
Mva 96,192,112
VM 190,212,39
TR 200,3,2048
Mva 112,192,116
VM 191,214,71
TR 202,3,2560
Mva 128,192,120
VM 39,213,39
VM 71,215,71
Mva 144,192,124
VF 190,213,55,190
VF 191,215,87,191
s_waitcnt lgkmcnt(2)
Mva 100,196,112
TR 204,3,3072
v_fma_f32 v55, -v212, v55, v39
Mva 116,196,116
v_fma_f32 v87, -v214, v87, v71
TR 206,3,3584
Mva 132,196,120
VC 167,190,55
VC 183,191,87
Mva 148,196,124
VF 39,22,38,190
VF 71,24,70,191
s_waitcnt lgkmcnt(2)
Mva 104,200,112
TR 208,2,16384
VF 55,22,54,55
Mva 120,200,116
TR 210,2,16896
VF 87,24,86,87
Mva 136,200,120
v_fma_f32 v39, -v23, v54, v39
v_fma_f32 v71, -v25, v86, v71
Mva 152,200,124
VF 55,23,38,55
TR 192,2,17408
s_waitcnt lgkmcnt(3)
Mva 108,204,112
VF 87,25,70,87
VM 40,168,40
Mva 124,204,116
TR 194,2,17920
VM 72,168,72
Mva 140,204,120
VM 56,168,56
VM 88,168,88
Mva 156,204,124
VM 36,212,40
TR 196,3,16384
s_waitcnt vmcnt(4)
s_barrier
s_waitcnt lgkmcnt(3)
Mva 96,208,128
s_mov_b32 m0, s68
VM 37,214,72
Mva 112,208,132
s_mov_b32 s15, 4132864
buffer_load_dwordx4 v1, s[16:19], s15 offen offset:0 lds
Mva 128,208,136
TR 198,3,16896
VM 40,213,40
Mva 144,208,140
VM 72,215,72
VF 36,213,56,36
s_waitcnt lgkmcnt(2)
Mva 100,192,128
VF 37,215,88,37
TR 200,3,17408
Mva 116,192,132
s_mov_b32 s15, 4131840
buffer_load_dwordx4 v1, s[20:23], s15 offen offset:1024 lds
v_fma_f32 v56, -v212, v56, v40
v_fma_f32 v88, -v214, v88, v72
Mva 132,192,136
TR 202,3,17920
VC 168,36,56
Mva 148,192,140
VC 184,37,88
VF 40,22,39,36
s_waitcnt lgkmcnt(2)
Mva 104,196,128
s_mov_b32 s15, 4425728
buffer_load_dwordx4 v1, s[16:19], s15 offen offset:2048 lds
VF 72,24,71,37
Mva 120,196,132
TR 204,2,18432
VF 56,22,55,56
Mva 136,196,136
TR 206,2,18944
VF 88,24,87,88
Mva 152,196,140
s_mov_b32 s15, 4424704
buffer_load_dwordx4 v1, s[20:23], s15 offen offset:3072 lds
v_fma_f32 v40, -v23, v55, v40
v_fma_f32 v72, -v25, v87, v72
s_waitcnt lgkmcnt(2)
Mva 108,200,128
VF 56,23,39,56
TR 208,2,19456
Mva 124,200,132
VF 88,25,71,88
VM 41,169,41
Mva 140,200,136
TR 210,2,19968
VM 73,169,73
Mva 156,200,140
VM 57,169,57
VM 89,169,89
s_waitcnt lgkmcnt(2)
Mva 96,204,144
VM 36,212,41
TR 192,3,18432
Mva 112,204,148
VM 37,214,73
TR 194,3,18944
Mva 128,204,152
VM 41,213,41
VM 73,215,73
Mva 144,204,156
VF 36,213,57,36
VF 37,215,89,37
s_waitcnt lgkmcnt(2)
Mva 100,208,144
TR 196,3,19456
v_fma_f32 v57, -v212, v57, v41
Mva 116,208,148
v_fma_f32 v89, -v214, v89, v73
TR 198,3,19968
Mva 132,208,152
VC 169,36,57
VC 185,37,89
Mva 148,208,156
VF 41,22,40,36
VF 73,24,72,37
s_waitcnt lgkmcnt(2)
Mva 104,192,144
TR 200,2,32768
VF 57,22,56,57
Mva 120,192,148
TR 202,2,33280
VF 89,24,88,89
Mva 136,192,152
v_fma_f32 v41, -v23, v56, v41
v_fma_f32 v73, -v25, v88, v73
Mva 152,192,156
VF 57,23,40,57
TR 204,2,33792
s_waitcnt lgkmcnt(3)
Mva 108,196,144
VF 89,25,72,89
VM 42,170,42
Mva 124,196,148
TR 206,2,34304
VM 74,170,74
Mva 140,196,152
VM 58,170,58
VM 90,170,90
Mva 156,196,156
VM 36,212,42
TR 208,3,32768
s_waitcnt vmcnt(4)
s_barrier
s_waitcnt lgkmcnt(3)
Mva 96,200,160
s_mov_b32 m0, s69
VM 37,214,74
Mva 112,200,164
s_mov_b32 s15, 4722688
buffer_load_dwordx4 v1, s[16:19], s15 offen offset:0 lds
Mva 128,200,168
TR 210,3,33280
VM 42,213,42
VM 74,215,74
Mva 144,200,172
VF 36,213,58,36
VF 37,215,90,37
s_waitcnt lgkmcnt(2)
Mva 100,204,160
s_mov_b32 s15, 4721664
buffer_load_dwordx4 v1, s[20:23], s15 offen offset:1024 lds
Mva 116,204,164
TR 192,3,33792
v_fma_f32 v58, -v212, v58, v42
v_fma_f32 v90, -v214, v90, v74
Mva 132,204,168
TR 194,3,34304
VC 170,36,58
Mva 148,204,172
VC 186,37,90
VF 42,22,41,36
s_waitcnt lgkmcnt(2)
Mva 104,208,160
s_mov_b32 s15, 5015552
buffer_load_dwordx4 v1, s[16:19], s15 offen offset:2048 lds
VF 74,24,73,37
Mva 120,208,164
TR 196,2,34816
VF 58,22,57,58
Mva 136,208,168
TR 198,2,35328
VF 90,24,89,90
v_fma_f32 v42, -v23, v57, v42
Mva 152,208,172
s_mov_b32 s15, 5014528
buffer_load_dwordx4 v1, s[20:23], s15 offen offset:3072 lds
v_fma_f32 v74, -v25, v89, v74
s_waitcnt lgkmcnt(2)
Mva 108,192,160
VF 58,23,41,58
TR 200,2,35840
Mva 124,192,164
VF 90,25,73,90
VM 43,171,43
Mva 140,192,168
TR 202,2,36352
VM 75,171,75
Mva 156,192,172
VM 59,171,59
VM 91,171,91
s_waitcnt lgkmcnt(2)
Mva 96,196,176
VM 36,212,43
TR 204,3,34816
Mva 112,196,180
VM 37,214,75
TR 206,3,35328
Mva 128,196,184
VM 43,213,43
VM 75,215,75
Mva 144,196,188
VF 36,213,59,36
VF 37,215,91,37
s_waitcnt lgkmcnt(2)
Mva 100,200,176
TR 208,3,35840
v_fma_f32 v59, -v212, v59, v43
Mva 116,200,180
v_fma_f32 v91, -v214, v91, v75
TR 210,3,36352
Mva 132,200,184
VC 171,36,59
VC 187,37,91
Mva 148,200,188
VF 43,22,42,36
VF 75,24,74,37
s_waitcnt lgkmcnt(2)
Mva 104,204,176
TR 192,2,49152
VF 59,22,58,59
Mva 120,204,180
TR 194,2,49664
VF 91,24,90,91
Mva 136,204,184
v_fma_f32 v43, -v23, v58, v43
v_fma_f32 v75, -v25, v90, v75
Mva 152,204,188
VF 59,23,42,59
TR 196,2,50176
s_waitcnt lgkmcnt(3)
Mva 108,208,176
VF 91,25,74,91
VM 44,172,44
Mva 124,208,180
TR 198,2,50688
VM 76,172,76
Mva 140,208,184
VM 60,172,60
VM 92,172,92
Mva 156,208,188
VM 36,212,44
TR 200,3,49152
s_waitcnt vmcnt(4)
s_barrier
s_waitcnt lgkmcnt(3)
Mva 96,192,192
s_mov_b32 m0, s70
VM 37,214,76
Mva 112,192,196
s_mov_b32 s15, 4224
buffer_load_dwordx4 v1, s[16:19], s15 offen offset:0 lds
Mva 128,192,200
TR 202,3,49664
VM 44,213,44
VM 76,215,76
Mva 144,192,204
VF 36,213,60,36
VF 37,215,92,37
s_waitcnt lgkmcnt(2)
Mva 100,196,192
s_mov_b32 s15, 3200
buffer_load_dwordx4 v1, s[20:23], s15 offen offset:1024 lds
Mva 116,196,196
TR 204,3,50176
v_fma_f32 v60, -v212, v60, v44
v_fma_f32 v92, -v214, v92, v76
Mva 132,196,200
TR 206,3,50688
VC 172,36,60
Mva 148,196,204
VC 188,37,92
VF 44,22,43,36
s_waitcnt lgkmcnt(2)
Mva 104,200,192
s_mov_b32 s15, 297088
buffer_load_dwordx4 v1, s[16:19], s15 offen offset:2048 lds
VF 76,24,75,37
Mva 120,200,196
TR 208,2,51200
VF 60,22,59,60
Mva 136,200,200
TR 210,2,51712
VF 92,24,91,92
v_fma_f32 v44, -v23, v59, v44
Mva 152,200,204
s_mov_b32 s15, 296064
buffer_load_dwordx4 v1, s[20:23], s15 offen offset:3072 lds
v_fma_f32 v76, -v25, v91, v76
s_waitcnt lgkmcnt(2)
Mva 108,204,192
VF 60,23,43,60
VF 92,25,75,92
Mva 124,204,196
TR 192,2,52224
VM 45,173,45
Mva 140,204,200
TR 194,2,52736
VM 77,173,77
Mva 156,204,204
VM 61,173,61
VM 93,173,93
s_waitcnt lgkmcnt(2)
Mva 96,208,208
VM 36,212,45
TR 196,3,51200
Mva 112,208,212
VM 37,214,77
TR 198,3,51712
Mva 128,208,216
VM 45,213,45
VM 77,215,77
Mva 144,208,220
VF 36,213,61,36
VF 37,215,93,37
s_waitcnt lgkmcnt(2)
Mva 100,192,208
v_fma_f32 v61, -v212, v61, v45
TR 200,3,52224
Mva 116,192,212
)"
R"(v_fma_f32 v93, -v214, v93, v77
TR 202,3,52736
Mva 132,192,216
VC 173,36,61
VC 189,37,93
Mva 148,192,220
VF 45,22,44,36
VF 77,24,76,37
s_waitcnt lgkmcnt(2)
Mva 104,196,208
TR 204,2,0
VF 61,22,60,61
Mva 120,196,212
TR 206,2,512
VF 93,24,92,93
Mva 136,196,216
v_fma_f32 v45, -v23, v60, v45
v_fma_f32 v77, -v25, v92, v77
Mva 152,196,220
VF 61,23,44,61
VF 93,25,76,93
s_waitcnt lgkmcnt(2)
Mva 108,200,208
TR 208,2,1024
VM 46,174,46
Mva 124,200,212
TR 210,2,1536
VM 78,174,78
Mva 140,200,216
VM 62,174,62
VM 94,174,94
Mva 156,200,220
VM 36,212,46
TR 192,3,0
s_waitcnt vmcnt(4)
s_barrier
s_waitcnt lgkmcnt(3)
Mva 96,204,224
s_mov_b32 m0, s71
VM 37,214,78
Mva 112,204,228
s_mov_b32 s15, 594048
buffer_load_dwordx4 v1, s[16:19], s15 offen offset:0 lds
Mva 128,204,232
TR 194,3,512
VM 46,213,46
VM 78,215,78
Mva 144,204,236
VF 36,213,62,36
VF 37,215,94,37
s_waitcnt lgkmcnt(2)
Mva 100,208,224
v_fma_f32 v62, -v212, v62, v46
TR 196,3,1024
Mva 116,208,228
s_mov_b32 s15, 593024
buffer_load_dwordx4 v1, s[20:23], s15 offen offset:1024 lds
v_fma_f32 v94, -v214, v94, v78
Mva 132,208,232
TR 198,3,1536
VC 174,36,62
Mva 148,208,236
VC 190,37,94
VF 46,22,45,36
s_waitcnt lgkmcnt(2)
Mva 104,192,224
s_mov_b32 s15, 886912
buffer_load_dwordx4 v1, s[16:19], s15 offen offset:2048 lds
VF 78,24,77,37
Mva 120,192,228
TR 200,2,2048
VF 62,22,61,62
VF 94,24,93,94
Mva 136,192,232
TR 202,2,2560
v_fma_f32 v46, -v23, v61, v46
Mva 152,192,236
s_mov_b32 s15, 885888
buffer_load_dwordx4 v1, s[20:23], s15 offen offset:3072 lds
v_fma_f32 v78, -v25, v93, v78
s_waitcnt lgkmcnt(2)
Mva 108,196,224
VF 62,23,45,62
VF 94,25,77,94
Mva 124,196,228
TR 204,2,3072
VM 47,175,47
Mva 140,196,232
TR 206,2,3584
VM 79,175,79
Mva 156,196,236
VM 63,175,63
VM 95,175,95
s_waitcnt lgkmcnt(2)
Mva 96,200,240
VM 36,212,47
TR 208,3,2048
Mva 112,200,244
VM 37,214,79
VM 47,213,47
Mva 128,200,248
TR 210,3,2560
VM 79,215,79
Mva 144,200,252
VF 36,213,63,36
VF 37,215,95,37
s_waitcnt lgkmcnt(2)
Mva 100,204,240
v_fma_f32 v63, -v212, v63, v47
TR 192,3,3072
Mva 116,204,244
v_fma_f32 v95, -v214, v95, v79
TR 194,3,3584
Mva 132,204,248
VC 175,36,63
VC 191,37,95
Mva 148,204,252
VF 32,22,46,36
VF 34,24,78,37
s_waitcnt lgkmcnt(2)
Mva 104,208,240
TR 196,2,16384
VF 33,22,62,63
Mva 120,208,244
VF 35,24,94,95
TR 198,2,16896
Mva 136,208,248
v_fma_f32 v32, -v23, v62, v32
v_fma_f32 v34, -v25, v94, v34
Mva 152,208,252
VF 33,23,46,33
VF 35,25,78,35
s_waitcnt lgkmcnt(2)
Mva 108,192,240
TR 200,2,17408
TR 202,2,17920
Mva 124,192,244
ds_write_b128 v31, v[32:35] offset:4096
Mva 140,192,248
TR 204,3,16384
Mva 156,192,252
TR 206,3,16896
s_waitcnt lgkmcnt(2)
s_waitcnt vmcnt(4)
s_barrier
Mvv 96,196,224
s_mov_b32 m0, s68
v_lshrrev_b32 v38, 1, v0
Mvv 112,196,228
v_and_b32 v38, 8, v38
v_add_u32 v38, v30, v38
Mvv 128,196,232
s_mov_b32 s15, 1183872
buffer_load_dwordx4 v1, s[16:19], s15 offen offset:0 lds
ds_read_b64 v[40:41], v38 offset:4096
ds_read_b64 v[42:43], v38 offset:4608
Mvv 144,196,236
ds_read_b64 v[44:45], v38 offset:5120
ds_read_b64 v[46:47], v38 offset:5632
Mvv 100,200,224
ds_read_b64 v[48:49], v38 offset:6144
TR 208,3,17408
Mvv 116,200,228
s_mov_b32 s15, 1182848
buffer_load_dwordx4 v1, s[20:23], s15 offen offset:1024 lds
ds_read_b64 v[50:51], v38 offset:6656
ds_read_b64 v[52:53], v38 offset:7168
Mvv 132,200,232
TR 210,3,17920
ds_read_b64 v[54:55], v38 offset:7680
s_lshl_b32 s90, s13, 1
s_add_u32 s90, s90, s65
Mvv 148,200,236
s_lshl_b32 s90, s90, 4
s_add_u32 s90, s90, s14
s_lshl_b32 s90, s90, 9
s_waitcnt lgkmcnt(10)
Mvv 104,204,224
s_mov_b32 s15, 1476736
buffer_load_dwordx4 v1, s[16:19], s15 offen offset:2048 lds
v_and_b32 v39, 15, v0
v_lshlrev_b32 v39, 5, v39
Mvv 120,204,228
TR 192,2,18432
v_and_b32 v60, 16, v0
Mvv 136,204,232
TR 194,2,18944
v_add3_u32 v39, v39, v60, s90
s_lshl_b32 s91, s64, 6
v_add_u32 v60, s91, v0
Mvv 152,204,236
s_mov_b32 s15, 1475712
buffer_load_dwordx4 v1, s[20:23], s15 offen offset:3072 lds
v_cmp_gt_u32 vcc, 32, v60
s_waitcnt lgkmcnt(3)
Mvv 108,208,224
v_mov_b32 v57, s101
v_mov_b32 v59, s101
Mvv 124,208,228
TR 196,2,19456
v_bfrev_b32 v60, 1
v_cndmask_b32 v39, v60, v39, vcc
Mvv 140,208,232
TR 198,2,19968
VF 42,222,40,42
VF 43,222,41,43
Mvv 156,208,236
v_fma_f32 v42, -v223, v41, v42
VF 43,223,40,43
s_waitcnt lgkmcnt(2)
Mvv 96,192,240
VF 44,222,42,44
TR 200,3,18432
Mvv 112,192,244
VF 45,222,43,45
v_fma_f32 v44, -v223, v43, v44
Mvv 128,192,248
TR 202,3,18944
VF 45,223,42,45
VF 46,222,44,46
Mvv 144,192,252
VF 47,222,45,47
v_fma_f32 v46, -v223, v45, v46
s_waitcnt lgkmcnt(2)
Mvv 100,196,240
VF 47,223,44,47
TR 204,3,19456
Mvv 116,196,244
VF 48,222,46,48
VF 49,222,47,49
Mvv 132,196,248
TR 206,3,19968
v_fma_f32 v48, -v223, v47, v48
VF 49,223,46,49
Mvv 148,196,252
VF 50,222,48,50
VF 51,222,49,51
s_waitcnt lgkmcnt(2)
Mvv 104,200,240
v_fma_f32 v50, -v223, v49, v50
TR 208,2,32768
Mvv 120,200,244
VF 51,223,48,51
VF 52,222,50,52
Mvv 136,200,248
TR 210,2,33280
VF 53,222,51,53
v_fma_f32 v52, -v223, v51, v52
Mvv 152,200,252
VF 53,223,50,53
VF 56,222,52,54
s_waitcnt lgkmcnt(2)
Mvv 108,204,240
VF 58,222,53,55
TR 192,2,33792
Mvv 124,204,244
v_fma_f32 v56, -v223, v53, v56
VF 58,223,52,58
Mvv 140,204,248
TR 194,2,34304
Mvv 156,204,252
buffer_store_dwordx4 v[56:59], v39, s[56:59], 0 offen sc1
s_waitcnt vmcnt(5)
s_barrier
s_waitcnt lgkmcnt(2)
Zva 32,208,0
s_mov_b32 m0, s69
TR 196,3,32768
Zva 48,208,4
s_mov_b32 s15, 1773696
buffer_load_dwordx4 v1, s[16:19], s15 offen offset:0 lds
Zva 64,208,8
TR 198,3,33280
VM 100,96,100
Zva 80,208,12
VM 101,97,101
TR 200,3,33792
s_waitcnt lgkmcnt(3)
Zva 36,192,0
s_mov_b32 s15, 1772672
buffer_load_dwordx4 v1, s[20:23], s15 offen offset:1024 lds
Zva 52,192,4
VM 102,98,102
TR 202,3,34304
Zva 68,192,8
VM 96,6,96
VM 103,99,103
Zva 84,192,12
s_mov_b32 s15, 2066560
buffer_load_dwordx4 v1, s[16:19], s15 offen offset:2048 lds
s_waitcnt lgkmcnt(2)
Zva 40,196,0
VM 97,6,97
TR 204,2,34816
Zva 56,196,4
VM 108,104,108
TR 206,2,35328
Zva 72,196,8
VM 98,6,98
TR 208,2,35840
Zva 88,196,12
s_mov_b32 s15, 2065536
buffer_load_dwordx4 v1, s[20:23], s15 offen offset:3072 lds
s_waitcnt lgkmcnt(3)
Zva 44,200,0
v_exp_f32 v96, v96
Zva 60,200,4
VM 109,105,109
TR 210,2,36352
Zva 76,200,8
VM 99,6,99
TR 192,3,34816
Zva 92,200,12
v_exp_f32 v97, v97
s_waitcnt lgkmcnt(3)
Mva 32,204,16
VM 110,106,110
TR 194,3,35328
Mva 48,204,20
VM 104,6,104
TR 196,3,35840
Mva 64,204,24
v_exp_f32 v98, v98
Mva 80,204,28
VF 96,96,10,10
TR 198,3,36352
s_waitcnt lgkmcnt(4)
Mva 36,208,16
VM 111,107,111
VM 105,6,105
Mva 52,208,20
TR 200,2,49152
Mva 68,208,24
v_exp_f32 v99, v99
Mva 84,208,28
TR 202,2,49664
VF 97,97,10,10
s_waitcnt lgkmcnt(4)
Mva 40,192,16
VM 106,6,106
TR 204,2,50176
Mva 56,192,20
v_exp_f32 v104, v104
Mva 72,192,24
TR 206,2,50688
VF 98,98,10,10
Mva 88,192,28
v_rcp_f32 v96, v96
s_waitcnt lgkmcnt(4)
Mva 44,196,16
TR 208,3,49152
VM 107,6,107
Mva 60,196,20
TR 210,3,49664
Mva 76,196,24
v_exp_f32 v105, v105
Mva 92,196,28
VF 99,99,10,10
TR 192,3,50176
s_waitcnt vmcnt(5)
s_barrier
s_waitcnt lgkmcnt(5)
Mva 32,200,32
s_mov_b32 m0, s70
TR 194,3,50688
Mva 48,200,36
s_mov_b32 s15, 2363520
buffer_load_dwordx4 v1, s[16:19], s15 offen offset:0 lds
Mva 64,200,40
v_rcp_f32 v97, v97
Mva 80,200,44
v_exp_f32 v106, v106
s_waitcnt lgkmcnt(4)
Mva 36,204,32
s_mov_b32 s15, 2362496
buffer_load_dwordx4 v1, s[20:23], s15 offen offset:1024 lds
Mva 52,204,36
TR 196,2,51200
VF 104,104,10,10
Mva 68,204,40
TR 198,2,51712
Mva 84,204,44
s_mov_b32 s15, 2656384
buffer_load_dwordx4 v1, s[16:19], s15 offen offset:2048 lds
s_waitcnt lgkmcnt(4)
Mva 40,208,32
v_rcp_f32 v98, v98
Mva 56,208,36
VM 100,100,96
TR 200,2,52224
Mva 72,208,40
s_mov_b32 s15, 2655360
buffer_load_dwordx4 v1, s[20:23], s15 offen offset:3072 lds
Mva 88,208,44
TR 202,2,52736
s_waitcnt lgkmcnt(4)
Mva 44,192,32
v_exp_f32 v107, v107
Mva 60,192,36
VF 105,105,10,10
TR 204,3,51200
Mva 76,192,40
v_rcp_f32 v99, v99
Mva 92,192,44
TR 206,3,51712
VM 101,101,97
s_waitcnt lgkmcnt(4)
Mva 32,196,48
VF 106,106,10,10
TR 208,3,52224
Mva 48,196,52
v_rcp_f32 v104, v104
Mva 64,196,56
TR 210,3,52736
VM 102,102,98
Mva 80,196,60
VF 107,107,10,10
TR 192,2,0
s_waitcnt lgkmcnt(5)
Mva 36,200,48
v_rcp_f32 v105, v105
Mva 52,200,52
TR 194,2,512
VM 103,103,99
Mva 68,200,56
v_lshrrev_b32 v212, 4, v0
s_lshl_b32 s91, s64, 2
Mva 84,200,60
v_add_u32 v212, s91, v212
v_lshlrev_b32 v212, 10, v212
s_waitcnt lgkmcnt(4)
Mva 40,204,48
v_rcp_f32 v106, v106
Mva 56,204,52
TR 196,2,1024
VM 108,108,104
Mva 72,204,56
v_and_b32 v213, 15, v0
TR 198,2,1536
Mva 88,204,60
v_rcp_f32 v107, v107
s_waitcnt lgkmcnt(4)
Mva 44,208,48
v_lshl_add_u32 v212, v213, 5, v212
VM 109,109,105
Mva 60,208,52
s_sub_u32 s91, s90, 8192
TR 200,3,0
Mva 76,208,56
VM 110,110,106
TR 202,3,512
Mva 92,208,60
VM 111,111,107
v_add_u32 v212, s91, v212
s_waitcnt vmcnt(4)
s_barrier
s_waitcnt lgkmcnt(4)
Mva 32,192,64
s_mov_b32 m0, s71
VC 96,100,101
Mva 48,192,68
s_mov_b32 s15, 2953344
buffer_load_dwordx4 v1, s[16:19], s15 offen offset:0 lds
Mva 64,192,72
v_add_u32 v213, 512, v212
VC 97,102,103
Mva 80,192,76
TR 204,3,1024
VC 98,108,109
s_waitcnt lgkmcnt(3)
Mva 36,196,64
s_mov_b32 s15, 2952320
buffer_load_dwordx4 v1, s[20:23], s15 offen offset:1024 lds
Mva 52,196,68
v_bfrev_b32 v214, 1
TR 206,3,1536
Mva 68,196,72
VC 99,110,111
v_cndmask_b32 v212, v214, v212, s[84:85]
Mva 84,196,76
s_mov_b32 s15, 3246208
buffer_load_dwordx4 v1, s[16:19], s15 offen offset:2048 lds
s_waitcnt lgkmcnt(2)
Mva 40,200,64
TR 208,2,2048
v_cndmask_b32 v213, v214, v213, s[86:87]
Mva 56,200,68
ds_write_b128 v5, v[96:99] offset:0
Mva 72,200,72
TR 210,2,2560
VM 116,112,116
s_mov_b32 s92, 0x40000
Mva 88,200,76
s_mov_b32 s15, 3245184
buffer_load_dwordx4 v1, s[20:23], s15 offen offset:3072 lds
VM 117,113,117
s_waitcnt lgkmcnt(3)
Mva 44,204,64
VM 118,114,118
buffer_load_dwordx4 v[96:99], v212, s[56:59], 0 offen offset:0 sc1
Mva 60,204,68
TR 192,2,3072
VM 112,7,112
Mva 76,204,72
TR 194,2,3584
VM 119,115,119
buffer_load_dwordx4 v[100:103], v212, s[56:59], 0 offen offset:16 sc1
Mva 92,204,76
VM 113,7,113
VM 124,120,124
s_waitcnt lgkmcnt(2)
Mva 32,208,80
buffer_load_dwordx4 v[104:107], v213, s[56:59], 0 offen offset:0 sc1
Mva 48,208,84
TR 196,3,2048
VM 114,7,114
Mva 64,208,88
TR 198,3,2560
buffer_load_dwordx4 v[108:111], v213, s[56:59], 0 offen offset:16 sc1
Mva 80,208,92
v_exp_f32 v112, v112
s_waitcnt lgkmcnt(2)
Mva 36,192,80
VM 125,121,125
TR 200,3,3072
Mva 52,192,84
VM 115,7,115
TR 202,3,3584
Mva 68,192,88
v_exp_f32 v113, v113
Mva 84,192,92
VM 126,122,126
TR 204,2,16384
s_waitcnt lgkmcnt(3)
Mva 40,196,80
VM 120,7,120
TR 206,2,16896
Mva 56,196,84
v_exp_f32 v114, v114
Mva 72,196,88
VF 112,112,11,11
TR 208,2,17408
Mva 88,196,92
VM 127,123,127
TR 210,2,17920
s_waitcnt lgkmcnt(4)
Mva 44,200,80
VM 121,7,121
TR 192,3,16384
Mva 60,200,84
v_exp_f32 v115, v115
Mva 76,200,88
VF 113,113,11,11
TR 194,3,16896
Mva 92,200,92
VM 122,7,122
TR 196,3,17408
s_waitcnt vmcnt(8)
s_barrier
s_waitcnt lgkmcnt(5)
Mva 32,204,96
s_mov_b32 m0, s68
TR 198,3,17920
Mva 48,204,100
s_mov_b32 s15, 3543168
buffer_load_dwordx4 v1, s[16:19], s15 offen offset:0 lds
Mva 64,204,104
v_exp_f32 v120, v120
Mva 80,204,108
VF 114,114,11,11
TR 200,2,18432
s_waitcnt lgkmcnt(5)
Mva 36,208,96
s_mov_b32 s15, 3542144
buffer_load_dwordx4 v1, s[20:23], s15 offen offset:1024 lds
Mva 52,208,100
v_rcp_f32 v112, v112
Mva 68,208,104
VM 123,7,123
TR 202,2,18944
Mva 84,208,108
s_mov_b32 s15, 3836032
buffer_load_dwordx4 v1, s[16:19], s15 offen offset:2048 lds
s_waitcnt lgkmcnt(4)
Mva 40,192,96
v_exp_f32 v121, v121
Mva 56,192,100
VF 115,115,11,11
TR 204,2,19456
Mva 72,192,104
s_mov_b32 s15, 3835008
buffer_load_dwordx4 v1, s[20:23], s15 offen offset:3072 lds
Mva 88,192,108
v_rcp_f32 v113, v113
s_waitcnt lgkmcnt(3)
Mva 44,196,96
TR 206,2,19968
Mva 60,196,100
v_exp_f32 v122, v122
Mva 76,196,104
)"
R"(VF 120,120,11,11
TR 208,3,18432
Mva 92,196,108
TR 210,3,18944
s_waitcnt lgkmcnt(4)
Mva 32,200,112
v_rcp_f32 v114, v114
Mva 48,200,116
VM 116,116,112
TR 192,3,19456
Mva 64,200,120
v_exp_f32 v123, v123
Mva 80,200,124
TR 194,3,19968
VF 121,121,11,11
s_waitcnt lgkmcnt(4)
Mva 36,204,112
v_rcp_f32 v115, v115
Mva 52,204,116
TR 196,2,32768
VM 117,117,113
Mva 68,204,120
TR 198,2,33280
VF 122,122,11,11
Mva 84,204,124
v_rcp_f32 v120, v120
s_waitcnt lgkmcnt(4)
Mva 40,208,112
TR 200,2,33792
VM 118,118,114
Mva 56,208,116
TR 202,2,34304
VF 123,123,11,11
Mva 72,208,120
v_rcp_f32 v121, v121
Mva 88,208,124
TR 204,3,32768
VM 119,119,115
s_waitcnt lgkmcnt(5)
Mva 44,192,112
s_waitcnt vmcnt(4)
LCHK:
v_xor_b32 v97, s101, v97
v_xor_b32 v99, s101, v99
v_xor_b32 v101, s101, v101
v_xor_b32 v103, s101, v103
v_xor_b32 v105, s101, v105
v_xor_b32 v107, s101, v107
v_xor_b32 v109, s101, v109
v_xor_b32 v111, s101, v111
v_or3_b32 v214, v97, v99, v101
v_or3_b32 v215, v105, v107, v109
v_or_b32 v214, v214, v103
v_or_b32 v215, v215, v111
v_cmp_eq_u32 vcc, 0, v214
v_cmp_eq_u32 s[94:95], 0, v215
s_andn2_b64 s[96:97], s[84:85], vcc
s_andn2_b64 s[94:95], s[86:87], s[94:95]
s_or_b64 s[94:95], s[94:95], s[96:97]
s_cbranch_scc1 LRETRY
LGO:
Mva 60,192,116
VF 212,216,96,104
TR 206,3,33280
Mva 76,192,120
v_rcp_f32 v122, v122
Mva 92,192,124
VF 214,218,100,108
VM 124,124,120
s_barrier
s_waitcnt lgkmcnt(4)
Mva 32,196,128
s_mov_b32 m0, s69
TR 208,3,33792
Mva 48,196,132
s_mov_b32 s15, 4132992
buffer_load_dwordx4 v1, s[16:19], s15 offen offset:0 lds
Mva 64,196,136
TR 210,3,34304
VF 213,216,98,106
Mva 80,196,140
v_rcp_f32 v123, v123
s_waitcnt lgkmcnt(4)
Mva 36,200,128
s_mov_b32 s15, 4131968
buffer_load_dwordx4 v1, s[20:23], s15 offen offset:1024 lds
Mva 52,200,132
VM 125,125,121
VM 126,126,122
Mva 68,200,136
TR 192,2,34816
VM 127,127,123
Mva 84,200,140
s_mov_b32 s15, 4425856
buffer_load_dwordx4 v1, s[16:19], s15 offen offset:2048 lds
s_waitcnt lgkmcnt(3)
Mva 40,204,128
VF 215,218,102,110
TR 194,2,35328
Mva 56,204,132
VC 112,116,117
VC 113,118,119
Mva 72,204,136
s_mov_b32 s15, 4424832
buffer_load_dwordx4 v1, s[20:23], s15 offen offset:3072 lds
Mva 88,204,140
VC 114,124,125
v_fma_f32 v212, -v217, v98, v212
s_waitcnt lgkmcnt(2)
Mva 44,208,128
TR 196,2,35840
VC 115,126,127
Mva 60,208,132
TR 198,2,36352
v_fma_f32 v214, -v219, v102, v214
Mva 76,208,136
ds_write_b128 v5, v[112:115] offset:1024
Mva 92,208,140
TR 200,3,34816
VM 132,128,132
s_waitcnt lgkmcnt(4)
Mva 32,192,144
TR 202,3,35328
VM 133,129,133
Mva 48,192,148
VF 213,217,96,213
VM 134,130,134
Mva 64,192,152
VM 128,8,128
TR 204,3,35840
Mva 80,192,156
VM 135,131,135
VF 215,219,100,215
s_waitcnt lgkmcnt(4)
Mva 36,196,144
TR 206,3,36352
VM 129,8,129
Mva 52,196,148
VM 140,136,140
VM 130,8,130
Mva 68,196,152
TR 208,2,49152
Mva 84,196,156
TR 210,2,49664
s_waitcnt lgkmcnt(4)
Mva 40,200,144
v_exp_f32 v128, v128
Mva 56,200,148
VM 141,137,141
VM 131,8,131
Mva 72,200,152
TR 192,2,50176
Mva 88,200,156
TR 194,2,50688
s_waitcnt lgkmcnt(4)
Mva 44,204,144
v_exp_f32 v129, v129
Mva 60,204,148
VM 142,138,142
VM 136,8,136
Mva 76,204,152
ds_write_b128 v31, v[212:215] offset:8192
Mva 92,204,156
TR 196,3,49152
s_waitcnt lgkmcnt(1)
s_waitcnt vmcnt(4)
s_barrier
Mva 32,208,160
s_mov_b32 m0, s70
s_lshl_b32 s90, s13, 7
s_add_u32 s90, s90, 200704
s_cmp_eq_u32 s14, 15
Mva 48,208,164
s_mov_b32 s15, 4722816
buffer_load_dwordx4 v1, s[16:19], s15 offen offset:0 lds
Mva 64,208,168
TR 198,3,49664
v_exp_f32 v130, v130
s_cselect_b32 s90, s90, 0x80000000
Mva 80,208,172
VF 128,128,12,12
v_mov_b32 v96, s90
Mva 36,192,160
VM 143,139,143
v_mov_b32 v97, s101
Mva 52,192,164
s_mov_b32 s15, 4721792
buffer_load_dwordx4 v1, s[20:23], s15 offen offset:1024 lds
TR 200,3,50176
VM 137,8,137
Mva 68,192,168
TR 202,3,50688
buffer_store_dword v97, v96, s[56:59], 0 offen sc1
Mva 84,192,172
v_exp_f32 v131, v131
s_waitcnt lgkmcnt(2)
Mva 40,196,160
s_mov_b32 s15, 5015680
buffer_load_dwordx4 v1, s[16:19], s15 offen offset:2048 lds
VF 129,129,12,12
v_lshrrev_b32 v96, 1, v0
Mva 56,196,164
TR 204,2,51200
VM 138,8,138
v_and_b32 v96, 8, v96
Mva 72,196,168
TR 206,2,51712
v_add_u32 v96, v30, v96
Mva 88,196,172
s_mov_b32 s15, 5014656
buffer_load_dwordx4 v1, s[20:23], s15 offen offset:3072 lds
v_exp_f32 v136, v136
ds_read_b64 v[112:113], v96 offset:8192
s_waitcnt lgkmcnt(3)
Mva 44,200,160
VF 130,130,12,12
ds_read_b64 v[114:115], v96 offset:8704
Mva 60,200,164
TR 208,2,52224
ds_read_b64 v[116:117], v96 offset:9216
Mva 76,200,168
TR 210,2,52736
v_rcp_f32 v128, v128
ds_read_b64 v[118:119], v96 offset:9728
Mva 92,200,172
VM 139,8,139
ds_read_b64 v[120:121], v96 offset:10240
s_waitcnt lgkmcnt(7)
Mva 32,204,176
TR 192,3,51200
ds_read_b64 v[122:123], v96 offset:10752
Mva 48,204,180
v_exp_f32 v137, v137
Mva 64,204,184
TR 194,3,51712
VF 131,131,12,12
ds_read_b64 v[124:125], v96 offset:11264
Mva 80,204,188
ds_read_b64 v[126:127], v96 offset:11776
s_waitcnt lgkmcnt(10)
VF 114,220,112,114
s_waitcnt lgkmcnt(7)
Mva 36,208,176
v_rcp_f32 v129, v129
Mva 52,208,180
TR 196,3,52224
VF 115,220,113,115
Mva 68,208,184
TR 198,3,52736
v_exp_f32 v138, v138
v_fma_f32 v114, -v221, v113, v114
Mva 84,208,188
VF 136,136,12,12
VF 115,221,112,115
s_waitcnt lgkmcnt(4)
Mva 40,192,176
TR 200,2,0
VF 116,220,114,116
Mva 56,192,180
v_rcp_f32 v130, v130
Mva 72,192,184
TR 202,2,512
VM 132,132,128
VF 117,220,115,117
Mva 88,192,188
v_fma_f32 v116, -v221, v115, v116
VF 117,221,114,117
s_waitcnt lgkmcnt(2)
Mva 44,196,176
v_exp_f32 v139, v139
Mva 60,196,180
TR 204,2,1024
VF 137,137,12,12
VF 118,220,116,118
Mva 76,196,184
TR 206,2,1536
VF 119,220,117,119
Mva 92,196,188
v_rcp_f32 v131, v131
s_waitcnt vmcnt(5)
s_barrier
s_waitcnt lgkmcnt(2)
Mva 32,200,192
s_mov_b32 m0, s71
VM 133,133,129
v_fma_f32 v118, -v221, v117, v118
Mva 48,200,196
TR 208,3,0
VF 138,138,12,12
VF 119,221,116,119
Mva 64,200,200
s_mov_b32 s15, 4352
buffer_load_dwordx4 v1, s[16:19], s15 offen offset:0 lds
TR 210,3,512
VF 120,220,118,120
Mva 80,200,204
v_rcp_f32 v136, v136
VF 121,220,119,121
s_waitcnt lgkmcnt(2)
Mva 36,204,192
VM 134,134,130
v_fma_f32 v120, -v221, v119, v120
Mva 52,204,196
s_mov_b32 s15, 3328
buffer_load_dwordx4 v1, s[20:23], s15 offen offset:1024 lds
TR 192,3,1024
VF 139,139,12,12
VF 121,221,118,121
Mva 68,204,200
TR 194,3,1536
VF 122,220,120,122
Mva 84,204,204
v_rcp_f32 v137, v137
s_waitcnt lgkmcnt(2)
Mva 40,208,192
s_mov_b32 s15, 297216
buffer_load_dwordx4 v1, s[16:19], s15 offen offset:2048 lds
VM 135,135,131
VF 123,220,121,123
Mva 56,208,196
TR 196,2,2048
v_fma_f32 v122, -v221, v121, v122
Mva 72,208,200
TR 198,2,2560
v_rcp_f32 v138, v138
VF 123,221,120,123
Mva 88,208,204
s_mov_b32 s15, 296192
buffer_load_dwordx4 v1, s[20:23], s15 offen offset:3072 lds
VM 140,140,136
VF 124,220,122,124
s_waitcnt lgkmcnt(2)
Mva 44,192,192
VF 125,220,123,125
TR 200,2,3072
Mva 60,192,196
v_rcp_f32 v139, v139
v_fma_f32 v124, -v221, v123, v124
Mva 76,192,200
TR 202,2,3584
VM 141,141,137
VF 125,221,122,125
Mva 92,192,204
VM 142,142,138
VM 143,143,139
s_waitcnt lgkmcnt(2)
Mva 32,196,208
VF 98,220,124,126
TR 204,3,2048
Mva 48,196,212
VC 128,132,133
VF 99,220,125,127
Mva 64,196,216
TR 206,3,2560
VC 129,134,135
v_fma_f32 v98, -v221, v125, v98
Mva 80,196,220
VC 130,140,141
VF 99,221,124,99
s_waitcnt lgkmcnt(2)
Mva 36,200,208
VC 131,142,143
TR 208,3,3072
Mva 52,200,212
ds_write_b64 v96, v[98:99] offset:3584
Mva 68,200,216
TR 210,3,3584
ds_read_b128 v[96:99], v31 offset:0
Mva 84,200,220
ds_write_b128 v5, v[128:131] offset:2048
s_waitcnt lgkmcnt(5)
Mva 40,204,208
VM 148,144,148
ds_read_b128 v[100:103], v31 offset:512
Mva 56,204,212
TR 192,2,16384
VM 149,145,149
ds_read_b128 v[104:107], v31 offset:1024
Mva 72,204,216
TR 194,2,16896
VM 150,146,150
ds_read_b128 v[108:111], v31 offset:1536
Mva 88,204,220
VM 144,9,144
ds_read_b128 v[112:115], v31 offset:2048
s_waitcnt lgkmcnt(8)
Mva 44,208,208
VM 151,147,151
ds_read_b128 v[116:119], v31 offset:2560
Mva 60,208,212
TR 196,2,17408
VM 145,9,145
ds_read_b128 v[120:123], v31 offset:3072
Mva 76,208,216
TR 198,2,17920
VM 156,152,156
ds_read_b128 v[124:127], v31 offset:3584
Mva 92,208,220
VM 146,9,146
s_waitcnt lgkmcnt(10)
VF 100,26,96,100
s_waitcnt vmcnt(4)
s_barrier
s_waitcnt lgkmcnt(7)
Mva 32,192,224
s_mov_b32 m0, s68
TR 200,3,16384
Mva 48,192,228
v_exp_f32 v144, v144
VF 102,28,98,102
Mva 64,192,232
s_mov_b32 s15, 594176
buffer_load_dwordx4 v1, s[16:19], s15 offen offset:0 lds
TR 202,3,16896
VM 157,153,157
VF 101,26,97,101
Mva 80,192,236
VM 147,9,147
VF 103,28,99,103
s_waitcnt lgkmcnt(3)
Mva 36,196,224
v_fma_f32 v100, -v27, v97, v100
TR 204,3,17408
Mva 52,196,228
s_mov_b32 s15, 593152
buffer_load_dwordx4 v1, s[20:23], s15 offen offset:1024 lds
v_exp_f32 v145, v145
v_fma_f32 v102, -v29, v99, v102
Mva 68,196,232
TR 206,3,17920
VM 158,154,158
VF 101,27,96,101
Mva 84,196,236
VM 152,9,152
VF 103,29,98,103
s_waitcnt lgkmcnt(2)
Mva 40,200,224
s_mov_b32 s15, 887040
buffer_load_dwordx4 v1, s[16:19], s15 offen offset:2048 lds
Mva 56,200,228
TR 208,2,18432
v_exp_f32 v146, v146
VF 104,26,100,104
Mva 72,200,232
TR 210,2,18944
VF 144,144,13,13
VF 106,28,102,106
Mva 88,200,236
s_mov_b32 s15, 886016
buffer_load_dwordx4 v1, s[20:23], s15 offen offset:3072 lds
VM 159,155,159
VF 105,26,101,105
s_waitcnt lgkmcnt(2)
Mva 44,204,224
VM 153,9,153
VF 107,28,103,107
Mva 60,204,228
TR 192,2,19456
v_fma_f32 v104, -v27, v101, v104
Mva 76,204,232
TR 194,2,19968
v_exp_f32 v147, v147
v_fma_f32 v106, -v29, v103, v106
Mva 92,204,236
VF 145,145,13,13
VM 154,9,154
s_waitcnt lgkmcnt(2)
Mva 32,208,240
VF 105,27,100,105
TR 196,3,18432
Mva 48,208,244
VF 107,29,102,107
TR 198,3,18944
Mva 64,208,248
v_exp_f32 v152, v152
VF 108,26,104,108
Mva 80,208,252
VF 146,146,13,13
VF 110,28,106,110
s_waitcnt lgkmcnt(2)
Mva 36,192,240
VF 109,26,105,109
TR 200,3,19456
Mva 52,192,244
v_rcp_f32 v144, v144
VF 111,28,107,111
Mva 68,192,248
TR 202,3,19968
VM 155,9,155
v_fma_f32 v108, -v27, v105, v108
Mva 84,192,252
v_exp_f32 v153, v153
s_waitcnt lgkmcnt(2)
Mva 40,196,240
v_fma_f32 v110, -v29, v107, v110
TR 204,2,32768
Mva 56,196,244
VF 147,147,13,13
VF 109,27,104,109
Mva 72,196,248
TR 206,2,33280
VF 111,29,106,111
Mva 88,196,252
v_rcp_f32 v145, v145
VF 112,26,108,112
s_waitcnt lgkmcnt(2)
Mva 44,200,240
VF 114,28,110,114
TR 208,2,33792
Mva 60,200,244
v_exp_f32 v154, v154
VF 113,26,109,113
Mva 76,200,248
TR 210,2,34304
VF 152,152,13,13
VF 115,28,111,115
Mva 92,200,252
v_rcp_f32 v146, v146
s_waitcnt vmcnt(4)
s_barrier
s_waitcnt lgkmcnt(2)
Mvv 32,204,224
s_mov_b32 m0, s69
v_fma_f32 v112, -v27, v109, v112
Mvv 48,204,228
TR 192,3,32768
VM 148,148,144
v_fma_f32 v114, -v29, v111, v114
Mvv 64,204,232
s_mov_b32 s15, 1184000
buffer_load_dwordx4 v1, s[16:19], s15 offen offset:0 lds
TR 194,3,33280
VF 113,27,108,113
Mvv 80,204,236
v_exp_f32 v155, v155
VF 115,29,110,115
s_waitcnt lgkmcnt(2)
Mvv 36,208,224
VF 153,153,13,13
VF 116,26,112,116
Mvv 52,208,228
s_mov_b32 s15, 1182976
buffer_load_dwordx4 v1, s[20:23], s15 offen offset:1024 lds
TR 196,3,33792
VF 118,28,114,118
Mvv 68,208,232
TR 198,3,34304
v_rcp_f32 v147, v147
VF 117,26,113,117
Mvv 84,208,236
VM 149,149,145
VF 154,154,13,13
s_waitcnt lgkmcnt(2)
Mvv 40,192,224
s_mov_b32 s15, 1476864
buffer_load_dwordx4 v1, s[16:19], s15 offen offset:2048 lds
VF 119,28,115,119
Mvv 56,192,228
TR 200,2,34816
v_fma_f32 v116, -v27, v113, v116
Mvv 72,192,232
TR 202,2,35328
v_rcp_f32 v152, v152
v_fma_f32 v118, -v29, v115, v118
Mvv 88,192,236
s_mov_b32 s15, 1475840
buffer_load_dwordx4 v1, s[20:23], s15 offen offset:3072 lds
VM 150,150,146
VF 117,27,112,117
s_waitcnt lgkmcnt(2)
Mvv 44,196,224
VF 155,155,13,13
VF 119,29,114,119
Mvv 60,196,228
TR 204,2,35840
VF 120,26,116,120
Mvv 76,196,232
TR 206,2,36352
v_rcp_f32 v153, v153
VF 122,28,118,122
Mvv 92,196,236
VM 151,151,147
VF 121,26,117,121
s_waitcnt lgkmcnt(2)
Mvv 32,200,240
TR 208,3,34816
VF 123,28,119,123
Mvv 48,200,244
v_rcp_f32 v154, v154
Mvv 64,200,248
TR 210,3,35328
VM 156,156,152
v_fma_f32 v120, -v27, v117, v120
Mvv 80,200,252
v_fma_f32 v122, -v29, v119, v122
VF 121,27,116,121
s_waitcnt lgkmcnt(2)
Mvv 36,204,240
v_rcp_f32 v155, v155
Mvv 52,204,244
)"
R"(TR 192,3,35840
VM 157,157,153
VF 123,29,118,123
Mvv 68,204,248
TR 194,3,36352
VM 158,158,154
VF 212,26,120,124
Mvv 84,204,252
VM 159,159,155
VC 144,148,149
s_waitcnt lgkmcnt(2)
Mvv 40,208,240
VF 214,28,122,126
TR 196,2,49152
Mvv 56,208,244
VC 145,150,151
VF 213,26,121,125
Mvv 72,208,248
TR 198,2,49664
VC 146,156,157
VF 215,28,123,127
Mvv 88,208,252
VC 147,158,159
v_fma_f32 v212, -v27, v121, v212
s_waitcnt lgkmcnt(2)
Mvv 44,192,240
v_fma_f32 v214, -v29, v123, v214
TR 200,2,50176
Mvv 60,192,244
VF 213,27,120,213
TR 202,2,50688
Mvv 76,192,248
ds_write_b128 v5, v[144:147] offset:3072
Mvv 92,192,252
VF 215,29,122,215
TR 204,3,49152
s_waitcnt lgkmcnt(1)
s_waitcnt vmcnt(4)
s_barrier
Zva 96,196,0
s_mov_b32 m0, s70
TR 206,3,49664
Zva 112,196,4
s_mov_b32 s15, 1773824
buffer_load_dwordx4 v1, s[16:19], s15 offen offset:0 lds
Zva 128,196,8
v_and_b32 v30, 15, v0
VM 36,32,36
Zva 144,196,12
VM 37,33,37
TR 208,3,50176
Zva 100,200,0
s_mov_b32 s15, 1772800
buffer_load_dwordx4 v1, s[20:23], s15 offen offset:1024 lds
Zva 116,200,4
VM 38,34,38
v_lshrrev_b32 v31, 1, v30
Zva 132,200,8
TR 210,3,50688
VM 32,6,32
Zva 148,200,12
VM 39,35,39
v_and_b32 v31, 3, v31
s_waitcnt lgkmcnt(2)
Zva 104,204,0
s_mov_b32 s15, 2066688
buffer_load_dwordx4 v1, s[16:19], s15 offen offset:2048 lds
VM 33,6,33
Zva 120,204,4
TR 192,2,51200
VM 44,40,44
Zva 136,204,8
TR 194,2,51712
VM 34,6,34
v_and_b32 v216, 1, v0
Zva 152,204,12
s_mov_b32 s15, 2065664
buffer_load_dwordx4 v1, s[20:23], s15 offen offset:3072 lds
s_waitcnt lgkmcnt(2)
Zva 108,208,0
v_exp_f32 v32, v32
v_lshl_or_b32 v31, v216, 2, v31
Zva 124,208,4
TR 196,2,52224
VM 45,41,45
Zva 140,208,8
TR 198,2,52736
VM 35,6,35
Zva 156,208,12
v_lshrrev_b32 v216, 3, v0
TR 200,3,51200
s_waitcnt lgkmcnt(3)
Mva 96,192,16
v_exp_f32 v33, v33
Mva 112,192,20
VM 46,42,46
v_and_b32 v216, 6, v216
Mva 128,192,24
TR 202,3,51712
VM 40,6,40
Mva 144,192,28
v_exp_f32 v34, v34
s_waitcnt lgkmcnt(2)
Mva 100,196,16
v_xor_b32 v31, v31, v216
TR 204,3,52224
Mva 116,196,20
VF 32,32,10,10
TR 206,3,52736
Mva 132,196,24
VM 47,43,47
v_lshlrev_b32 v31, 4, v31
Mva 148,196,28
VM 41,6,41
TR 208,2,0
s_waitcnt lgkmcnt(3)
Mva 104,200,16
v_exp_f32 v35, v35
Mva 120,200,20
v_lshl_add_u32 v30, v30, 7, v31
TR 210,2,512
Mva 136,200,24
VF 33,33,10,10
s_lshl_b32 s90, s64, 14
s_add_u32 s90, s90, 114688
Mva 152,200,28
VM 42,6,42
TR 192,2,1024
s_waitcnt lgkmcnt(3)
Mva 108,204,16
v_exp_f32 v40, v40
Mva 124,204,20
s_lshl_b32 s91, s65, 13
TR 194,2,1536
s_add_u32 s91, s91, s90
Mva 140,204,24
VF 34,34,10,10
TR 196,3,0
Mva 156,204,28
v_rcp_f32 v32, v32
s_waitcnt vmcnt(4)
s_barrier
s_waitcnt lgkmcnt(3)
Mva 96,208,32
s_mov_b32 m0, s71
VM 43,6,43
Mva 112,208,36
v_add_u32 v30, s91, v30
TR 198,3,512
Mva 128,208,40
s_mov_b32 s15, 2363648
buffer_load_dwordx4 v1, s[16:19], s15 offen offset:0 lds
Mva 144,208,44
v_exp_f32 v41, v41
v_xor_b32 v31, 16, v30
s_waitcnt lgkmcnt(2)
Mva 100,192,32
VF 35,35,10,10
TR 200,3,1024
Mva 116,192,36
s_mov_b32 s15, 2362624
buffer_load_dwordx4 v1, s[20:23], s15 offen offset:1024 lds
Mva 132,192,40
TR 202,3,1536
v_rcp_f32 v33, v33
v_bfe_u32 v216, v0, 2, 2
Mva 148,192,44
s_mov_b32 s15, 2656512
buffer_load_dwordx4 v1, s[16:19], s15 offen offset:2048 lds
s_waitcnt lgkmcnt(2)
Mva 104,196,32
v_exp_f32 v42, v42
v_lshrrev_b32 v217, 1, v216
Mva 120,196,36
TR 204,2,2048
VF 40,40,10,10
Mva 136,196,40
TR 206,2,2560
v_and_b32 v218, 1, v216
Mva 152,196,44
s_mov_b32 s15, 2655488
buffer_load_dwordx4 v1, s[20:23], s15 offen offset:3072 lds
v_rcp_f32 v34, v34
s_waitcnt lgkmcnt(2)
Mva 108,200,32
VM 36,36,32
TR 208,2,3072
Mva 124,200,36
v_lshl_or_b32 v217, v218, 2, v217
TR 210,2,3584
Mva 140,200,40
v_exp_f32 v43, v43
Mva 156,200,44
VF 41,41,10,10
v_and_b32 v218, 3, v0
s_waitcnt lgkmcnt(2)
Mva 96,204,48
TR 192,3,2048
TR 194,3,2560
Mva 112,204,52
v_rcp_f32 v35, v35
Mva 128,204,56
VM 37,37,33
VF 42,42,10,10
Mva 144,204,60
v_lshlrev_b32 v218, 1, v218
TR 196,3,3072
s_waitcnt lgkmcnt(3)
Mva 100,208,48
v_rcp_f32 v40, v40
Mva 116,208,52
v_xor_b32 v217, v217, v218
TR 198,3,3584
Mva 132,208,56
VM 38,38,34
VF 43,43,10,10
Mva 148,208,60
v_lshlrev_b32 v217, 4, v217
TR 200,2,16384
s_waitcnt lgkmcnt(3)
Mva 104,192,48
v_rcp_f32 v41, v41
Mva 120,192,52
TR 202,2,16896
VM 39,39,35
Mva 136,192,56
v_lshl_add_u32 v217, v216, 7, v217
TR 204,2,17408
Mva 152,192,60
v_rcp_f32 v42, v42
s_waitcnt lgkmcnt(3)
Mva 108,196,48
VM 44,44,40
v_lshrrev_b32 v218, 4, v0
Mva 124,196,52
TR 206,2,17920
v_lshl_add_u32 v217, v218, 10, v217
Mva 140,196,56
v_rcp_f32 v43, v43
Mva 156,196,60
VM 45,45,41
TR 208,3,16384
s_waitcnt vmcnt(4)
s_barrier
s_waitcnt lgkmcnt(3)
Mva 96,200,64
s_mov_b32 m0, s68
VM 46,46,42
Mva 112,200,68
s_mov_b32 s15, 2953472
buffer_load_dwordx4 v1, s[16:19], s15 offen offset:0 lds
Mva 128,200,72
TR 210,3,16896
VM 47,47,43
v_add_u32 v216, s90, v217
Mva 144,200,76
VC 32,36,37
VC 33,38,39
s_waitcnt lgkmcnt(2)
Mva 100,204,64
v_xor_b32 v217, 16, v216
TR 192,3,17408
Mva 116,204,68
s_mov_b32 s15, 2952448
buffer_load_dwordx4 v1, s[20:23], s15 offen offset:1024 lds
VC 34,44,45
Mva 132,204,72
TR 194,3,17920
VC 35,46,47
Mva 148,204,76
v_xor_b32 v218, 32, v216
TR 196,2,18432
s_waitcnt lgkmcnt(3)
Mva 104,208,64
s_mov_b32 s15, 3246336
buffer_load_dwordx4 v1, s[16:19], s15 offen offset:2048 lds
Mva 120,208,68
ds_write_b128 v5, v[32:35] offset:16384
Mva 136,208,72
TR 198,2,18944
VM 52,48,52
v_xor_b32 v219, 48, v216
Mva 152,208,76
s_mov_b32 s15, 3245312
buffer_load_dwordx4 v1, s[20:23], s15 offen offset:3072 lds
VM 53,49,53
v_lshlrev_b32 v32, 16, v160
s_waitcnt lgkmcnt(3)
Mva 108,192,64
VM 54,50,54
TR 200,2,19456
Mva 124,192,68
VM 48,7,48
v_and_b32 v40, 0xffff0000, v160
Mva 140,192,72
TR 202,2,19968
VM 55,51,55
Mva 156,192,76
VM 49,7,49
VF 32,22,212,32
s_waitcnt lgkmcnt(2)
Mva 96,196,80
VM 60,56,60
VF 40,22,213,40
Mva 112,196,84
TR 204,3,18432
VM 50,7,50
Mva 128,196,88
TR 206,3,18944
v_fma_f32 v32, -v23, v213, v32
Mva 144,196,92
v_exp_f32 v48, v48
s_waitcnt lgkmcnt(2)
Mva 100,200,80
VM 61,57,61
VF 40,23,212,40
Mva 116,200,84
TR 208,3,19456
VM 51,7,51
Mva 132,200,88
TR 210,3,19968
v_lshlrev_b32 v33, 16, v161
Mva 148,200,92
v_exp_f32 v49, v49
v_and_b32 v41, 0xffff0000, v161
s_waitcnt lgkmcnt(2)
Mva 104,204,80
VM 62,58,62
TR 192,2,32768
Mva 120,204,84
VM 56,7,56
VF 33,22,32,33
Mva 136,204,88
TR 194,2,33280
VF 41,22,40,41
Mva 152,204,92
v_exp_f32 v50, v50
s_waitcnt lgkmcnt(2)
Mva 108,208,80
VF 48,48,11,11
v_fma_f32 v33, -v23, v40, v33
Mva 124,208,84
TR 196,2,33792
VM 63,59,63
Mva 140,208,88
TR 198,2,34304
VM 57,7,57
VF 41,23,32,41
Mva 156,208,92
v_exp_f32 v51, v51
s_waitcnt vmcnt(4)
s_barrier
s_waitcnt lgkmcnt(2)
Mva 96,192,96
s_mov_b32 m0, s69
v_lshlrev_b32 v34, 16, v162
Mva 112,192,100
TR 200,3,32768
VF 49,49,11,11
Mva 128,192,104
s_mov_b32 s15, 3543296
buffer_load_dwordx4 v1, s[16:19], s15 offen offset:0 lds
TR 202,3,33280
VM 58,7,58
v_and_b32 v42, 0xffff0000, v162
Mva 144,192,108
VF 34,22,33,34
TR 204,3,33792
s_waitcnt lgkmcnt(3)
Mva 100,196,96
v_exp_f32 v56, v56
Mva 116,196,100
s_mov_b32 s15, 3542272
buffer_load_dwordx4 v1, s[20:23], s15 offen offset:1024 lds
VF 50,50,11,11
VF 42,22,41,42
Mva 132,196,104
TR 206,3,34304
v_fma_f32 v34, -v23, v41, v34
Mva 148,196,108
v_rcp_f32 v48, v48
s_waitcnt lgkmcnt(2)
Mva 104,200,96
s_mov_b32 s15, 3836160
buffer_load_dwordx4 v1, s[16:19], s15 offen offset:2048 lds
VM 59,7,59
Mva 120,200,100
TR 208,2,34816
VF 42,23,33,42
Mva 136,200,104
TR 210,2,35328
v_exp_f32 v57, v57
v_lshlrev_b32 v35, 16, v163
Mva 152,200,108
s_mov_b32 s15, 3835136
buffer_load_dwordx4 v1, s[20:23], s15 offen offset:3072 lds
VF 51,51,11,11
s_waitcnt lgkmcnt(2)
Mva 108,204,96
v_and_b32 v43, 0xffff0000, v163
TR 192,2,35840
Mva 124,204,100
v_rcp_f32 v49, v49
Mva 140,204,104
TR 194,2,36352
VF 35,22,34,35
Mva 156,204,108
v_exp_f32 v58, v58
VF 43,22,42,43
s_waitcnt lgkmcnt(2)
Mva 96,208,112
VF 56,56,11,11
TR 196,3,34816
Mva 112,208,116
v_fma_f32 v35, -v23, v42, v35
TR 198,3,35328
Mva 128,208,120
v_rcp_f32 v50, v50
Mva 144,208,124
VM 52,52,48
VF 43,23,34,43
s_waitcnt lgkmcnt(2)
Mva 100,192,112
TR 200,3,35840
v_lshlrev_b32 v36, 16, v164
Mva 116,192,116
v_exp_f32 v59, v59
Mva 132,192,120
TR 202,3,36352
VF 57,57,11,11
v_and_b32 v44, 0xffff0000, v164
Mva 148,192,124
v_rcp_f32 v51, v51
s_waitcnt lgkmcnt(2)
Mva 104,196,112
VF 36,22,35,36
TR 204,2,49152
Mva 120,196,116
VM 53,53,49
TR 206,2,49664
Mva 136,196,120
VF 58,58,11,11
VF 44,22,43,44
Mva 152,196,124
v_rcp_f32 v56, v56
s_waitcnt lgkmcnt(2)
Mva 108,200,112
v_fma_f32 v36, -v23, v43, v36
TR 208,2,50176
Mva 124,200,116
VM 54,54,50
VF 44,23,35,44
Mva 140,200,120
TR 210,2,50688
VF 59,59,11,11
Mva 156,200,124
v_lshlrev_b32 v37, 16, v165
TR 192,3,49152
s_waitcnt vmcnt(4)
s_barrier
s_waitcnt lgkmcnt(3)
Mva 96,204,128
s_mov_b32 m0, s70
v_rcp_f32 v57, v57
Mva 112,204,132
VM 55,55,51
v_and_b32 v45, 0xffff0000, v165
Mva 128,204,136
s_mov_b32 s15, 4133120
buffer_load_dwordx4 v1, s[16:19], s15 offen offset:0 lds
TR 194,3,49664
VF 37,22,36,37
Mva 144,204,140
v_rcp_f32 v58, v58
s_waitcnt lgkmcnt(2)
Mva 100,208,128
VM 60,60,56
VF 45,22,44,45
Mva 116,208,132
s_mov_b32 s15, 4132096
buffer_load_dwordx4 v1, s[20:23], s15 offen offset:1024 lds
TR 196,3,50176
Mva 132,208,136
TR 198,3,50688
v_rcp_f32 v59, v59
v_fma_f32 v37, -v23, v44, v37
Mva 148,208,140
VM 61,61,57
VM 62,62,58
s_waitcnt lgkmcnt(2)
Mva 104,192,128
s_mov_b32 s15, 4425984
buffer_load_dwordx4 v1, s[16:19], s15 offen offset:2048 lds
VF 45,23,36,45
Mva 120,192,132
TR 200,2,51200
VM 63,63,59
v_lshlrev_b32 v38, 16, v166
Mva 136,192,136
TR 202,2,51712
VC 48,52,53
Mva 152,192,140
s_mov_b32 s15, 4424960
buffer_load_dwordx4 v1, s[20:23], s15 offen offset:3072 lds
VC 49,54,55
v_and_b32 v46, 0xffff0000, v166
s_waitcnt lgkmcnt(2)
Mva 108,196,128
VC 50,60,61
TR 204,2,52224
Mva 124,196,132
VC 51,62,63
VF 38,22,37,38
Mva 140,196,136
TR 206,2,52736
VF 46,22,45,46
Mva 156,196,140
ds_write_b128 v5, v[48:51] offset:17408
s_waitcnt lgkmcnt(3)
Mva 96,200,144
v_fma_f32 v38, -v23, v45, v38
TR 208,3,51200
Mva 112,200,148
VM 68,64,68
TR 210,3,51712
Mva 128,200,152
VM 69,65,69
VF 46,23,37,46
Mva 144,200,156
VM 70,66,70
VM 64,8,64
s_waitcnt lgkmcnt(3)
Mva 100,204,144
v_lshlrev_b32 v39, 16, v167
TR 192,3,52224
Mva 116,204,148
VM 71,67,71
v_and_b32 v47, 0xffff0000, v167
Mva 132,204,152
TR 194,3,52736
VM 65,8,65
Mva 148,204,156
VM 76,72,76
VF 39,22,38,39
s_waitcnt lgkmcnt(2)
Mva 104,208,144
VM 66,8,66
TR 196,2,0
Mva 120,208,148
VF 47,22,46,47
TR 198,2,512
Mva 136,208,152
v_exp_f32 v64, v64
Mva 152,208,156
VM 77,73,77
v_fma_f32 v39, -v23, v46, v39
s_waitcnt lgkmcnt(2)
Mva 108,192,144
VM 67,8,67
VF 47,23,38,47
Mva 124,192,148
TR 200,2,1024
TR 202,2,1536
Mva 140,192,152
v_exp_f32 v65, v65
VC 32,32,33
Mva 156,192,156
VM 78,74,78
VM 72,8,72
s_waitcnt vmcnt(4)
s_barrier
s_waitcnt lgkmcnt(2)
Mva 96,196,160
s_mov_b32 m0, s71
VC 33,34,35
Mva 112,196,164
TR 204,3,0
TR 206,3,512
Mva 128,196,168
s_mov_b32 s15, 4722944
buffer_load_dwordx4 v1, s[16:19], s15 offen offset:0 lds
v_exp_f32 v66, v66
VC 34,36,37
Mva 144,196,172
VF 64,64,12,12
VC 35,38,39
s_waitcnt lgkmcnt(2)
Mva 100,200,160
VM 79,75,79
TR 208,3,1024
Mva 116,200,164
s_mov_b32 s15, 4721920
buffer_load_dwordx4 v1, s[20:23], s15 offen offset:1024 lds
VM 73,8,73
Mva 132,200,168
TR 210,3,1536
Mva 148,200,172
v_exp_f32 v67, v67
s_waitcnt lgkmcnt(2)
Mva 104,204,160
)"
R"(s_mov_b32 s15, 5015808
buffer_load_dwordx4 v1, s[16:19], s15 offen offset:2048 lds
VF 65,65,12,12
ds_write_b128 v30, v[32:35] offset:0
Mva 120,204,164
TR 192,2,2048
VM 74,8,74
Mva 136,204,168
TR 194,2,2560
VC 40,40,41
Mva 152,204,172
s_mov_b32 s15, 5014784
buffer_load_dwordx4 v1, s[20:23], s15 offen offset:3072 lds
v_exp_f32 v72, v72
s_waitcnt lgkmcnt(3)
Mva 108,208,160
VF 66,66,12,12
VC 41,42,43
Mva 124,208,164
TR 196,2,3072
TR 198,2,3584
Mva 140,208,168
v_rcp_f32 v64, v64
VC 42,44,45
Mva 156,208,172
VM 75,8,75
VC 43,46,47
s_waitcnt lgkmcnt(2)
Mva 96,192,176
TR 200,3,2048
TR 202,3,2560
Mva 112,192,180
v_exp_f32 v73, v73
Mva 128,192,184
VF 67,67,12,12
TR 204,3,3072
Mva 144,192,188
v_rcp_f32 v65, v65
s_waitcnt lgkmcnt(3)
Mva 100,196,176
ds_write_b128 v30, v[40:43] offset:4096
v_lshlrev_b32 v48, 16, v168
v_and_b32 v56, 0xffff0000, v168
VF 48,22,39,48
VF 56,22,47,56
v_fma_f32 v48, -v23, v47, v48
VF 56,23,39,56
v_lshlrev_b32 v49, 16, v169
v_and_b32 v57, 0xffff0000, v169
VF 49,22,48,49
VF 57,22,56,57
v_fma_f32 v49, -v23, v56, v49
VF 57,23,48,57
v_lshlrev_b32 v50, 16, v170
v_and_b32 v58, 0xffff0000, v170
VF 50,22,49,50
Mva 116,196,180
VF 58,22,57,58
TR 206,3,3584
Mva 132,196,184
v_exp_f32 v74, v74
Mva 148,196,188
VF 72,72,12,12
v_fma_f32 v50, -v23, v57, v50
s_waitcnt lgkmcnt(3)
Mva 104,200,176
TR 208,2,16384
TR 210,2,16896
Mva 120,200,180
v_rcp_f32 v66, v66
Mva 136,200,184
VM 68,68,64
VF 58,23,49,58
Mva 152,200,188
v_exp_f32 v75, v75
s_waitcnt lgkmcnt(2)
Mva 108,204,176
v_lshlrev_b32 v51, 16, v171
TR 192,2,17408
Mva 124,204,180
VF 73,73,12,12
TR 194,2,17920
Mva 140,204,184
v_and_b32 v59, 0xffff0000, v171
TR 196,3,16384
Mva 156,204,188
v_rcp_f32 v67, v67
s_waitcnt vmcnt(4)
s_barrier
s_waitcnt lgkmcnt(3)
Mva 96,208,192
s_mov_b32 m0, s68
VM 69,69,65
Mva 112,208,196
VF 74,74,12,12
VF 51,22,50,51
Mva 128,208,200
s_mov_b32 s15, 4480
buffer_load_dwordx4 v1, s[16:19], s15 offen offset:0 lds
TR 198,3,16896
Mva 144,208,204
v_rcp_f32 v72, v72
VF 59,22,58,59
s_waitcnt lgkmcnt(2)
Mva 100,192,192
VM 70,70,66
TR 200,3,17408
Mva 116,192,196
s_mov_b32 s15, 3456
buffer_load_dwordx4 v1, s[20:23], s15 offen offset:1024 lds
VF 75,75,12,12
v_fma_f32 v51, -v23, v58, v51
Mva 132,192,200
TR 202,3,17920
VF 59,23,50,59
Mva 148,192,204
v_rcp_f32 v73, v73
s_waitcnt lgkmcnt(2)
Mva 104,196,192
s_mov_b32 s15, 297344
buffer_load_dwordx4 v1, s[16:19], s15 offen offset:2048 lds
VM 71,71,67
Mva 120,196,196
TR 204,2,18432
TR 206,2,18944
Mva 136,196,200
v_rcp_f32 v74, v74
v_lshlrev_b32 v52, 16, v172
Mva 152,196,204
s_mov_b32 s15, 296320
buffer_load_dwordx4 v1, s[20:23], s15 offen offset:3072 lds
VM 76,76,72
s_waitcnt lgkmcnt(2)
Mva 108,200,192
v_and_b32 v60, 0xffff0000, v172
TR 208,2,19456
Mva 124,200,196
v_rcp_f32 v75, v75
Mva 140,200,200
TR 210,2,19968
VM 77,77,73
VF 52,22,51,52
Mva 156,200,204
VM 78,78,74
VM 79,79,75
s_waitcnt lgkmcnt(2)
Mva 96,204,208
TR 192,3,18432
VC 64,68,69
Mva 112,204,212
VF 60,22,59,60
TR 194,3,18944
Mva 128,204,216
VC 65,70,71
VC 66,76,77
Mva 144,204,220
v_fma_f32 v52, -v23, v59, v52
VC 67,78,79
s_waitcnt lgkmcnt(2)
Mva 100,208,208
TR 196,3,19456
VF 60,23,51,60
Mva 116,208,212
TR 198,3,19968
v_lshlrev_b32 v53, 16, v173
Mva 132,208,216
ds_write_b128 v5, v[64:67] offset:18432
Mva 148,208,220
VM 84,80,84
TR 200,2,32768
s_waitcnt lgkmcnt(4)
Mva 104,192,208
VM 85,81,85
TR 202,2,33280
Mva 120,192,212
VM 86,82,86
v_and_b32 v61, 0xffff0000, v173
Mva 136,192,216
VM 80,9,80
VM 87,83,87
Mva 152,192,220
VF 53,22,52,53
TR 204,2,33792
s_waitcnt lgkmcnt(4)
Mva 108,196,208
VM 81,9,81
TR 206,2,34304
Mva 124,196,212
VM 92,88,92
VF 61,22,60,61
Mva 140,196,216
VM 82,9,82
TR 208,3,32768
Mva 156,196,220
v_exp_f32 v80, v80
s_waitcnt vmcnt(4)
s_barrier
s_waitcnt lgkmcnt(3)
Mva 96,200,224
s_mov_b32 m0, s69
v_fma_f32 v53, -v23, v60, v53
Mva 112,200,228
s_mov_b32 s15, 594304
buffer_load_dwordx4 v1, s[16:19], s15 offen offset:0 lds
Mva 128,200,232
TR 210,3,33280
VM 93,89,93
Mva 144,200,236
VM 83,9,83
VF 61,23,52,61
s_waitcnt lgkmcnt(2)
Mva 100,204,224
s_mov_b32 s15, 593280
buffer_load_dwordx4 v1, s[20:23], s15 offen offset:1024 lds
Mva 116,204,228
TR 192,3,33792
v_exp_f32 v81, v81
v_lshlrev_b32 v54, 16, v174
Mva 132,204,232
TR 194,3,34304
VM 94,90,94
Mva 148,204,236
VM 88,9,88
v_and_b32 v62, 0xffff0000, v174
s_waitcnt lgkmcnt(2)
Mva 104,208,224
s_mov_b32 s15, 887168
buffer_load_dwordx4 v1, s[16:19], s15 offen offset:2048 lds
Mva 120,208,228
TR 196,2,34816
v_exp_f32 v82, v82
Mva 136,208,232
TR 198,2,35328
VF 80,80,13,13
VF 54,22,53,54
Mva 152,208,236
s_mov_b32 s15, 886144
buffer_load_dwordx4 v1, s[20:23], s15 offen offset:3072 lds
VM 95,91,95
s_waitcnt lgkmcnt(2)
Mva 108,192,224
VM 89,9,89
VF 62,22,61,62
Mva 124,192,228
TR 200,2,35840
TR 202,2,36352
Mva 140,192,232
v_exp_f32 v83, v83
v_fma_f32 v54, -v23, v61, v54
Mva 156,192,236
VF 81,81,13,13
VM 90,9,90
s_waitcnt lgkmcnt(2)
Mva 96,196,240
TR 204,3,34816
VF 62,23,53,62
Mva 112,196,244
TR 206,3,35328
v_lshlrev_b32 v55, 16, v175
Mva 128,196,248
v_exp_f32 v88, v88
Mva 144,196,252
VF 82,82,13,13
TR 208,3,35840
s_waitcnt lgkmcnt(3)
Mva 100,200,240
v_rcp_f32 v80, v80
Mva 116,200,244
v_and_b32 v63, 0xffff0000, v175
TR 210,3,36352
Mva 132,200,248
VM 91,9,91
VF 55,22,54,55
Mva 148,200,252
v_exp_f32 v89, v89
s_waitcnt lgkmcnt(2)
Mva 104,204,240
TR 192,2,49152
VF 83,83,13,13
Mva 120,204,244
TR 194,2,49664
VF 63,22,62,63
Mva 136,204,248
v_rcp_f32 v81, v81
Mva 152,204,252
v_fma_f32 v55, -v23, v62, v55
TR 196,2,50176
s_waitcnt lgkmcnt(3)
Mva 108,208,240
v_exp_f32 v90, v90
Mva 124,208,244
TR 198,2,50688
VF 88,88,13,13
Mva 140,208,248
VF 63,23,54,63
TR 200,3,49152
Mva 156,208,252
v_rcp_f32 v82, v82
s_waitcnt vmcnt(4)
s_barrier
s_waitcnt lgkmcnt(3)
Mvv 96,192,224
s_mov_b32 m0, s70
VM 84,84,80
Mvv 112,192,228
VC 48,48,49
TR 202,3,49664
Mvv 128,192,232
s_mov_b32 s15, 1184128
buffer_load_dwordx4 v1, s[16:19], s15 offen offset:0 lds
Mvv 144,192,236
v_exp_f32 v91, v91
VC 49,50,51
s_waitcnt lgkmcnt(2)
Mvv 100,196,224
VF 89,89,13,13
TR 204,3,50176
Mvv 116,196,228
s_mov_b32 s15, 1183104
buffer_load_dwordx4 v1, s[20:23], s15 offen offset:1024 lds
VC 50,52,53
Mvv 132,196,232
TR 206,3,50688
v_rcp_f32 v83, v83
Mvv 148,196,236
VM 85,85,81
VF 90,90,13,13
s_waitcnt lgkmcnt(2)
Mvv 104,200,224
s_mov_b32 s15, 1476992
buffer_load_dwordx4 v1, s[16:19], s15 offen offset:2048 lds
VC 51,54,55
Mvv 120,200,228
TR 208,2,51200
TR 210,2,51712
Mvv 136,200,232
v_rcp_f32 v88, v88
Mvv 152,200,236
s_mov_b32 s15, 1475968
buffer_load_dwordx4 v1, s[20:23], s15 offen offset:3072 lds
VM 86,86,82
s_waitcnt lgkmcnt(2)
Mvv 108,204,224
VF 91,91,13,13
TR 192,2,52224
Mvv 124,204,228
TR 194,2,52736
Mvv 140,204,232
v_rcp_f32 v89, v89
ds_write_b128 v31, v[48:51] offset:0
Mvv 156,204,236
VM 87,87,83
TR 196,3,51200
s_waitcnt lgkmcnt(4)
Mvv 96,208,240
v_rcp_f32 v90, v90
Mvv 112,208,244
VC 56,56,57
TR 198,3,51712
Mvv 128,208,248
VM 92,92,88
VC 57,58,59
Mvv 144,208,252
v_rcp_f32 v91, v91
s_waitcnt lgkmcnt(3)
Mvv 100,192,240
TR 200,3,52224
VM 93,93,89
Mvv 116,192,244
VC 58,60,61
TR 202,3,52736
Mvv 132,192,248
VM 94,94,90
VM 95,95,91
Mvv 148,192,252
VC 80,84,85
VC 59,62,63
s_waitcnt lgkmcnt(2)
Mvv 104,196,240
TR 204,2,0
VC 81,86,87
Mvv 120,196,244
TR 206,2,512
VC 82,92,93
Mvv 136,196,248
VC 83,94,95
TR 208,2,1024
Mvv 152,196,252
TR 210,2,1536
s_waitcnt lgkmcnt(4)
Mvv 108,200,240
ds_write_b128 v5, v[80:83] offset:19456
Mvv 124,200,244
ds_write_b128 v31, v[56:59] offset:4096
Mvv 140,200,248
TR 192,3,0
Mvv 156,200,252
TR 194,3,512
s_waitcnt lgkmcnt(3)
s_waitcnt vmcnt(4)
s_barrier
Zva 32,204,0
s_mov_b32 m0, s71
TR 196,3,1024
Zva 48,204,4
s_mov_b32 s15, 1773952
buffer_load_dwordx4 v1, s[16:19], s15 offen offset:0 lds
Zva 64,204,8
VM 100,96,100
VM 101,97,101
Zva 80,204,12
VM 102,98,102
TR 198,3,1536
Zva 36,208,0
s_mov_b32 s15, 1772928
buffer_load_dwordx4 v1, s[20:23], s15 offen offset:1024 lds
Zva 52,208,4
VM 96,6,96
VM 103,99,103
Zva 68,208,8
VM 97,6,97
TR 200,2,2048
Zva 84,208,12
s_mov_b32 s15, 2066816
buffer_load_dwordx4 v1, s[16:19], s15 offen offset:2048 lds
s_waitcnt lgkmcnt(3)
Zva 40,192,0
VM 108,104,108
TR 202,2,2560
Zva 56,192,4
VM 98,6,98
TR 204,2,3072
Zva 72,192,8
s_mov_b32 s15, 2065792
buffer_load_dwordx4 v1, s[20:23], s15 offen offset:3072 lds
Zva 88,192,12
v_exp_f32 v96, v96
s_waitcnt lgkmcnt(3)
Zva 44,196,0
VM 109,105,109
TR 206,2,3584
Zva 60,196,4
VM 99,6,99
TR 208,3,2048
Zva 76,196,8
v_exp_f32 v97, v97
Zva 92,196,12
VM 110,106,110
TR 210,3,2560
s_waitcnt lgkmcnt(4)
Mva 32,200,16
VM 104,6,104
TR 192,3,3072
Mva 48,200,20
v_exp_f32 v98, v98
Mva 64,200,24
VF 96,96,10,10
TR 194,3,3584
Mva 80,200,28
VM 111,107,111
VM 105,6,105
s_waitcnt lgkmcnt(4)
Mva 36,204,16
TR 196,2,16384
Mva 52,204,20
v_exp_f32 v99, v99
Mva 68,204,24
TR 198,2,16896
VF 97,97,10,10
Mva 84,204,28
VM 106,6,106
TR 200,2,17408
s_waitcnt lgkmcnt(5)
Mva 40,208,16
v_exp_f32 v104, v104
Mva 56,208,20
TR 202,2,17920
VF 98,98,10,10
Mva 72,208,24
v_rcp_f32 v96, v96
Mva 88,208,28
TR 204,3,16384
VM 107,6,107
s_waitcnt lgkmcnt(5)
Mva 44,192,16
TR 206,3,16896
Mva 60,192,20
v_exp_f32 v105, v105
Mva 76,192,24
VF 99,99,10,10
TR 208,3,17408
Mva 92,192,28
TR 210,3,17920
s_waitcnt vmcnt(4)
s_barrier
s_waitcnt lgkmcnt(6)
Mva 32,196,32
s_mov_b32 m0, s68
ds_read_b128 a[0:3], v4 offset:0
Mva 48,196,36
s_mov_b32 s15, 2363776
buffer_load_dwordx4 v1, s[16:19], s15 offen offset:0 lds
Mva 64,196,40
v_rcp_f32 v97, v97
Mva 80,196,44
ds_read_b128 a[4:7], v4 offset:1024
s_waitcnt lgkmcnt(6)
Mva 36,200,32
s_mov_b32 s15, 2362752
buffer_load_dwordx4 v1, s[20:23], s15 offen offset:1024 lds
Mva 52,200,36
ds_read_b128 a[8:11], v4 offset:2048
ds_read_b128 a[12:15], v4 offset:3072
Mva 68,200,40
v_exp_f32 v106, v106
Mva 84,200,44
s_mov_b32 s15, 2656640
buffer_load_dwordx4 v1, s[16:19], s15 offen offset:2048 lds
s_waitcnt lgkmcnt(6)
Mva 40,204,32
TR 192,2,18432
VF 104,104,10,10
Mva 56,204,36
TR 194,2,18944
Mva 72,204,40
s_mov_b32 s15, 2655616
buffer_load_dwordx4 v1, s[20:23], s15 offen offset:3072 lds
Mva 88,204,44
v_rcp_f32 v98, v98
s_waitcnt lgkmcnt(6)
Mva 44,208,32
VM 100,100,96
TR 196,2,19456
Mva 60,208,36
TR 198,2,19968
Mva 76,208,40
v_exp_f32 v107, v107
Mva 92,208,44
VF 105,105,10,10
TR 200,3,18432
s_waitcnt lgkmcnt(3)
Mva 32,192,48
v_rcp_f32 v99, v99
Mva 48,192,52
TR 202,3,18944
VM 101,101,97
Mva 64,192,56
VF 106,106,10,10
TR 204,3,19456
Mva 80,192,60
v_rcp_f32 v104, v104
s_waitcnt lgkmcnt(3)
Mva 36,196,48
TR 206,3,19968
VM 102,102,98
Mva 52,196,52
VF 107,107,10,10
ds_read_b128 a[16:19], v4 offset:4096
Mva 68,196,56
ds_read_b128 a[20:23], v4 offset:5120
ds_read_b128 a[24:27], v4 offset:6144
Mva 84,196,60
ds_read_b128 a[28:31], v4 offset:7168
TR 208,2,32768
s_waitcnt lgkmcnt(7)
Mva 40,200,48
v_rcp_f32 v105, v105
Mva 56,200,52
TR 210,2,33280
VM 103,103,99
Mva 72,200,56
v_rcp_f32 v106, v106
Mva 88,200,60
TR 192,2,33792
VM 108,108,104
s_waitcnt lgkmcnt(7)
Mva 44,204,48
TR 194,2,34304
Mva 60,204,52
v_rcp_f32 v107, v107
Mva 76,204,56
VM 109,109,105
TR 196,3,32768
Mva 92,204,60
VM 110,110,106
TR 198,3,33280
s_waitcnt vmcnt(4)
s_barrier
s_waitcnt lgkmcnt(4)
Mva 32,208,64
s_mov_b32 m0, s69
VM 111,111,107
Mva 48,208,68
s_mov_b32 s15, 2953600
buffer_load_dwordx4 v1, s[16:19], s15 offen offset:0 lds
Mva 64,208,72
VC 96,100,101
VC 97,102,103
Mva 80,208,76
TR 200,3,33792
)"
R"(VC 98,108,109
s_waitcnt lgkmcnt(3)
Mva 36,192,64
s_mov_b32 s15, 2952576
buffer_load_dwordx4 v1, s[20:23], s15 offen offset:1024 lds
Mva 52,192,68
TR 202,3,34304
VC 99,110,111
Mva 68,192,72
ds_read_b128 a[32:35], v4 offset:16384
ds_read_b128 a[36:39], v4 offset:17408
Mva 84,192,76
ds_read_b128 a[40:43], v4 offset:18432
ds_read_b128 a[44:47], v4 offset:19456
s_waitcnt lgkmcnt(6)
Mva 40,196,64
s_mov_b32 s15, 3246464
buffer_load_dwordx4 v1, s[16:19], s15 offen offset:2048 lds
Mva 56,196,68
TR 204,2,34816
ds_write_b128 v5, v[96:99] offset:32768
Mva 72,196,72
TR 206,2,35328
VM 116,112,116
Mva 88,196,76
s_mov_b32 s15, 3245440
buffer_load_dwordx4 v1, s[20:23], s15 offen offset:3072 lds
VM 117,113,117
v_lshlrev_b32 v96, 16, v176
s_waitcnt lgkmcnt(7)
Mva 44,200,64
VM 118,114,118
TR 208,2,35840
Mva 60,200,68
VM 112,7,112
v_and_b32 v104, 0xffff0000, v176
Mva 76,200,72
TR 210,2,36352
VM 119,115,119
Mva 92,200,76
VM 113,7,113
VF 96,24,214,96
s_waitcnt lgkmcnt(2)
Mva 32,204,80
VM 124,120,124
VF 104,24,215,104
Mva 48,204,84
TR 192,3,34816
VM 114,7,114
Mva 64,204,88
TR 194,3,35328
v_fma_f32 v96, -v25, v215, v96
Mva 80,204,92
v_exp_f32 v112, v112
s_waitcnt lgkmcnt(2)
Mva 36,208,80
VM 125,121,125
VF 104,25,214,104
Mva 52,208,84
TR 196,3,35840
VM 115,7,115
Mva 68,208,88
TR 198,3,36352
v_lshlrev_b32 v97, 16, v177
Mva 84,208,92
v_exp_f32 v113, v113
ds_read_b128 a[48:51], v4 offset:20480
ds_read_b128 a[52:55], v4 offset:21504
ds_read_b128 a[56:59], v4 offset:22528
ds_read_b128 a[60:63], v4 offset:23552
v_and_b32 v105, 0xffff0000, v177
s_waitcnt lgkmcnt(6)
Mva 40,192,80
VM 126,122,126
TR 200,2,49152
Mva 56,192,84
VM 120,7,120
VF 97,24,96,97
Mva 72,192,88
TR 202,2,49664
VF 105,24,104,105
Mva 88,192,92
v_exp_f32 v114, v114
s_waitcnt lgkmcnt(6)
Mva 44,196,80
VF 112,112,11,11
v_fma_f32 v97, -v25, v104, v97
Mva 60,196,84
TR 204,2,50176
VM 127,123,127
Mva 76,196,88
TR 206,2,50688
VM 121,7,121
VF 105,25,96,105
Mva 92,196,92
v_exp_f32 v115, v115
s_waitcnt vmcnt(4)
s_barrier
s_waitcnt lgkmcnt(2)
Mva 32,200,96
s_mov_b32 m0, s70
v_lshlrev_b32 v98, 16, v178
Mva 48,200,100
TR 208,3,49152
VF 113,113,11,11
Mva 64,200,104
s_mov_b32 s15, 3543424
buffer_load_dwordx4 v1, s[16:19], s15 offen offset:0 lds
TR 210,3,49664
VM 122,7,122
v_and_b32 v106, 0xffff0000, v178
Mva 80,200,108
VF 98,24,97,98
TR 192,3,50176
s_waitcnt lgkmcnt(3)
Mva 36,204,96
v_exp_f32 v120, v120
Mva 52,204,100
s_mov_b32 s15, 3542400
buffer_load_dwordx4 v1, s[20:23], s15 offen offset:1024 lds
VF 114,114,11,11
VF 106,24,105,106
Mva 68,204,104
TR 194,3,50688
v_fma_f32 v98, -v25, v105, v98
Mva 84,204,108
v_rcp_f32 v112, v112
s_waitcnt lgkmcnt(2)
Mva 40,208,96
s_mov_b32 s15, 3836288
buffer_load_dwordx4 v1, s[16:19], s15 offen offset:2048 lds
VM 123,7,123
Mva 56,208,100
TR 196,2,51200
VF 106,25,97,106
Mva 72,208,104
TR 198,2,51712
v_exp_f32 v121, v121
v_lshlrev_b32 v99, 16, v179
Mva 88,208,108
s_mov_b32 s15, 3835264
buffer_load_dwordx4 v1, s[20:23], s15 offen offset:3072 lds
VF 115,115,11,11
s_waitcnt lgkmcnt(2)
Mva 44,192,96
v_and_b32 v107, 0xffff0000, v179
TR 200,2,52224
Mva 60,192,100
v_rcp_f32 v113, v113
Mva 76,192,104
TR 202,2,52736
VF 99,24,98,99
Mva 92,192,108
v_exp_f32 v122, v122
VF 107,24,106,107
s_waitcnt lgkmcnt(2)
Mva 32,196,112
VF 120,120,11,11
TR 204,3,51200
Mva 48,196,116
v_fma_f32 v99, -v25, v106, v99
TR 206,3,51712
Mva 64,196,120
v_rcp_f32 v114, v114
Mva 80,196,124
VM 116,116,112
VF 107,25,98,107
s_waitcnt lgkmcnt(2)
Mva 36,200,112
TR 208,3,52224
v_lshlrev_b32 v100, 16, v180
Mva 52,200,116
v_exp_f32 v123, v123
Mva 68,200,120
TR 210,3,52736
VF 121,121,11,11
v_and_b32 v108, 0xffff0000, v180
Mva 84,200,124
v_rcp_f32 v115, v115
s_waitcnt lgkmcnt(2)
Mva 40,204,112
VF 100,24,99,100
TR 192,2,0
Mva 56,204,116
VM 117,117,113
TR 194,2,512
Mva 72,204,120
VF 122,122,11,11
VF 108,24,107,108
Mva 88,204,124
v_rcp_f32 v120, v120
s_waitcnt lgkmcnt(2)
Mva 44,208,112
v_fma_f32 v100, -v25, v107, v100
TR 196,2,1024
Mva 60,208,116
VM 118,118,114
VF 108,25,99,108
Mva 76,208,120
TR 198,2,1536
VF 123,123,11,11
Mva 92,208,124
v_lshlrev_b32 v101, 16, v181
TR 200,3,0
s_waitcnt vmcnt(4)
s_barrier
s_waitcnt lgkmcnt(3)
Mva 32,192,128
s_mov_b32 m0, s71
v_rcp_f32 v121, v121
Mva 48,192,132
VM 119,119,115
v_and_b32 v109, 0xffff0000, v181
Mva 64,192,136
s_mov_b32 s15, 4133248
buffer_load_dwordx4 v1, s[16:19], s15 offen offset:0 lds
TR 202,3,512
VF 101,24,100,101
Mva 80,192,140
v_rcp_f32 v122, v122
s_waitcnt lgkmcnt(2)
Mva 36,196,128
VM 124,124,120
VF 109,24,108,109
Mva 52,196,132
s_mov_b32 s15, 4132224
buffer_load_dwordx4 v1, s[20:23], s15 offen offset:1024 lds
TR 204,3,1024
Mva 68,196,136
TR 206,3,1536
v_rcp_f32 v123, v123
v_fma_f32 v101, -v25, v108, v101
Mva 84,196,140
VM 125,125,121
VM 126,126,122
s_waitcnt lgkmcnt(2)
Mva 40,200,128
s_mov_b32 s15, 4426112
buffer_load_dwordx4 v1, s[16:19], s15 offen offset:2048 lds
VF 109,25,100,109
Mva 56,200,132
TR 208,2,2048
VM 127,127,123
v_lshlrev_b32 v102, 16, v182
Mva 72,200,136
TR 210,2,2560
VC 112,116,117
Mva 88,200,140
s_mov_b32 s15, 4425088
buffer_load_dwordx4 v1, s[20:23], s15 offen offset:3072 lds
VC 113,118,119
v_and_b32 v110, 0xffff0000, v182
s_waitcnt lgkmcnt(2)
Mva 44,204,128
VC 114,124,125
TR 192,2,3072
Mva 60,204,132
VC 115,126,127
VF 102,24,101,102
Mva 76,204,136
TR 194,2,3584
VF 110,24,109,110
Mva 92,204,140
ds_write_b128 v5, v[112:115] offset:33792
s_waitcnt lgkmcnt(3)
Mva 32,208,144
v_fma_f32 v102, -v25, v109, v102
TR 196,3,2048
Mva 48,208,148
VM 132,128,132
TR 198,3,2560
Mva 64,208,152
VM 133,129,133
VF 110,25,101,110
Mva 80,208,156
VM 134,130,134
VM 128,8,128
s_waitcnt lgkmcnt(3)
Mva 36,192,144
v_lshlrev_b32 v103, 16, v183
TR 200,3,3072
Mva 52,192,148
VM 135,131,135
v_and_b32 v111, 0xffff0000, v183
Mva 68,192,152
TR 202,3,3584
VM 129,8,129
Mva 84,192,156
VM 140,136,140
VF 103,24,102,103
s_waitcnt lgkmcnt(2)
Mva 40,196,144
VM 130,8,130
TR 204,2,16384
Mva 56,196,148
VF 111,24,110,111
TR 206,2,16896
Mva 72,196,152
v_exp_f32 v128, v128
Mva 88,196,156
VM 141,137,141
v_fma_f32 v103, -v25, v110, v103
s_waitcnt lgkmcnt(2)
Mva 44,200,144
VM 131,8,131
VF 111,25,102,111
Mva 60,200,148
TR 208,2,17408
TR 210,2,17920
Mva 76,200,152
v_exp_f32 v129, v129
VC 96,96,97
Mva 92,200,156
VM 142,138,142
VM 136,8,136
s_waitcnt vmcnt(4)
s_barrier
s_waitcnt lgkmcnt(2)
Mva 32,204,160
s_mov_b32 m0, s68
VC 97,98,99
Mva 48,204,164
TR 192,3,16384
TR 194,3,16896
Mva 64,204,168
s_mov_b32 s15, 4723072
buffer_load_dwordx4 v1, s[16:19], s15 offen offset:0 lds
v_exp_f32 v130, v130
VC 98,100,101
Mva 80,204,172
VF 128,128,12,12
VC 99,102,103
s_waitcnt lgkmcnt(2)
Mva 36,208,160
VM 143,139,143
TR 196,3,17408
Mva 52,208,164
s_mov_b32 s15, 4722048
buffer_load_dwordx4 v1, s[20:23], s15 offen offset:1024 lds
VM 137,8,137
Mva 68,208,168
TR 198,3,17920
Mva 84,208,172
v_exp_f32 v131, v131
s_waitcnt lgkmcnt(2)
Mva 40,192,160
s_mov_b32 s15, 5015936
buffer_load_dwordx4 v1, s[16:19], s15 offen offset:2048 lds
VF 129,129,12,12
ds_write_b128 v30, v[96:99] offset:2048
Mva 56,192,164
TR 200,2,18432
VM 138,8,138
Mva 72,192,168
TR 202,2,18944
VC 104,104,105
Mva 88,192,172
s_mov_b32 s15, 5014912
buffer_load_dwordx4 v1, s[20:23], s15 offen offset:3072 lds
v_exp_f32 v136, v136
s_waitcnt lgkmcnt(3)
Mva 44,196,160
VF 130,130,12,12
VC 105,106,107
Mva 60,196,164
TR 204,2,19456
TR 206,2,19968
Mva 76,196,168
v_rcp_f32 v128, v128
VC 106,108,109
Mva 92,196,172
VM 139,8,139
VC 107,110,111
s_waitcnt lgkmcnt(2)
Mva 32,200,176
TR 208,3,18432
TR 210,3,18944
Mva 48,200,180
v_exp_f32 v137, v137
Mva 64,200,184
VF 131,131,12,12
TR 192,3,19456
Mva 80,200,188
v_rcp_f32 v129, v129
s_waitcnt lgkmcnt(3)
Mva 36,204,176
ds_write_b128 v30, v[104:107] offset:6144
v_lshlrev_b32 v112, 16, v184
v_and_b32 v120, 0xffff0000, v184
VF 112,24,103,112
VF 120,24,111,120
v_fma_f32 v112, -v25, v111, v112
VF 120,25,103,120
v_lshlrev_b32 v113, 16, v185
v_and_b32 v121, 0xffff0000, v185
VF 113,24,112,113
VF 121,24,120,121
v_fma_f32 v113, -v25, v120, v113
VF 121,25,112,121
v_lshlrev_b32 v114, 16, v186
v_and_b32 v122, 0xffff0000, v186
VF 114,24,113,114
Mva 52,204,180
VF 122,24,121,122
TR 194,3,19968
Mva 68,204,184
v_exp_f32 v138, v138
Mva 84,204,188
VF 136,136,12,12
v_fma_f32 v114, -v25, v121, v114
s_waitcnt lgkmcnt(3)
Mva 40,208,176
TR 196,2,32768
TR 198,2,33280
Mva 56,208,180
v_rcp_f32 v130, v130
Mva 72,208,184
VM 132,132,128
VF 122,25,113,122
Mva 88,208,188
v_exp_f32 v139, v139
s_waitcnt lgkmcnt(2)
Mva 44,192,176
v_lshlrev_b32 v115, 16, v187
TR 200,2,33792
Mva 60,192,180
VF 137,137,12,12
TR 202,2,34304
Mva 76,192,184
v_and_b32 v123, 0xffff0000, v187
TR 204,3,32768
Mva 92,192,188
v_rcp_f32 v131, v131
s_waitcnt vmcnt(4)
s_barrier
s_waitcnt lgkmcnt(3)
Mva 32,196,192
s_mov_b32 m0, s69
VM 133,133,129
Mva 48,196,196
VF 138,138,12,12
VF 115,24,114,115
Mva 64,196,200
s_mov_b32 s15, 4096
buffer_load_dwordx4 v18, s[24:27], s15 offen offset:0 lds
TR 206,3,33280
Mva 80,196,204
v_rcp_f32 v136, v136
VF 123,24,122,123
s_waitcnt lgkmcnt(2)
Mva 36,200,192
VM 134,134,130
TR 208,3,33792
Mva 52,200,196
s_mov_b32 s15, 39936
buffer_load_dwordx4 v18, s[24:27], s15 offen offset:1024 lds
VF 139,139,12,12
v_fma_f32 v115, -v25, v122, v115
Mva 68,200,200
TR 210,3,34304
VF 123,25,114,123
Mva 84,200,204
v_rcp_f32 v137, v137
s_waitcnt lgkmcnt(2)
Mva 40,204,192
s_mov_b32 s15, 75776
buffer_load_dwordx4 v18, s[24:27], s15 offen offset:2048 lds
VM 135,135,131
Mva 56,204,196
TR 192,2,34816
TR 194,2,35328
Mva 72,204,200
v_rcp_f32 v138, v138
v_lshlrev_b32 v116, 16, v188
Mva 88,204,204
s_mov_b32 s15, 111616
buffer_load_dwordx4 v18, s[24:27], s15 offen offset:3072 lds
VM 140,140,136
s_waitcnt lgkmcnt(2)
Mva 44,208,192
v_and_b32 v124, 0xffff0000, v188
TR 196,2,35840
Mva 60,208,196
v_rcp_f32 v139, v139
Mva 76,208,200
TR 198,2,36352
VM 141,141,137
VF 116,24,115,116
Mva 92,208,204
VM 142,142,138
VM 143,143,139
s_waitcnt lgkmcnt(2)
Mva 32,192,208
TR 200,3,34816
VC 128,132,133
Mva 48,192,212
VF 124,24,123,124
TR 202,3,35328
Mva 64,192,216
VC 129,134,135
VC 130,140,141
Mva 80,192,220
v_fma_f32 v116, -v25, v123, v116
VC 131,142,143
s_waitcnt lgkmcnt(2)
Mva 36,196,208
TR 204,3,35840
VF 124,25,115,124
Mva 52,196,212
TR 206,3,36352
v_lshlrev_b32 v117, 16, v189
Mva 68,196,216
ds_write_b128 v5, v[128:131] offset:34816
Mva 84,196,220
VM 148,144,148
TR 208,2,49152
s_waitcnt lgkmcnt(4)
Mva 40,200,208
VM 149,145,149
TR 210,2,49664
Mva 56,200,212
VM 150,146,150
v_and_b32 v125, 0xffff0000, v189
Mva 72,200,216
VM 144,9,144
VM 151,147,151
Mva 88,200,220
VF 117,24,116,117
TR 192,2,50176
s_waitcnt lgkmcnt(4)
Mva 44,204,208
VM 145,9,145
TR 194,2,50688
Mva 60,204,212
VM 156,152,156
VF 125,24,124,125
Mva 76,204,216
VM 146,9,146
TR 196,3,49152
Mva 92,204,220
v_exp_f32 v144, v144
s_waitcnt vmcnt(4)
s_barrier
s_waitcnt lgkmcnt(3)
Mva 32,208,224
s_mov_b32 m0, s70
v_fma_f32 v117, -v25, v124, v117
Mva 48,208,228
s_mov_b32 s15, 4224
buffer_load_dwordx4 v18, s[24:27], s15 offen offset:0 lds
Mva 64,208,232
TR 198,3,49664
VM 157,153,157
Mva 80,208,236
VM 147,9,147
VF 125,25,116,125
s_waitcnt lgkmcnt(2)
Mva 36,192,224
s_mov_b32 s15, 40064
buffer_load_dwordx4 v18, s[24:27], s15 offen offset:1024 lds
Mva 52,192,228
TR 200,3,50176
v_exp_f32 v145, v145
v_lshlrev_b32 v118, 16, v190
Mva 68,192,232
TR 202,3,50688
VM 158,154,158
Mva 84,192,236
VM 152,9,152
v_and_b32 v126, 0xffff0000, v190
s_waitcnt lgkmcnt(2)
Mva 40,196,224
s_mov_b32 s15, 75904
buffer_load_dwordx4 v18, s[24:27], s15 offen offset:2048 lds
Mva 56,196,228
TR 204,2,51200
v_exp_f32 v146, v146
Mva 72,196,232
TR 206,2,51712
VF 144,144,13,13
VF 118,24,117,118
Mva 88,196,236
s_mov_b32 s15, 111744
buffer_load_dwordx4 v18, s[24:27], s15 offen offset:3072 lds
VM 159,155,159
s_waitcnt lgkmcnt(2)
Mva 44,200,224
VM 153,9,153
VF 126,24,125,126
Mva 60,200,228
TR 208,2,52224
TR 210,2,52736
Mva 76,200,232
v_exp_f32 v147, v147
v_fma_f32 v118, -v25, v125, v118
Mva 92,200,236
VF 145,145,13,13
VM 154,9,154
s_waitcnt lgkmcnt(2)
Mva 32,204,240
)"
R"(TR 192,3,51200
VF 126,25,117,126
Mva 48,204,244
TR 194,3,51712
v_lshlrev_b32 v119, 16, v191
Mva 64,204,248
v_exp_f32 v152, v152
Mva 80,204,252
VF 146,146,13,13
TR 196,3,52224
s_waitcnt lgkmcnt(3)
Mva 36,208,240
v_rcp_f32 v144, v144
Mva 52,208,244
v_and_b32 v127, 0xffff0000, v191
TR 198,3,52736
Mva 68,208,248
VM 155,9,155
VF 119,24,118,119
Mva 84,208,252
v_exp_f32 v153, v153
s_waitcnt lgkmcnt(2)
Mva 40,192,240
TR 200,2,0
VF 147,147,13,13
Mva 56,192,244
TR 202,2,512
VF 127,24,126,127
Mva 72,192,248
v_rcp_f32 v145, v145
Mva 88,192,252
v_fma_f32 v119, -v25, v126, v119
TR 204,2,1024
s_waitcnt lgkmcnt(3)
Mva 44,196,240
v_exp_f32 v154, v154
Mva 60,196,244
TR 206,2,1536
VF 152,152,13,13
Mva 76,196,248
VF 127,25,118,127
TR 208,3,0
Mva 92,196,252
v_rcp_f32 v146, v146
s_waitcnt vmcnt(4)
s_barrier
s_waitcnt lgkmcnt(3)
Mvv 32,200,224
s_mov_b32 m0, s71
VM 148,148,144
Mvv 48,200,228
VC 112,112,113
TR 210,3,512
Mvv 64,200,232
s_mov_b32 s15, 4352
buffer_load_dwordx4 v18, s[24:27], s15 offen offset:0 lds
Mvv 80,200,236
v_exp_f32 v155, v155
VC 113,114,115
s_waitcnt lgkmcnt(2)
Mvv 36,204,224
VF 153,153,13,13
TR 192,3,1024
Mvv 52,204,228
s_mov_b32 s15, 40192
buffer_load_dwordx4 v18, s[24:27], s15 offen offset:1024 lds
VC 114,116,117
Mvv 68,204,232
TR 194,3,1536
v_rcp_f32 v147, v147
Mvv 84,204,236
VM 149,149,145
VF 154,154,13,13
s_waitcnt lgkmcnt(2)
Mvv 40,208,224
s_mov_b32 s15, 76032
buffer_load_dwordx4 v18, s[24:27], s15 offen offset:2048 lds
VC 115,118,119
Mvv 56,208,228
TR 196,2,2048
TR 198,2,2560
Mvv 72,208,232
v_rcp_f32 v152, v152
Mvv 88,208,236
s_mov_b32 s15, 111872
buffer_load_dwordx4 v18, s[24:27], s15 offen offset:3072 lds
VM 150,150,146
s_waitcnt lgkmcnt(2)
Mvv 44,192,224
VF 155,155,13,13
TR 200,2,3072
Mvv 60,192,228
TR 202,2,3584
Mvv 76,192,232
v_rcp_f32 v153, v153
ds_write_b128 v31, v[112:115] offset:2048
Mvv 92,192,236
VM 151,151,147
TR 204,3,2048
s_waitcnt lgkmcnt(4)
Mvv 32,196,240
v_rcp_f32 v154, v154
Mvv 48,196,244
VC 120,120,121
TR 206,3,2560
Mvv 64,196,248
VM 156,156,152
VC 121,122,123
Mvv 80,196,252
v_rcp_f32 v155, v155
s_waitcnt lgkmcnt(3)
Mvv 36,200,240
TR 208,3,3072
VM 157,157,153
Mvv 52,200,244
VC 122,124,125
TR 210,3,3584
Mvv 68,200,248
VM 158,158,154
VM 159,159,155
Mvv 84,200,252
v_add_u32 v2, 0x10000, v19
v_add_u32 v3, 0x10000, v20
s_waitcnt lgkmcnt(2)
Mvv 40,204,240
VC 144,148,149
VC 123,126,127
Mvv 56,204,244
TR 192,19,16384
VC 145,150,151
Mvv 72,204,248
TR 194,19,24576
VC 146,156,157
Mvv 88,204,252
VC 147,158,159
TR 196,20,16384
s_waitcnt lgkmcnt(3)
Mvv 44,208,240
TR 198,20,24576
Mvv 60,208,244
ds_write_b128 v5, v[144:147] offset:35840
Mvv 76,208,248
ds_write_b128 v31, v[120:123] offset:6144
Mvv 92,208,252
TR 200,19,17408
s_waitcnt lgkmcnt(1)
s_waitcnt vmcnt(4)
s_barrier
Zva 160,192,0
s_mov_b32 m0, s68
TR 202,19,25600
Zva 168,192,4
s_mov_b32 s15, 4608
buffer_load_dwordx4 v18, s[24:27], s15 offen offset:0 lds
Zva 176,192,8
v_add_u32 v5, 0x10000, v5
VM 36,32,36
Zva 184,192,12
TR 204,20,17408
VM 37,33,37
Zva 164,196,0
s_mov_b32 s15, 40448
buffer_load_dwordx4 v18, s[24:27], s15 offen offset:1024 lds
Zva 172,196,4
ds_read_b64_tr_b16 a[96:97], v216 offset:0
TR 206,20,25600
Zva 180,196,8
VM 38,34,38
VM 32,6,32
Zva 188,196,12
s_mov_b32 s15, 76288
buffer_load_dwordx4 v18, s[24:27], s15 offen offset:2048 lds
s_waitcnt lgkmcnt(3)
Mva 160,200,16
VM 39,35,39
TR 208,19,18432
Mva 168,200,20
VM 33,6,33
TR 210,19,26624
Mva 176,200,24
VM 44,40,44
VM 34,6,34
Mva 184,200,28
s_mov_b32 s15, 112128
buffer_load_dwordx4 v18, s[24:27], s15 offen offset:3072 lds
ds_read_b64_tr_b16 a[98:99], v218 offset:512
s_waitcnt lgkmcnt(3)
Mva 164,204,16
TR 192,20,18432
TR 194,20,26624
Mva 172,204,20
v_exp_f32 v32, v32
Mva 180,204,24
VM 45,41,45
VM 35,6,35
Mva 188,204,28
TR 196,19,19456
ds_read_b64_tr_b16 a[100:101], v216 offset:8
s_waitcnt lgkmcnt(5)
Mva 160,208,32
v_exp_f32 v33, v33
Mva 168,208,36
TR 198,19,27648
VM 46,42,46
Mva 176,208,40
VM 40,6,40
TR 200,20,19456
Mva 184,208,44
v_exp_f32 v34, v34
s_waitcnt lgkmcnt(4)
Mva 164,192,32
TR 202,20,27648
VF 32,32,10,10
Mva 172,192,36
ds_read_b64_tr_b16 a[102:103], v218 offset:520
VM 47,43,47
Mva 180,192,40
VM 41,6,41
ds_read_b128 a[64:67], v4 offset:32768
Mva 188,192,44
ds_read_b128 a[68:71], v4 offset:33792
ds_read_b128 a[72:75], v4 offset:34816
s_waitcnt lgkmcnt(6)
Mva 160,196,48
ds_read_b128 a[76:79], v4 offset:35840
ds_read_b128 a[80:83], v4 offset:36864
ds_read_b128 a[84:87], v4 offset:37888
ds_read_b128 a[88:91], v4 offset:38912
ds_read_b128 a[92:95], v4 offset:39936
Mva 168,196,52
TR 204,19,32768
TR 206,19,40960
Mva 176,196,56
v_exp_f32 v35, v35
Mva 184,196,60
VF 33,33,10,10
VM 42,6,42
s_waitcnt lgkmcnt(11)
Mva 164,200,48
ds_read_b64_tr_b16 a[104:105], v217 offset:0
TR 208,20,32768
Mva 172,200,52
s_sub_u32 s52, s56, 42991616
s_subb_u32 s53, s57, 0
s_mov_b32 s54, 37748736
Mva 180,200,56
TR 210,20,40960
v_exp_f32 v40, v40
Mva 188,200,60
VF 34,34,10,10
TR 192,19,33792
s_waitcnt vmcnt(4)
s_barrier
s_waitcnt lgkmcnt(4)
Zva 96,204,0
s_mov_b32 m0, s69
TR 194,19,41984
Zva 104,204,4
v_rcp_f32 v32, v32
Zva 112,204,8
s_mov_b32 s15, 151552
buffer_load_dwordx4 v18, s[24:27], s15 offen offset:0 lds
VM 43,6,43
ds_read_b64_tr_b16 a[106:107], v219 offset:512
Zva 120,204,12
v_exp_f32 v41, v41
s_waitcnt lgkmcnt(3)
Zva 100,208,0
s_mov_b32 s15, 187392
buffer_load_dwordx4 v18, s[24:27], s15 offen offset:1024 lds
Zva 108,208,4
TR 196,20,33792
VF 35,35,10,10
Zva 116,208,8
TR 198,20,41984
ds_read_b64_tr_b16 a[108:109], v217 offset:8
Zva 124,208,12
v_rcp_f32 v33, v33
s_waitcnt lgkmcnt(4)
Mva 96,192,16
s_mov_b32 s15, 149632
buffer_load_dwordx4 v18, s[24:27], s15 offen offset:2048 lds
Mva 104,192,20
TR 200,19,34816
v_exp_f32 v42, v42
Mva 112,192,24
TR 202,19,43008
VF 40,40,10,10
Mva 120,192,28
s_mov_b32 s15, 185472
buffer_load_dwordx4 v18, s[24:27], s15 offen offset:3072 lds
s_waitcnt lgkmcnt(3)
Mva 100,196,16
v_rcp_f32 v34, v34
Mva 108,196,20
TR 204,20,34816
VM 36,36,32
ds_read_b64_tr_b16 a[110:111], v219 offset:520
Mva 116,196,24
TR 206,20,43008
Mva 124,196,28
v_exp_f32 v43, v43
s_waitcnt lgkmcnt(3)
Mva 96,200,32
VF 41,41,10,10
TR 208,19,35840
Mva 104,200,36
TR 210,19,44032
ds_read_b64_tr_b16 a[112:113], v216 offset:8192
Mva 112,200,40
v_rcp_f32 v35, v35
Mva 120,200,44
VM 37,37,33
VF 42,42,10,10
s_waitcnt lgkmcnt(3)
Mva 100,204,32
TR 192,20,35840
TR 194,20,44032
Mva 108,204,36
v_rcp_f32 v40, v40
Mva 116,204,40
VM 38,38,34
VF 43,43,10,10
Mva 124,204,44
ds_read_b64_tr_b16 a[114:115], v218 offset:8704
TR 196,19,49152
s_waitcnt lgkmcnt(5)
Mva 96,208,48
TR 198,19,57344
Mva 104,208,52
v_rcp_f32 v41, v41
Mva 112,208,56
VM 39,39,35
TR 200,20,49152
Mva 120,208,60
v_rcp_f32 v42, v42
s_waitcnt lgkmcnt(4)
Mva 100,192,48
TR 202,20,57344
VM 44,44,40
Mva 108,192,52
ds_read_b64_tr_b16 a[116:117], v216 offset:8200
Mva 116,192,56
v_rcp_f32 v43, v43
Mva 124,192,60
TR 204,19,50176
VM 45,45,41
s_waitcnt vmcnt(4)
s_barrier
s_waitcnt lgkmcnt(4)
Zva 128,196,0
s_mov_b32 m0, s70
TR 206,19,58368
Zva 136,196,4
s_mov_b32 s15, 151808
buffer_load_dwordx4 v18, s[24:27], s15 offen offset:0 lds
Zva 144,196,8
VM 46,46,42
VM 47,47,43
Zva 152,196,12
VC 32,36,37
ds_read_b64_tr_b16 a[118:119], v218 offset:8712
s_waitcnt lgkmcnt(4)
Zva 132,200,0
s_mov_b32 s15, 187648
buffer_load_dwordx4 v18, s[24:27], s15 offen offset:1024 lds
Zva 140,200,4
TR 208,20,50176
VC 33,38,39
Zva 148,200,8
TR 210,20,58368
VC 34,44,45
Zva 156,200,12
VC 35,46,47
TR 192,19,51200
s_waitcnt lgkmcnt(4)
Mva 128,204,16
s_mov_b32 s15, 150016
buffer_load_dwordx4 v18, s[24:27], s15 offen offset:2048 lds
Mva 136,204,20
ds_read_b64_tr_b16 a[120:121], v217 offset:8192
TR 194,19,59392
Mva 144,204,24
ds_write_b128 v5, v[32:35] offset:16384
Mva 152,204,28
s_mov_b32 s15, 185856
buffer_load_dwordx4 v18, s[24:27], s15 offen offset:3072 lds
VM 52,48,52
s_waitcnt lgkmcnt(4)
Mva 132,208,16
VM 53,49,53
TR 196,20,51200
Mva 140,208,20
VM 54,50,54
TR 198,20,59392
Mva 148,208,24
VM 48,7,48
VM 55,51,55
Mva 156,208,28
ds_read_b64_tr_b16 a[122:123], v219 offset:8704
VM 49,7,49
s_waitcnt lgkmcnt(4)
Mva 128,192,32
TR 200,19,52224
VM 60,56,60
Mva 136,192,36
TR 202,19,60416
VM 50,7,50
Mva 144,192,40
v_exp_f32 v48, v48
Mva 152,192,44
TR 204,20,52224
VM 61,57,61
s_waitcnt lgkmcnt(4)
Mva 132,196,32
ds_read_b64_tr_b16 a[124:125], v217 offset:8200
TR 206,20,60416
Mva 140,196,36
VM 51,7,51
TR 208,19,0
Mva 148,196,40
v_exp_f32 v49, v49
Mva 156,196,44
VM 62,58,62
TR 210,19,8192
s_waitcnt lgkmcnt(5)
Mva 128,200,48
VM 56,7,56
ds_read_b64_tr_b16 a[126:127], v219 offset:8712
Mva 136,200,52
v_exp_f32 v50, v50
Mva 144,200,56
TR 192,20,0
VF 48,48,11,11
Mva 152,200,60
TR 194,20,8192
VM 63,59,63
s_waitcnt lgkmcnt(5)
Mva 132,204,48
VM 57,7,57
ds_read_b64_tr_b16 a[128:129], v216 offset:4096
Mva 140,204,52
TR 196,19,1024
Mva 148,204,56
v_exp_f32 v51, v51
Mva 156,204,60
TR 198,19,9216
VF 49,49,11,11
s_waitcnt vmcnt(4)
s_barrier
s_waitcnt lgkmcnt(6)
Zva 224,208,0
s_mov_b32 m0, s71
VM 58,7,58
Zva 232,208,4
s_mov_b32 s15, 4096
buffer_load_dwordx4 v14, s[40:43], s15 offen offset:0 lds
Zva 240,208,8
TR 200,20,1024
Zva 248,208,12
v_exp_f32 v56, v56
s_waitcnt lgkmcnt(4)
Zva 228,192,0
s_mov_b32 s15, 3072
buffer_load_dwordx4 v14, s[44:47], s15 offen offset:1024 lds
Zva 236,192,4
TR 202,20,9216
VF 50,50,11,11
Zva 244,192,8
ds_read_b64_tr_b16 a[130:131], v218 offset:4608
Zva 252,192,12
s_mov_b32 s15, 2048
buffer_load_dwordx4 v15, s[40:43], s15 offen offset:2048 lds
s_waitcnt lgkmcnt(3)
Mva 224,196,16
v_rcp_f32 v48, v48
Mva 232,196,20
TR 204,19,2048
VM 59,7,59
Mva 240,196,24
TR 206,19,10240
ds_read_b64_tr_b16 a[132:133], v216 offset:4104
Mva 248,196,28
s_mov_b32 s15, 1024
buffer_load_dwordx4 v15, s[44:47], s15 offen offset:3072 lds
v_exp_f32 v57, v57
s_waitcnt lgkmcnt(4)
Mva 228,200,16
VF 51,51,11,11
TR 208,20,2048
Mva 236,200,20
TR 210,20,10240
Mva 244,200,24
v_rcp_f32 v49, v49
Mva 252,200,28
v_exp_f32 v58, v58
s_waitcnt lgkmcnt(3)
Mva 224,204,32
TR 192,19,3072
VF 56,56,11,11
Mva 232,204,36
ds_read_b64_tr_b16 a[134:135], v218 offset:4616
TR 194,19,11264
Mva 240,204,40
v_rcp_f32 v50, v50
Mva 248,204,44
VM 52,52,48
TR 196,20,3072
s_waitcnt lgkmcnt(4)
Mva 228,208,32
TR 198,20,11264
Mva 236,208,36
v_exp_f32 v59, v59
Mva 244,208,40
VF 57,57,11,11
ds_read_b64_tr_b16 a[136:137], v217 offset:4096
Mva 252,208,44
TR 200,19,16384
s_waitcnt lgkmcnt(4)
Mva 224,192,48
v_rcp_f32 v51, v51
Mva 232,192,52
TR 202,19,24576
VM 53,53,49
Mva 240,192,56
VF 58,58,11,11
TR 204,20,16384
Mva 248,192,60
v_rcp_f32 v56, v56
s_waitcnt lgkmcnt(4)
Mva 228,196,48
ds_read_b64_tr_b16 a[138:139], v219 offset:4608
TR 206,20,24576
Mva 236,196,52
VM 54,54,50
VF 59,59,11,11
Mva 244,196,56
TR 208,19,17408
Mva 252,196,60
v_rcp_f32 v57, v57
s_waitcnt vmcnt(4)
s_barrier
s_waitcnt lgkmcnt(4)
Mva 160,200,64
s_mov_b32 m0, s68
TR 210,19,25600
Mva 168,200,68
s_mov_b32 s15, 77824
buffer_load_dwordx4 v14, s[40:43], s15 offen offset:0 lds
Mva 176,200,72
VM 55,55,51
ds_read_b64_tr_b16 a[140:141], v217 offset:4104
Mva 184,200,76
v_rcp_f32 v58, v58
s_waitcnt lgkmcnt(3)
Mva 164,204,64
s_mov_b32 s15, 76800
buffer_load_dwordx4 v14, s[44:47], s15 offen offset:1024 lds
Mva 172,204,68
TR 192,20,17408
VM 60,60,56
Mva 180,204,72
TR 194,20,25600
ds_read_b64_tr_b16 a[142:143], v219 offset:4616
Mva 188,204,76
v_rcp_f32 v59, v59
s_waitcnt lgkmcnt(4)
Mva 160,208,80
s_mov_b32 s15, 75776
buffer_load_dwordx4 v15, s[40:43], s15 offen offset:2048 lds
VM 61,61,57
Mva 168,208,84
TR 196,19,18432
VM 62,62,58
Mva 176,208,88
TR 198,19,26624
VM 63,63,59
Mva 184,208,92
s_mov_b32 s15, 74752
buffer_load_dwordx4 v15, s[44:47], s15 offen offset:3072 lds
VC 48,52,53
s_waitcnt lgkmcnt(3)
Mva 164,192,80
VC 49,54,55
TR 200,20,18432
Mva 172,192,84
VC 50,60,61
TR 202,20,26624
Mva 180,192,88
VC 51,62,63
ds_read_b64_tr_b16 a[144:145], v216 offset:12288
Mva 188,192,92
TR 204,19,19456
s_waitcnt lgkmcnt(4)
Mva 96,196,64
ds_write_b128 v5, v[48:51] offset:17408
Mva 104,196,68
TR 206,19,27648
VM 68,64,68
Mva 112,196,72
)"
R"(VM 69,65,69
ds_read_b64_tr_b16 a[146:147], v218 offset:12800
Mva 120,196,76
VM 70,66,70
TR 208,20,19456
s_waitcnt lgkmcnt(6)
Mva 100,200,64
VM 64,8,64
TR 210,20,27648
Mva 108,200,68
VM 71,67,71
VM 65,8,65
Mva 116,200,72
VM 76,72,76
TR 192,19,32768
Mva 124,200,76
VM 66,8,66
ds_read_b64_tr_b16 a[148:149], v216 offset:12296
s_waitcnt lgkmcnt(5)
Mva 96,204,80
TR 194,19,40960
Mva 104,204,84
v_exp_f32 v64, v64
Mva 112,204,88
VM 77,73,77
TR 196,20,32768
Mva 120,204,92
VM 67,8,67
TR 198,20,40960
s_waitcnt lgkmcnt(5)
Mva 100,208,80
v_exp_f32 v65, v65
Mva 108,208,84
ds_read_b64_tr_b16 a[150:151], v218 offset:12808
VM 78,74,78
Mva 116,208,88
TR 200,19,33792
VM 72,8,72
Mva 124,208,92
TR 202,19,41984
s_waitcnt vmcnt(4)
s_barrier
s_waitcnt lgkmcnt(5)
Mva 128,192,64
s_mov_b32 m0, s69
ds_read_b64_tr_b16 a[152:153], v217 offset:12288
Mva 136,192,68
s_mov_b32 s15, 151552
buffer_load_dwordx4 v14, s[40:43], s15 offen offset:0 lds
Mva 144,192,72
v_exp_f32 v66, v66
Mva 152,192,76
VF 64,64,12,12
TR 204,20,33792
s_waitcnt lgkmcnt(5)
Mva 132,196,64
s_mov_b32 s15, 150528
buffer_load_dwordx4 v14, s[44:47], s15 offen offset:1024 lds
Mva 140,196,68
VM 79,75,79
TR 206,20,41984
Mva 148,196,72
VM 73,8,73
TR 208,19,34816
Mva 156,196,76
s_mov_b32 s15, 149504
buffer_load_dwordx4 v15, s[40:43], s15 offen offset:2048 lds
s_waitcnt lgkmcnt(4)
Mva 128,200,80
v_exp_f32 v67, v67
Mva 136,200,84
VF 65,65,12,12
TR 210,19,43008
Mva 144,200,88
VM 74,8,74
ds_read_b64_tr_b16 a[154:155], v219 offset:12800
Mva 152,200,92
s_mov_b32 s15, 148480
buffer_load_dwordx4 v15, s[44:47], s15 offen offset:3072 lds
s_waitcnt lgkmcnt(3)
Mva 132,204,80
v_exp_f32 v72, v72
Mva 140,204,84
TR 192,20,34816
VF 66,66,12,12
Mva 148,204,88
TR 194,20,43008
ds_read_b64_tr_b16 a[156:157], v217 offset:12296
Mva 156,204,92
v_rcp_f32 v64, v64
s_waitcnt lgkmcnt(4)
Mva 224,208,64
VM 75,8,75
TR 196,19,35840
Mva 232,208,68
TR 198,19,44032
Mva 240,208,72
v_exp_f32 v73, v73
Mva 248,208,76
VF 67,67,12,12
TR 200,20,35840
s_waitcnt lgkmcnt(4)
Mva 228,192,64
v_rcp_f32 v65, v65
Mva 236,192,68
TR 202,20,44032
ds_read_b64_tr_b16 a[158:159], v219 offset:12808
Mva 244,192,72
v_exp_f32 v74, v74
Mva 252,192,76
VF 72,72,12,12
ds_read_b128 v[204:207], v16 offset:49152
s_waitcnt lgkmcnt(4)
Mva 224,196,80
v_rcp_f32 v66, v66
Mva 232,196,84
VM 68,68,64
ds_read_b128 v[208:211], v16 offset:53248
Mva 240,196,88
v_exp_f32 v75, v75
Mva 248,196,92
VF 73,73,12,12
ds_read_b128 v[192:195], v17 offset:49152
s_waitcnt lgkmcnt(4)
Mva 228,200,80
v_rcp_f32 v67, v67
Mva 236,200,84
VM 69,69,65
VF 74,74,12,12
Mva 244,200,88
v_rcp_f32 v72, v72
Mva 252,200,92
ds_read_b128 v[196:199], v17 offset:53248
VM 70,70,66
s_waitcnt vmcnt(4)
s_barrier
s_waitcnt lgkmcnt(3)
Mva 160,204,96
s_mov_b32 m0, s70
VF 75,75,12,12
Mva 168,204,100
s_mov_b32 s15, 299008
buffer_load_dwordx4 v14, s[40:43], s15 offen offset:0 lds
Mva 176,204,104
v_rcp_f32 v73, v73
Mva 184,204,108
ds_read_b128 v[200:203], v16 offset:50176
VM 71,71,67
s_waitcnt lgkmcnt(3)
Mva 164,208,96
s_mov_b32 s15, 297984
buffer_load_dwordx4 v14, s[44:47], s15 offen offset:1024 lds
Mva 172,208,100
v_rcp_f32 v74, v74
Mva 180,208,104
VM 76,76,72
ds_read_b128 v[204:207], v16 offset:54272
Mva 188,208,108
s_mov_b32 s15, 296960
buffer_load_dwordx4 v15, s[40:43], s15 offen offset:2048 lds
s_waitcnt lgkmcnt(3)
Mva 160,192,112
v_rcp_f32 v75, v75
Mva 168,192,116
VM 77,77,73
VM 78,78,74
Mva 176,192,120
s_mov_b32 s15, 295936
buffer_load_dwordx4 v15, s[44:47], s15 offen offset:3072 lds
Mva 184,192,124
ds_read_b128 v[208:211], v17 offset:50176
VM 79,79,75
s_waitcnt lgkmcnt(3)
Mva 164,196,112
VC 64,68,69
VC 65,70,71
Mva 172,196,116
VC 66,76,77
VC 67,78,79
Mva 180,196,120
ds_read_b128 v[192:195], v17 offset:54272
Mva 188,196,124
ds_write_b128 v5, v[64:67] offset:18432
s_waitcnt lgkmcnt(4)
Mva 160,200,128
VM 84,80,84
VM 85,81,85
Mva 168,200,132
VM 86,82,86
VM 80,9,80
Mva 176,200,136
ds_read_b128 v[196:199], v16 offset:0
VM 87,83,87
Mva 184,200,140
VM 81,9,81
VM 92,88,92
s_waitcnt lgkmcnt(4)
Mva 164,204,128
VM 82,9,82
Mva 172,204,132
v_exp_f32 v80, v80
Mva 180,204,136
ds_read_b128 v[200:203], v16 offset:4096
VM 93,89,93
Mva 188,204,140
VM 83,9,83
s_waitcnt lgkmcnt(4)
Mva 160,208,144
v_exp_f32 v81, v81
Mva 168,208,148
VM 94,90,94
VM 88,9,88
Mva 176,208,152
ds_read_b128 v[204:207], v17 offset:0
Mva 184,208,156
v_exp_f32 v82, v82
s_waitcnt lgkmcnt(4)
Mva 164,192,144
VF 80,80,13,13
VM 95,91,95
Mva 172,192,148
VM 89,9,89
Mva 180,192,152
ds_read_b128 v[208:211], v17 offset:4096
Mva 188,192,156
v_exp_f32 v83, v83
s_waitcnt vmcnt(4)
s_barrier
s_waitcnt lgkmcnt(3)
Mva 96,196,96
s_mov_b32 m0, s71
VF 81,81,13,13
Mva 104,196,100
s_mov_b32 s15, 225280
buffer_load_dwordx4 v18, s[24:27], s15 offen offset:0 lds
Mva 112,196,104
ds_read_b128 v[192:195], v16 offset:1024
VM 90,9,90
Mva 120,196,108
v_exp_f32 v88, v88
s_waitcnt lgkmcnt(3)
Mva 100,200,96
s_mov_b32 s15, 261120
buffer_load_dwordx4 v18, s[24:27], s15 offen offset:1024 lds
Mva 108,200,100
VF 82,82,13,13
Mva 116,200,104
ds_read_b128 v[196:199], v16 offset:5120
Mva 124,200,108
s_mov_b32 s15, 223360
buffer_load_dwordx4 v18, s[24:27], s15 offen offset:2048 lds
s_waitcnt lgkmcnt(3)
Mva 96,204,112
v_rcp_f32 v80, v80
Mva 104,204,116
VM 91,9,91
Mva 112,204,120
s_mov_b32 s15, 259200
buffer_load_dwordx4 v18, s[24:27], s15 offen offset:3072 lds
Mva 120,204,124
ds_read_b128 v[200:203], v17 offset:1024
s_waitcnt lgkmcnt(3)
Mva 100,208,112
v_exp_f32 v89, v89
Mva 108,208,116
VF 83,83,13,13
Mva 116,208,120
ds_read_b128 v[204:207], v17 offset:5120
Mva 124,208,124
v_rcp_f32 v81, v81
s_waitcnt lgkmcnt(3)
Mva 96,192,128
v_exp_f32 v90, v90
Mva 104,192,132
VF 88,88,13,13
Mva 112,192,136
ds_read_b128 v[208:211], v16 offset:16384
Mva 120,192,140
v_rcp_f32 v82, v82
s_waitcnt lgkmcnt(3)
Mva 100,196,128
VM 84,84,80
Mva 108,196,132
v_exp_f32 v91, v91
Mva 116,196,136
ds_read_b128 v[192:195], v16 offset:20480
VF 89,89,13,13
Mva 124,196,140
v_rcp_f32 v83, v83
s_waitcnt lgkmcnt(3)
Mva 96,200,144
VM 85,85,81
VF 90,90,13,13
Mva 104,200,148
v_rcp_f32 v88, v88
Mva 112,200,152
ds_read_b128 v[196:199], v17 offset:16384
VM 86,86,82
Mva 120,200,156
VF 91,91,13,13
s_waitcnt lgkmcnt(3)
Mva 100,204,144
v_rcp_f32 v89, v89
Mva 108,204,148
VM 87,87,83
Mva 116,204,152
ds_read_b128 v[200:203], v17 offset:20480
Mva 124,204,156
v_rcp_f32 v90, v90
s_waitcnt vmcnt(4)
s_barrier
s_waitcnt lgkmcnt(3)
Mva 128,208,96
s_mov_b32 m0, s68
VM 92,92,88
Mva 136,208,100
s_mov_b32 s15, 225536
buffer_load_dwordx4 v18, s[24:27], s15 offen offset:0 lds
Mva 144,208,104
ds_read_b128 v[204:207], v16 offset:17408
Mva 152,208,108
v_rcp_f32 v91, v91
s_waitcnt lgkmcnt(3)
Mva 132,192,96
s_mov_b32 s15, 261376
buffer_load_dwordx4 v18, s[24:27], s15 offen offset:1024 lds
Mva 140,192,100
VM 93,93,89
VM 94,94,90
Mva 148,192,104
ds_read_b128 v[208:211], v16 offset:21504
VM 95,95,91
Mva 156,192,108
s_mov_b32 s15, 223744
buffer_load_dwordx4 v18, s[24:27], s15 offen offset:2048 lds
s_waitcnt lgkmcnt(3)
Mva 128,196,112
VC 80,84,85
VC 81,86,87
Mva 136,196,116
VC 82,92,93
VC 83,94,95
Mva 144,196,120
s_mov_b32 s15, 259584
buffer_load_dwordx4 v18, s[24:27], s15 offen offset:3072 lds
Mva 152,196,124
ds_read_b128 v[192:195], v17 offset:17408
s_waitcnt lgkmcnt(3)
Mva 132,200,112
ds_write_b128 v5, v[80:83] offset:19456
Mva 140,200,116
Mva 148,200,120
ds_read_b128 v[196:199], v17 offset:21504
Mva 156,200,124
s_waitcnt lgkmcnt(4)
Mva 128,204,128
Mva 136,204,132
Mva 144,204,136
ds_read_b128 v[200:203], v16 offset:32768
Mva 152,204,140
s_waitcnt lgkmcnt(4)
Mva 132,208,128
Mva 140,208,132
Mva 148,208,136
ds_read_b128 v[204:207], v16 offset:36864
Mva 156,208,140
s_waitcnt lgkmcnt(4)
Mva 128,192,144
Mva 136,192,148
Mva 144,192,152
ds_read_b128 v[208:211], v17 offset:32768
Mva 152,192,156
s_waitcnt lgkmcnt(3)
Mva 132,196,144
Mva 140,196,148
Mva 148,196,152
ds_read_b128 v[192:195], v17 offset:36864
Mva 156,196,156
s_waitcnt vmcnt(4)
s_barrier
s_waitcnt lgkmcnt(3)
Mva 224,200,96
s_mov_b32 m0, s69
v_add_u32 v4, 0x10000, v4
Mva 232,200,100
s_mov_b32 s15, 4480
buffer_load_dwordx4 v18, s[24:27], s15 offen offset:0 lds
Mva 240,200,104
ds_read_b128 v[196:199], v16 offset:33792
Mva 248,200,108
ds_read_b128 a[160:163], v4 offset:16384
s_waitcnt lgkmcnt(4)
Mva 228,204,96
s_mov_b32 s15, 40320
buffer_load_dwordx4 v18, s[24:27], s15 offen offset:1024 lds
Mva 236,204,100
ds_read_b128 a[164:167], v4 offset:17408
Mva 244,204,104
ds_read_b128 v[200:203], v16 offset:37888
Mva 252,204,108
s_mov_b32 s15, 76160
buffer_load_dwordx4 v18, s[24:27], s15 offen offset:2048 lds
s_waitcnt lgkmcnt(5)
Mva 224,208,112
ds_read_b128 a[168:171], v4 offset:18432
Mva 232,208,116
ds_read_b128 a[172:175], v4 offset:19456
Mva 240,208,120
s_mov_b32 s15, 112000
buffer_load_dwordx4 v18, s[24:27], s15 offen offset:3072 lds
Mva 248,208,124
ds_read_b128 v[204:207], v17 offset:33792
s_waitcnt lgkmcnt(7)
Mva 228,192,112
ds_read_b128 a[176:179], v4 offset:20480
Mva 236,192,116
ds_read_b128 a[180:183], v4 offset:21504
Mva 244,192,120
ds_read_b128 v[208:211], v17 offset:37888
Mva 252,192,124
ds_read_b128 a[184:187], v4 offset:22528
s_waitcnt lgkmcnt(10)
Mva 224,196,128
ds_read_b128 a[188:191], v4 offset:23552
Mva 232,196,132
Mva 240,196,136
TR 192,19,49152
Mva 248,196,140
TR 194,19,57344
s_waitcnt lgkmcnt(10)
Mva 228,200,128
Mva 236,200,132
Mva 244,200,136
TR 196,20,49152
Mva 252,200,140
TR 198,20,57344
s_waitcnt lgkmcnt(9)
Mva 224,204,144
Mva 232,204,148
Mva 240,204,152
TR 200,19,50176
Mva 248,204,156
TR 202,19,58368
s_waitcnt lgkmcnt(8)
Mva 228,208,144
Mva 236,208,148
Mva 244,208,152
TR 204,20,50176
Mva 252,208,156
TR 206,20,58368
s_waitcnt vmcnt(4)
s_barrier
s_waitcnt lgkmcnt(6)
Mva 160,192,160
s_mov_b32 m0, s70
Mva 168,192,164
s_mov_b32 s15, 225280
buffer_load_dwordx4 v14, s[40:43], s15 offen offset:0 lds
Mva 176,192,168
TR 208,19,51200
Mva 184,192,172
TR 210,19,59392
s_waitcnt lgkmcnt(6)
Mva 164,196,160
s_mov_b32 s15, 224256
buffer_load_dwordx4 v14, s[44:47], s15 offen offset:1024 lds
Mva 172,196,164
Mva 180,196,168
TR 192,20,51200
Mva 188,196,172
s_mov_b32 s15, 223232
buffer_load_dwordx4 v15, s[40:43], s15 offen offset:2048 lds
s_waitcnt lgkmcnt(5)
Mva 160,200,176
TR 194,20,59392
Mva 168,200,180
Mva 176,200,184
s_mov_b32 s15, 222208
buffer_load_dwordx4 v15, s[44:47], s15 offen offset:3072 lds
Mva 184,200,188
TR 196,19,52224
s_waitcnt lgkmcnt(5)
Mva 164,204,176
TR 198,19,60416
Mva 172,204,180
Mva 180,204,184
TR 200,20,52224
Mva 188,204,188
TR 202,20,60416
s_waitcnt lgkmcnt(6)
Mva 96,208,160
Mva 104,208,164
Mva 112,208,168
VC 160,160,161
VC 161,162,163
Mva 120,208,172
VC 162,164,165
VC 163,166,167
s_waitcnt lgkmcnt(4)
Mva 100,192,160
s_mov_b32 s67, 0
buffer_store_dwordx4 v[160:163], v21, s[52:55], s67 offen sc1
Mva 108,192,164
TR 204,19,0
VC 168,168,169
Mva 116,192,168
TR 206,19,8192
VC 169,170,171
Mva 124,192,172
VC 170,172,173
VC 171,174,175
s_waitcnt lgkmcnt(4)
Mva 96,196,176
TR 208,20,0
Mva 104,196,180
TR 210,20,8192
Mva 112,196,184
s_mov_b32 s67, 9216
buffer_store_dwordx4 v[168:171], v21, s[52:55], s67 offen sc1
Mva 120,196,188
VC 176,176,177
VC 177,178,179
s_waitcnt lgkmcnt(4)
Mva 100,200,176
TR 192,19,1024
VC 178,180,181
Mva 108,200,180
TR 194,19,9216
VC 179,182,183
Mva 116,200,184
s_mov_b32 s67, 18432
buffer_store_dwordx4 v[176:179], v21, s[52:55], s67 offen sc1
Mva 124,200,188
TR 196,20,1024
VC 184,184,185
s_waitcnt vmcnt(7)
s_barrier
s_waitcnt lgkmcnt(5)
Mva 128,204,160
s_mov_b32 m0, s71
TR 198,20,9216
Mva 136,204,164
s_mov_b32 s15, 151936
buffer_load_dwordx4 v18, s[24:27], s15 offen offset:0 lds
Mva 144,204,168
VC 185,186,187
VC 186,188,189
Mva 152,204,172
VC 187,190,191
TR 200,19,2048
s_waitcnt lgkmcnt(5)
Mva 132,208,160
s_mov_b32 s15, 187776
buffer_load_dwordx4 v18, s[24:27], s15 offen offset:1024 lds
Mva 140,208,164
TR 202,19,10240
Mva 148,208,168
s_mov_b32 s67, 27648
buffer_store_dwordx4 v[184:187], v21, s[52:55], s67 offen sc1
Mva 156,208,172
s_mov_b32 s15, 223616
buffer_load_dwordx4 v18, s[24:27], s15 offen offset:2048 lds
s_waitcnt lgkmcnt(4)
Mva 128,192,176
TR 204,20,2048
Mva 136,192,180
TR 206,20,10240
Mva 144,192,184
s_mov_b32 s15, 259456
buffer_load_dwordx4 v18, s[24:27], s15 offen offset:3072 lds
Mva 152,192,188
VC 96,96,97
VC 97,98,99
s_waitcnt lgkmcnt(4)
Mva 132,196,176
VC 98,100,101
s_load_dwordx4 s[72:75], s[8:9], 96
Mva 140,196,180
VC 99,102,103
v_and_b32 v180, 7, v0
Mva 148,196,184
TR 208,19,3072
v_lshrrev_b32 v181, 5, v0
Mva 156,196,188
s_mov_b32 s67, 1024
buffer_store_dwordx4 v[96:99], v21, s[52:55], s67 offen sc1
s_waitcnt lgkmcnt(0)
Mva 224,200,160
TR 210,19,11264
VC 104,104,105
Mva 232,200,164
VC 105,106,107
v_bfe_u32 v182, v0, 3, 2
Mva 240,200,168
VC 106,108,109
TR 192,20,3072
Mva 248,200,172
VC 107,110,111
v_and_b32 v183, 4, v0
Mva 228,204,160
)"
R"(TR 194,20,11264
v_and_b32 v184, 3, v0
Mva 236,204,164
s_mov_b32 s67, 10240
buffer_store_dwordx4 v[104:107], v21, s[52:55], s67 offen sc1
Mva 244,204,168
VC 112,112,113
TR 196,19,16384
Mva 252,204,172
VC 113,114,115
v_lshl_add_u32 v183, v183, 2, v184
s_waitcnt lgkmcnt(3)
Mva 224,208,176
TR 198,19,24576
VC 114,116,117
Mva 232,208,180
s_lshl_b32 s76, s64, 3
VC 115,118,119
Mva 240,208,184
v_lshl_add_u32 v188, v182, 4, v180
TR 200,20,16384
Mva 248,208,188
s_mov_b32 s67, 19456
buffer_store_dwordx4 v[112:115], v21, s[52:55], s67 offen sc1
s_waitcnt lgkmcnt(3)
Mva 228,192,176
TR 202,20,24576
VC 120,120,121
Mva 236,192,180
v_add_lshl_u32 v188, v188, s76, 4
VC 121,122,123
Mva 244,192,184
VC 122,124,125
v_mul_u32_u24 v185, 147456, v181
Mva 252,192,188
TR 204,19,17408
VC 123,126,127
s_waitcnt vmcnt(11)
s_barrier
s_waitcnt lgkmcnt(3)
Zva 32,196,0
s_mov_b32 m0, s68
TR 206,19,25600
Zva 40,196,4
s_mov_b32 s15, 4736
buffer_load_dwordx4 v18, s[24:27], s15 offen offset:0 lds
Zva 48,196,8
v_add_u32 v188, v185, v188
s_lshr_b32 s90, s13, 1
s_and_b32 s91, s13, 1
Zva 56,196,12
s_mov_b32 s67, 28672
buffer_store_dwordx4 v[120:123], v21, s[52:55], s67 offen sc1
s_waitcnt lgkmcnt(2)
Zva 36,200,0
VC 128,128,129
s_lshr_b32 s77, s13, 3
s_lshl_b32 s93, s14, 6
Zva 44,200,4
s_mov_b32 s15, 40576
buffer_load_dwordx4 v18, s[24:27], s15 offen offset:1024 lds
TR 208,20,17408
VC 129,130,131
Zva 52,200,8
TR 210,20,25600
VC 130,132,133
s_add_u32 s93, s93, s77
Zva 60,200,12
VC 131,134,135
s_add_u32 s93, s93, s91
s_add_u32 s93, s93, s91
s_waitcnt lgkmcnt(2)
Mva 32,204,16
s_mov_b32 s15, 76416
buffer_load_dwordx4 v18, s[24:27], s15 offen offset:2048 lds
Mva 40,204,20
TR 192,19,18432
s_lshl_b32 s93, s93, 2
s_and_b32 s92, s90, 3
Mva 48,204,24
TR 194,19,26624
s_mov_b32 s67, 2048
buffer_store_dwordx4 v[128:131], v21, s[52:55], s67 offen sc1
Mva 56,204,28
s_mov_b32 s15, 112256
buffer_load_dwordx4 v18, s[24:27], s15 offen offset:3072 lds
VC 136,136,137
s_add_u32 s93, s93, s92
s_waitcnt lgkmcnt(2)
Mva 36,208,16
VC 137,138,139
s_mul_i32 s93, s93, 9
s_add_u32 s93, s93, s65
Mva 44,208,20
TR 196,20,18432
VC 138,140,141
Mva 52,208,24
TR 198,20,26624
VC 139,142,143
s_lshl_b32 s93, s93, 10
Mva 60,208,28
s_mov_b32 s67, 11264
buffer_store_dwordx4 v[136:139], v21, s[52:55], s67 offen sc1
s_waitcnt lgkmcnt(2)
Mva 32,192,32
v_add_u32 v188, s93, v188
TR 200,19,19456
Mva 40,192,36
VC 144,144,145
s_lshl_b32 s93, s77, 4
s_and_b32 s90, s90, 3
Mva 48,192,40
TR 202,19,27648
VC 145,146,147
Mva 56,192,44
VC 146,148,149
s_add_u32 s93, s93, s90
s_add_u32 s93, s93, s76
s_waitcnt lgkmcnt(2)
Mva 36,196,32
VC 147,150,151
TR 204,20,19456
Mva 44,196,36
s_lshl_b32 s93, s93, 2
TR 206,20,27648
Mva 52,196,40
s_mov_b32 s67, 20480
buffer_store_dwordx4 v[144:147], v21, s[52:55], s67 offen sc1
Mva 60,196,44
VC 152,152,153
v_add_u32 v183, s93, v183
s_waitcnt lgkmcnt(2)
Mva 32,200,48
VC 153,154,155
ds_read_b128 v[208:211], v16 offset:32768
Mva 40,200,52
VC 154,156,157
v_and_b32 v185, 16, v0
Mva 48,200,56
VC 155,158,159
v_lshl_add_u32 v185, v181, 3, v185
Mva 56,200,60
ds_read_b128 v[192:195], v16 offset:36864
v_and_b32 v182, 1, v182
s_waitcnt lgkmcnt(2)
Mva 36,204,48
s_mov_b32 s67, 29696
buffer_store_dwordx4 v[152:155], v21, s[52:55], s67 offen sc1
Mva 44,204,52
v_lshl_add_u32 v185, v182, 2, v185
s_lshl_b32 s93, s65, 1
s_add_u32 s93, s93, s91
Mva 52,204,56
ds_read_b128 v[196:199], v17 offset:32768
s_lshl_b32 s93, s93, 5
Mva 60,204,60
v_add_lshl_u32 v185, v185, s93, 1
s_lshl_b32 s93, s14, 7
s_waitcnt vmcnt(12)
s_barrier
s_waitcnt lgkmcnt(2)
Mva 32,208,96
s_mov_b32 m0, s69
ds_read_b128 v[200:203], v17 offset:36864
Mva 40,208,100
s_mov_b32 s15, 372736
buffer_load_dwordx4 v14, s[40:43], s15 offen offset:0 lds
Mva 48,208,104
v_add_u32 v184, s93, v183
v_mul_u32_u24 v184, 1152, v184
Mva 56,208,108
ds_read_b128 v[204:207], v16 offset:33792
v_add_u32 v184, v184, v185
s_waitcnt lgkmcnt(3)
Mva 36,192,96
s_mov_b32 s15, 371712
buffer_load_dwordx4 v14, s[44:47], s15 offen offset:1024 lds
Mva 44,192,100
v_lshlrev_b32 v189, 1, v184
v_lshlrev_b32 v183, 3, v183
Mva 52,192,104
ds_read_b128 v[208:211], v16 offset:37888
s_lshl_b32 s91, s91, 1
s_add_u32 s91, s91, s77
Mva 60,192,108
s_mov_b32 s15, 370688
buffer_load_dwordx4 v15, s[40:43], s15 offen offset:2048 lds
s_waitcnt lgkmcnt(3)
Mva 32,196,112
s_lshl_b32 s93, s14, 2
s_add_u32 s91, s91, s93
s_lshl_b32 s91, s91, 9
s_add_u32 s91, s91, 163840
Mva 40,196,116
v_lshl_add_u32 v190, v0, 3, s91
s_add_u32 s93, s93, s11
s_lshl_b32 s93, s93, 6
Mva 48,196,120
s_mov_b32 s15, 369664
buffer_load_dwordx4 v15, s[44:47], s15 offen offset:3072 lds
Mva 56,196,124
ds_read_b128 v[192:195], v17 offset:33792
s_add_u32 s93, s93, s13
s_lshl_b32 s93, s93, 3
s_waitcnt lgkmcnt(3)
Mva 36,200,112
s_add_u32 s93, s93, 163840
v_bfrev_b32 v191, 1
Mva 44,200,116
v_mov_b32 v186, s93
v_cmp_eq_u32 vcc, 0, v0
Mva 52,200,120
ds_read_b128 v[196:199], v17 offset:37888
v_cndmask_b32 v191, v191, v186, vcc
Mva 60,200,124
v_mov_b32 v186, s100
v_mov_b32 v187, s100
s_waitcnt lgkmcnt(3)
Mva 32,204,128
s_mov_b32 s92, 0x40000
s_mov_b32 s16, s74
s_mov_b32 s17, s75
s_mov_b32 s18, 4718592
Mva 40,204,132
s_mov_b32 s19, 0x20000
s_mov_b32 s20, s72
s_mov_b32 s21, s73
s_mov_b32 s22, 1152
Mva 48,204,136
TR 200,19,49152
s_mov_b32 s23, 0x20000
Mva 56,204,140
TR 202,19,57344
s_waitcnt lgkmcnt(4)
Mva 36,208,128
Mva 44,208,132
Mva 52,208,136
TR 204,20,49152
Mva 60,208,140
TR 206,20,57344
s_waitcnt lgkmcnt(5)
Mva 32,192,144
Mva 40,192,148
Mva 48,192,152
TR 208,19,50176
Mva 56,192,156
TR 210,19,58368
s_waitcnt lgkmcnt(6)
Mva 36,196,144
Mva 44,196,148
Mva 52,196,152
TR 192,20,50176
Mva 60,196,156
TR 194,20,58368
s_waitcnt vmcnt(7)
s_barrier
s_waitcnt lgkmcnt(6)
Mva 32,200,64
s_mov_b32 m0, s70
Mva 40,200,68
s_mov_b32 s15, 152192
buffer_load_dwordx4 v18, s[24:27], s15 offen offset:0 lds
Mva 48,200,72
TR 196,19,51200
Mva 56,200,76
TR 198,19,59392
s_waitcnt lgkmcnt(6)
Mva 36,204,64
s_mov_b32 s15, 188032
buffer_load_dwordx4 v18, s[24:27], s15 offen offset:1024 lds
Mva 44,204,68
Mva 52,204,72
TR 200,20,51200
Mva 60,204,76
s_mov_b32 s15, 223872
buffer_load_dwordx4 v18, s[24:27], s15 offen offset:2048 lds
s_waitcnt lgkmcnt(5)
Mva 32,208,80
TR 202,20,59392
Mva 40,208,84
Mva 48,208,88
s_mov_b32 s15, 259712
buffer_load_dwordx4 v18, s[24:27], s15 offen offset:3072 lds
Mva 56,208,92
TR 204,19,52224
s_waitcnt lgkmcnt(5)
Mva 36,192,80
TR 206,19,60416
Mva 44,192,84
Mva 52,192,88
TR 208,20,52224
Mva 60,192,92
TR 210,20,60416
s_waitcnt lgkmcnt(6)
Mva 32,196,160
Mva 40,196,164
Mva 48,196,168
TR 192,19,0
Mva 56,196,172
TR 194,19,8192
s_waitcnt lgkmcnt(6)
Mva 36,200,160
Mva 44,200,164
Mva 52,200,168
TR 196,20,0
Mva 60,200,172
TR 198,20,8192
s_waitcnt lgkmcnt(6)
Mva 32,204,176
Mva 40,204,180
Mva 48,204,184
TR 200,19,1024
Mva 56,204,188
TR 202,19,9216
s_waitcnt lgkmcnt(6)
Mva 36,208,176
Mva 44,208,180
Mva 52,208,184
TR 204,20,1024
Mva 60,208,188
TR 206,20,9216
s_waitcnt vmcnt(4)
s_barrier
s_waitcnt lgkmcnt(6)
Zva 64,192,0
s_mov_b32 m0, s71
Zva 72,192,4
s_mov_b32 s15, 4864
buffer_load_dwordx4 v18, s[24:27], s15 offen offset:0 lds
Zva 80,192,8
VC 32,32,33
VC 33,34,35
Zva 88,192,12
VC 34,36,37
VC 35,38,39
s_waitcnt lgkmcnt(4)
Zva 68,196,0
s_mov_b32 s15, 40704
buffer_load_dwordx4 v18, s[24:27], s15 offen offset:1024 lds
Zva 76,196,4
s_mov_b32 s67, 3072
buffer_store_dwordx4 v[32:35], v21, s[52:55], s67 offen sc1
Zva 84,196,8
TR 208,19,2048
VC 40,40,41
Zva 92,196,12
s_mov_b32 s15, 76544
buffer_load_dwordx4 v18, s[24:27], s15 offen offset:2048 lds
s_waitcnt lgkmcnt(3)
Mva 64,200,16
TR 210,19,10240
VC 41,42,43
Mva 72,200,20
VC 42,44,45
VC 43,46,47
Mva 80,200,24
s_mov_b32 s15, 112384
buffer_load_dwordx4 v18, s[24:27], s15 offen offset:3072 lds
Mva 88,200,28
TR 192,20,2048
s_waitcnt lgkmcnt(3)
Mva 68,204,16
TR 194,20,10240
Mva 76,204,20
s_mov_b32 s67, 12288
buffer_store_dwordx4 v[40:43], v21, s[52:55], s67 offen sc1
Mva 84,204,24
VC 48,48,49
VC 49,50,51
Mva 92,204,28
TR 196,19,3072
VC 50,52,53
s_waitcnt lgkmcnt(3)
Mva 64,208,32
TR 198,19,11264
VC 51,54,55
Mva 72,208,36
s_mov_b32 s67, 21504
buffer_store_dwordx4 v[48:51], v21, s[52:55], s67 offen sc1
Mva 80,208,40
TR 200,20,3072
VC 56,56,57
Mva 88,208,44
TR 202,20,11264
VC 57,58,59
s_waitcnt lgkmcnt(4)
Mva 68,192,32
VC 58,60,61
VC 59,62,63
Mva 76,192,36
ds_read_b128 v[204:207], v16 offset:16384
Mva 84,192,40
s_mov_b32 s67, 30720
buffer_store_dwordx4 v[56:59], v21, s[52:55], s67 offen sc1
Mva 92,192,44
ds_read_b128 v[208:211], v16 offset:20480
s_waitcnt lgkmcnt(4)
Mva 64,196,48
Mva 72,196,52
Mva 80,196,56
ds_read_b128 v[192:195], v17 offset:16384
Mva 88,196,60
VC 224,224,225
VC 225,226,227
s_waitcnt lgkmcnt(3)
Mva 68,200,48
VC 226,228,229
VC 227,230,231
Mva 76,200,52
buffer_load_dwordx2 v[52:53], v183, s[60:63], 0 offen sc1
Mva 84,200,56
s_mov_b32 s67, 4096
buffer_store_dwordx4 v[224:227], v21, s[52:55], s67 offen sc1
Mva 92,200,60
buffer_load_dwordx2 v[32:33], v184, s[48:51], 0 offen offset:0
s_waitcnt vmcnt(11)
s_barrier
s_waitcnt lgkmcnt(2)
Mva 64,204,96
s_mov_b32 m0, s68
VC 232,232,233
Mva 72,204,100
s_mov_b32 s15, 446464
buffer_load_dwordx4 v14, s[40:43], s15 offen offset:0 lds
Mva 80,204,104
VC 233,234,235
VC 234,236,237
Mva 88,204,108
buffer_load_dwordx2 v[42:43], v185, s[20:23], 0 offen offset:0
s_waitcnt lgkmcnt(1)
Mva 68,208,96
s_mov_b32 s15, 445440
buffer_load_dwordx4 v14, s[44:47], s15 offen offset:1024 lds
Mva 76,208,100
ds_read_b128 v[196:199], v17 offset:20480
VC 235,238,239
Mva 84,208,104
s_mov_b32 s67, 13312
buffer_store_dwordx4 v[232:235], v21, s[52:55], s67 offen sc1
Mva 92,208,108
buffer_load_dwordx2 v[34:35], v184, s[48:51], 0 offen offset:256
s_waitcnt lgkmcnt(1)
Mva 64,192,112
s_mov_b32 s15, 444416
buffer_load_dwordx4 v15, s[40:43], s15 offen offset:2048 lds
VC 240,240,241
Mva 72,192,116
ds_read_b128 v[200:203], v16 offset:17408
VC 241,242,243
Mva 80,192,120
VC 242,244,245
buffer_load_dwordx2 v[44:45], v185, s[20:23], 0 offen offset:256
Mva 88,192,124
s_mov_b32 s15, 443392
buffer_load_dwordx4 v15, s[44:47], s15 offen offset:3072 lds
VC 243,246,247
s_waitcnt lgkmcnt(1)
Mva 68,196,112
buffer_load_dwordx2 v[36:37], v184, s[48:51], 0 offen offset:512
Mva 76,196,116
ds_read_b128 v[204:207], v16 offset:21504
s_mov_b32 s67, 22528
buffer_store_dwordx4 v[240:243], v21, s[52:55], s67 offen sc1
Mva 84,196,120
VC 248,248,249
VC 249,250,251
Mva 92,196,124
buffer_load_dwordx2 v[46:47], v185, s[20:23], 0 offen offset:512
s_waitcnt lgkmcnt(1)
Mva 64,200,128
VC 250,252,253
ds_read_b128 v[208:211], v17 offset:17408
Mva 72,200,132
VC 251,254,255
ds_read_b128 v[192:195], v17 offset:21504
Mva 80,200,136
buffer_load_dwordx2 v[38:39], v184, s[48:51], 0 offen offset:768
Mva 88,200,140
s_mov_b32 s67, 31744
buffer_store_dwordx4 v[248:251], v21, s[52:55], s67 offen sc1
s_waitcnt lgkmcnt(2)
Mva 68,204,128
buffer_load_dwordx2 v[48:49], v185, s[20:23], 0 offen offset:768
Mva 76,204,132
buffer_load_dwordx2 v[40:41], v184, s[48:51], 0 offen offset:1024
Mva 84,204,136
TR 196,19,32768
Mva 92,204,140
TR 198,19,40960
s_waitcnt lgkmcnt(3)
Mva 64,208,144
buffer_load_dwordx2 v[50:51], v185, s[20:23], 0 offen offset:1024
Mva 72,208,148
TR 200,20,32768
Mva 80,208,152
TR 202,20,40960
Mva 88,208,156
TR 204,19,33792
s_waitcnt lgkmcnt(5)
Mva 68,192,144
TR 206,19,41984
Mva 76,192,148
Mva 84,192,152
TR 208,20,33792
Mva 92,192,156
TR 210,20,41984
s_waitcnt vmcnt(22)
s_barrier
s_waitcnt lgkmcnt(6)
Mva 64,196,64
s_mov_b32 m0, s69
Mva 72,196,68
s_mov_b32 s15, 152320
buffer_load_dwordx4 v18, s[24:27], s15 offen offset:0 lds
Mva 80,196,72
TR 192,19,34816
Mva 88,196,76
TR 194,19,43008
s_waitcnt lgkmcnt(6)
Mva 68,200,64
s_mov_b32 s15, 188160
buffer_load_dwordx4 v18, s[24:27], s15 offen offset:1024 lds
Mva 76,200,68
Mva 84,200,72
TR 196,20,34816
Mva 92,200,76
s_mov_b32 s15, 224000
buffer_load_dwordx4 v18, s[24:27], s15 offen offset:2048 lds
s_waitcnt lgkmcnt(5)
Mva 64,204,80
TR 198,20,43008
Mva 72,204,84
Mva 80,204,88
s_mov_b32 s15, 259840
buffer_load_dwordx4 v18, s[24:27], s15 offen offset:3072 lds
Mva 88,204,92
TR 200,19,35840
s_waitcnt lgkmcnt(5)
Mva 68,208,80
TR 202,19,44032
Mva 76,208,84
Mva 84,208,88
TR 204,20,35840
Mva 92,208,92
TR 206,20,44032
s_waitcnt lgkmcnt(6)
Mva 64,192,160
Mva 72,192,164
Mva 80,192,168
TR 208,19,49152
Mva 88,192,172
TR 210,19,57344
s_waitcnt lgkmcnt(6)
Mva 68,196,160
Mva 76,196,164
Mva 84,196,168
TR 192,20,49152
Mva 92,196,172
TR 194,20,57344
s_waitcnt lgkmcnt(6)
Mva 64,200,176
Mva 72,200,180
Mva 80,200,184
TR 196,19,50176
Mva 88,200,188
s_waitcnt vmcnt(23)
buffer_store_dwordx2 v[186:187], v191, s[56:59], 0 offen offset:0 sc1
s_waitcnt lgkmcnt(5)
Mva 68,204,176
TR 198,19,58368
Mva 76,204,180
Mva 84,204,184
TR 200,20,50176
Mva 92,204,188
TR 202,20,58368
s_waitcnt vmcnt(13)
s_barrier
s_waitcnt lgkmcnt(6)
Zva 96,208,0
s_mov_b32 m0, s70
Zva 104,208,4
s_mov_b32 s15, 4992
buffer_load_dwordx4 v18, s[24:27], s15 offen offset:0 lds
Zva 112,208,8
VC 64,64,65
VC 65,66,67
Zva 120,208,12
VC 66,68,69
VC 67,70,71
s_waitcnt lgkmcnt(4)
Zva 100,192,0
s_mov_b32 s15, 40832
buffer_load_dwordx4 v18, s[24:27], s15 offen offset:1024 lds
Zva 108,192,4
s_mov_b32 s67, 5120
buffer_store_dwordx4 v[64:67], v21, s[52:55], s67 offen sc1
Zva 116,192,8
TR 204,19,51200
VC 72,72,73
Zva 124,192,12
)"
R"(s_mov_b32 s15, 76672
buffer_load_dwordx4 v18, s[24:27], s15 offen offset:2048 lds
s_waitcnt lgkmcnt(3)
Mva 96,196,16
TR 206,19,59392
VC 73,74,75
Mva 104,196,20
VC 74,76,77
VC 75,78,79
Mva 112,196,24
s_mov_b32 s15, 112512
buffer_load_dwordx4 v18, s[24:27], s15 offen offset:3072 lds
Mva 120,196,28
TR 208,20,51200
s_waitcnt lgkmcnt(3)
Mva 100,200,16
TR 210,20,59392
Mva 108,200,20
s_mov_b32 s67, 14336
buffer_store_dwordx4 v[72:75], v21, s[52:55], s67 offen sc1
Mva 116,200,24
VC 80,80,81
VC 81,82,83
Mva 124,200,28
TR 192,19,52224
VC 82,84,85
s_waitcnt lgkmcnt(3)
Mva 96,204,32
TR 194,19,60416
VC 83,86,87
Mva 104,204,36
s_mov_b32 s67, 23552
buffer_store_dwordx4 v[80:83], v21, s[52:55], s67 offen sc1
Mva 112,204,40
TR 196,20,52224
VC 88,88,89
Mva 120,204,44
TR 198,20,60416
VC 89,90,91
s_waitcnt lgkmcnt(4)
Mva 100,208,32
VC 90,92,93
VC 91,94,95
Mva 108,208,36
ds_read_b128 v[200:203], v16 offset:0
Mva 116,208,40
s_mov_b32 s67, 32768
buffer_store_dwordx4 v[88:91], v21, s[52:55], s67 offen sc1
Mva 124,208,44
ds_read_b128 v[204:207], v16 offset:4096
s_waitcnt lgkmcnt(4)
Mva 96,192,48
Mva 104,192,52
Mva 112,192,56
ds_read_b128 v[208:211], v17 offset:0
Mva 120,192,60
s_waitcnt lgkmcnt(3)
Mva 100,196,48
v_rcp_f32 v53, v53
Mva 108,196,52
VM 52,52,53
v_lshlrev_b32 v160, 16, v32
Mva 116,196,56
ds_read_b128 v[192:195], v17 offset:4096
v_and_b32 v161, 0xffff0000, v32
Mva 124,196,60
v_lshlrev_b32 v56, 16, v42
v_and_b32 v42, 0xffff0000, v42
s_waitcnt vmcnt(9)
s_barrier
s_waitcnt lgkmcnt(3)
Mva 96,200,96
s_mov_b32 m0, s71
v_fma_f32 v32, v160, v53, -v52
Mva 104,200,100
s_mov_b32 s15, 520192
buffer_load_dwordx4 v14, s[40:43], s15 offen offset:0 lds
Mva 112,200,104
ds_read_b128 v[196:199], v16 offset:1024
v_fma_f32 v57, v161, v53, -v52
Mva 120,200,108
VF 160,32,56,160
VF 161,57,42,161
s_waitcnt lgkmcnt(3)
Mva 100,204,96
s_mov_b32 s15, 519168
buffer_load_dwordx4 v14, s[44:47], s15 offen offset:1024 lds
Mva 108,204,100
v_lshlrev_b32 v162, 16, v33
v_and_b32 v163, 0xffff0000, v33
Mva 116,204,104
ds_read_b128 v[200:203], v16 offset:5120
v_lshlrev_b32 v56, 16, v43
Mva 124,204,108
s_mov_b32 s15, 518144
buffer_load_dwordx4 v15, s[40:43], s15 offen offset:2048 lds
s_waitcnt lgkmcnt(3)
Mva 96,208,112
v_and_b32 v43, 0xffff0000, v43
v_fma_f32 v33, v162, v53, -v52
Mva 104,208,116
v_fma_f32 v57, v163, v53, -v52
VF 162,33,56,162
Mva 112,208,120
s_mov_b32 s15, 517120
buffer_load_dwordx4 v15, s[44:47], s15 offen offset:3072 lds
Mva 120,208,124
ds_read_b128 v[204:207], v17 offset:1024
VF 163,57,43,163
s_waitcnt lgkmcnt(3)
Mva 100,192,112
v_lshlrev_b32 v164, 16, v34
v_and_b32 v165, 0xffff0000, v34
Mva 108,192,116
v_lshlrev_b32 v56, 16, v44
v_and_b32 v44, 0xffff0000, v44
Mva 116,192,120
ds_read_b128 v[208:211], v17 offset:5120
v_fma_f32 v34, v164, v53, -v52
Mva 124,192,124
v_fma_f32 v57, v165, v53, -v52
VF 164,34,56,164
s_waitcnt lgkmcnt(3)
Mva 96,196,128
VF 165,57,44,165
v_lshlrev_b32 v166, 16, v35
Mva 104,196,132
v_and_b32 v167, 0xffff0000, v35
v_lshlrev_b32 v56, 16, v45
Mva 112,196,136
TR 192,19,16384
v_and_b32 v45, 0xffff0000, v45
Mva 120,196,140
TR 194,19,24576
v_fma_f32 v35, v166, v53, -v52
s_waitcnt lgkmcnt(4)
Mva 100,200,128
v_fma_f32 v57, v167, v53, -v52
VF 166,35,56,166
Mva 108,200,132
VF 167,57,45,167
v_lshlrev_b32 v168, 16, v36
Mva 116,200,136
TR 196,20,16384
v_and_b32 v169, 0xffff0000, v36
Mva 124,200,140
TR 198,20,24576
v_lshlrev_b32 v56, 16, v46
s_waitcnt lgkmcnt(5)
Mva 96,204,144
v_and_b32 v46, 0xffff0000, v46
v_fma_f32 v36, v168, v53, -v52
Mva 104,204,148
v_fma_f32 v57, v169, v53, -v52
VF 168,36,56,168
Mva 112,204,152
TR 200,19,17408
VF 169,57,46,169
Mva 120,204,156
TR 202,19,25600
v_lshlrev_b32 v170, 16, v37
s_waitcnt lgkmcnt(6)
Mva 100,208,144
v_and_b32 v171, 0xffff0000, v37
v_lshlrev_b32 v56, 16, v47
Mva 108,208,148
v_and_b32 v47, 0xffff0000, v47
v_fma_f32 v37, v170, v53, -v52
Mva 116,208,152
TR 204,20,17408
v_fma_f32 v57, v171, v53, -v52
Mva 124,208,156
TR 206,20,25600
VF 170,37,56,170
s_waitcnt vmcnt(7)
s_barrier
s_waitcnt lgkmcnt(6)
Mva 96,192,64
s_mov_b32 m0, s68
VF 171,57,47,171
Mva 104,192,68
s_mov_b32 s15, 152448
buffer_load_dwordx4 v18, s[24:27], s15 offen offset:0 lds
Mva 112,192,72
TR 208,19,18432
v_lshlrev_b32 v172, 16, v38
Mva 120,192,76
TR 210,19,26624
v_and_b32 v173, 0xffff0000, v38
s_waitcnt lgkmcnt(6)
Mva 100,196,64
s_mov_b32 s15, 188288
buffer_load_dwordx4 v18, s[24:27], s15 offen offset:1024 lds
Mva 108,196,68
v_lshlrev_b32 v56, 16, v48
v_and_b32 v48, 0xffff0000, v48
Mva 116,196,72
TR 192,20,18432
v_fma_f32 v38, v172, v53, -v52
Mva 124,196,76
s_mov_b32 s15, 224128
buffer_load_dwordx4 v18, s[24:27], s15 offen offset:2048 lds
s_waitcnt lgkmcnt(5)
Mva 96,200,80
TR 194,20,26624
v_fma_f32 v57, v173, v53, -v52
Mva 104,200,84
VF 172,38,56,172
VF 173,57,48,173
Mva 112,200,88
s_mov_b32 s15, 259968
buffer_load_dwordx4 v18, s[24:27], s15 offen offset:3072 lds
Mva 120,200,92
TR 196,19,19456
v_lshlrev_b32 v174, 16, v39
s_waitcnt lgkmcnt(5)
Mva 100,204,80
TR 198,19,27648
v_and_b32 v175, 0xffff0000, v39
Mva 108,204,84
v_lshlrev_b32 v56, 16, v49
v_and_b32 v49, 0xffff0000, v49
Mva 116,204,88
TR 200,20,19456
v_fma_f32 v39, v174, v53, -v52
Mva 124,204,92
TR 202,20,27648
v_fma_f32 v57, v175, v53, -v52
s_waitcnt lgkmcnt(6)
Mva 96,208,160
VF 174,39,56,174
VF 175,57,49,175
Mva 104,208,164
v_lshlrev_b32 v176, 16, v40
v_and_b32 v177, 0xffff0000, v40
Mva 112,208,168
TR 204,19,32768
v_lshlrev_b32 v56, 16, v50
Mva 120,208,172
TR 206,19,40960
v_and_b32 v50, 0xffff0000, v50
s_waitcnt lgkmcnt(6)
Mva 100,192,160
v_fma_f32 v40, v176, v53, -v52
v_fma_f32 v57, v177, v53, -v52
Mva 108,192,164
VF 176,40,56,176
VF 177,57,50,177
Mva 116,192,168
TR 208,20,32768
v_lshlrev_b32 v178, 16, v41
Mva 124,192,172
TR 210,20,40960
v_and_b32 v179, 0xffff0000, v41
s_waitcnt lgkmcnt(6)
Mva 96,196,176
v_lshlrev_b32 v56, 16, v51
v_and_b32 v51, 0xffff0000, v51
Mva 104,196,180
v_fma_f32 v41, v178, v53, -v52
v_fma_f32 v57, v179, v53, -v52
Mva 112,196,184
TR 192,19,33792
VF 178,41,56,178
Mva 120,196,188
s_waitcnt vmcnt(8)
buffer_store_dwordx2 v[186:187], v191, s[56:59], 0 offen offset:128 sc1
s_waitcnt lgkmcnt(5)
Mva 100,200,176
TR 194,19,41984
VF 179,57,51,179
Mva 108,200,180
v_and_b32 v56, 1, v0
v_lshlrev_b32 v56, 4, v56
Mva 116,200,184
TR 196,20,33792
v_mov_b32 v57, 0x3f80
Mva 124,200,188
TR 198,20,41984
v_lshlrev_b32 v57, v56, v57
s_waitcnt vmcnt(5)
s_barrier
s_waitcnt lgkmcnt(6)
Zva 128,204,0
s_mov_b32 m0, s69
v_bfe_u32 v56, v0, 1, 3
Zva 136,204,4
s_mov_b32 s15, 5120
buffer_load_dwordx4 v18, s[24:27], s15 offen offset:0 lds
Zva 144,204,8
VC 96,96,97
VC 97,98,99
Zva 152,204,12
VC 98,100,101
VC 99,102,103
s_waitcnt lgkmcnt(4)
Zva 132,208,0
s_mov_b32 s15, 40960
buffer_load_dwordx4 v18, s[24:27], s15 offen offset:1024 lds
Zva 140,208,4
s_mov_b32 s67, 6144
buffer_store_dwordx4 v[96:99], v21, s[52:55], s67 offen sc1
Zva 148,208,8
TR 200,19,34816
VC 104,104,105
Zva 156,208,12
s_mov_b32 s15, 76800
buffer_load_dwordx4 v18, s[24:27], s15 offen offset:2048 lds
s_waitcnt lgkmcnt(3)
Mva 128,192,16
TR 202,19,43008
VC 105,106,107
Mva 136,192,20
VC 106,108,109
VC 107,110,111
Mva 144,192,24
s_mov_b32 s15, 112640
buffer_load_dwordx4 v18, s[24:27], s15 offen offset:3072 lds
Mva 152,192,28
TR 204,20,34816
v_lshrrev_b32 v32, 2, v0
s_waitcnt lgkmcnt(3)
Mva 132,196,16
TR 206,20,43008
v_and_b32 v32, 4, v32
Mva 140,196,20
s_mov_b32 s67, 15360
buffer_store_dwordx4 v[104:107], v21, s[52:55], s67 offen sc1
Mva 148,196,24
VC 112,112,113
VC 113,114,115
Mva 156,196,28
TR 208,19,35840
VC 114,116,117
s_waitcnt lgkmcnt(3)
Mva 128,200,32
TR 210,19,44032
VC 115,118,119
Mva 136,200,36
s_mov_b32 s67, 24576
buffer_store_dwordx4 v[112:115], v21, s[52:55], s67 offen sc1
Mva 144,200,40
TR 192,20,35840
VC 120,120,121
Mva 152,200,44
TR 194,20,44032
VC 121,122,123
s_waitcnt lgkmcnt(4)
Mva 132,204,32
VC 122,124,125
VC 123,126,127
Mva 140,204,36
ds_read_b128 v[196:199], v16 offset:49152
v_xor_b32 v56, v56, v32
Mva 148,204,40
s_mov_b32 s67, 33792
buffer_store_dwordx4 v[120:123], v21, s[52:55], s67 offen sc1
Mva 156,204,44
ds_read_b128 v[200:203], v16 offset:53248
v_cmp_eq_u32 vcc, 0, v56
s_waitcnt lgkmcnt(4)
Mva 128,208,48
v_cndmask_b32 v32, 0, v57, vcc
v_cmp_eq_u32 vcc, 1, v56
Mva 136,208,52
s_nop 0
v_cndmask_b32 v33, 0, v57, vcc
v_cmp_eq_u32 vcc, 2, v56
Mva 144,208,56
ds_read_b128 v[204:207], v17 offset:49152
v_cndmask_b32 v34, 0, v57, vcc
Mva 152,208,60
v_cmp_eq_u32 vcc, 3, v56
s_nop 1
v_cndmask_b32 v35, 0, v57, vcc
s_waitcnt lgkmcnt(3)
Mva 132,192,48
Mva 140,192,52
Mva 148,192,56
ds_read_b128 v[208:211], v17 offset:53248
Mva 156,192,60
s_waitcnt vmcnt(9)
s_barrier
s_waitcnt lgkmcnt(3)
Mva 128,196,96
s_mov_b32 m0, s70
Mva 136,196,100
s_mov_b32 s15, 593920
buffer_load_dwordx4 v14, s[40:43], s15 offen offset:0 lds
Mva 144,196,104
ds_read_b128 v[192:195], v16 offset:50176
Mva 152,196,108
s_waitcnt lgkmcnt(3)
Mva 132,200,96
s_mov_b32 s15, 592896
buffer_load_dwordx4 v14, s[44:47], s15 offen offset:1024 lds
Mva 140,200,100
Mva 148,200,104
ds_read_b128 v[196:199], v16 offset:54272
Mva 156,200,108
s_mov_b32 s15, 591872
buffer_load_dwordx4 v15, s[40:43], s15 offen offset:2048 lds
s_waitcnt lgkmcnt(3)
Mva 128,204,112
Mva 136,204,116
Mva 144,204,120
s_mov_b32 s15, 590848
buffer_load_dwordx4 v15, s[44:47], s15 offen offset:3072 lds
Mva 152,204,124
ds_read_b128 v[200:203], v17 offset:50176
s_waitcnt lgkmcnt(3)
Mva 132,208,112
Mva 140,208,116
Mva 148,208,120
ds_read_b128 v[204:207], v17 offset:54272
Mva 156,208,124
s_waitcnt lgkmcnt(3)
Mva 128,192,128
Mva 136,192,132
Mva 144,192,136
TR 208,19,0
Mva 152,192,140
TR 210,19,8192
s_waitcnt lgkmcnt(4)
Mva 132,196,128
Mva 140,196,132
Mva 148,196,136
TR 192,20,0
Mva 156,196,140
TR 194,20,8192
s_waitcnt lgkmcnt(5)
Mva 128,200,144
Mva 136,200,148
Mva 144,200,152
TR 196,19,1024
Mva 152,200,156
TR 198,19,9216
s_waitcnt lgkmcnt(6)
Mva 132,204,144
Mva 140,204,148
Mva 148,204,152
TR 200,20,1024
Mva 156,204,156
TR 202,20,9216
s_waitcnt vmcnt(7)
s_barrier
s_waitcnt lgkmcnt(6)
Mva 128,208,64
s_mov_b32 m0, s71
Mva 136,208,68
s_mov_b32 s15, 152576
buffer_load_dwordx4 v18, s[24:27], s15 offen offset:0 lds
Mva 144,208,72
TR 204,19,2048
Mva 152,208,76
TR 206,19,10240
s_waitcnt lgkmcnt(6)
Mva 132,192,64
s_mov_b32 s15, 188416
buffer_load_dwordx4 v18, s[24:27], s15 offen offset:1024 lds
Mva 140,192,68
Mva 148,192,72
TR 208,20,2048
Mva 156,192,76
s_mov_b32 s15, 224256
buffer_load_dwordx4 v18, s[24:27], s15 offen offset:2048 lds
s_waitcnt lgkmcnt(5)
Mva 128,196,80
TR 210,20,10240
Mva 136,196,84
Mva 144,196,88
s_mov_b32 s15, 260096
buffer_load_dwordx4 v18, s[24:27], s15 offen offset:3072 lds
Mva 152,196,92
TR 192,19,3072
s_waitcnt lgkmcnt(5)
Mva 132,200,80
TR 194,19,11264
Mva 140,200,84
Mva 148,200,88
TR 196,20,3072
Mva 156,200,92
TR 198,20,11264
s_waitcnt lgkmcnt(6)
Mva 128,204,160
Mva 136,204,164
Mva 144,204,168
buffer_load_dwordx2 v[180:181], v190, s[56:59], 0 offen sc1
Mva 152,204,172
TR 200,19,16384
s_waitcnt lgkmcnt(5)
Mva 132,208,160
TR 202,19,24576
Mva 140,208,164
Mva 148,208,168
TR 204,20,16384
Mva 156,208,172
TR 206,20,24576
s_waitcnt lgkmcnt(6)
Mva 128,192,176
Mva 136,192,180
Mva 144,192,184
TR 208,19,17408
Mva 152,192,188
TR 210,19,25600
s_waitcnt lgkmcnt(6)
Mva 132,196,176
Mva 140,196,180
Mva 148,196,184
TR 192,20,17408
Mva 156,196,188
TR 194,20,25600
s_waitcnt vmcnt(5)
s_barrier
s_waitcnt lgkmcnt(6)
Zva 224,200,0
Zva 232,200,4
Zva 240,200,8
VC 128,128,129
VC 129,130,131
Zva 248,200,12
VC 130,132,133
VC 131,134,135
s_waitcnt lgkmcnt(4)
Zva 228,204,0
s_mov_b32 s67, 7168
buffer_store_dwordx4 v[128:131], v21, s[52:55], s67 offen sc1
Zva 236,204,4
TR 196,19,18432
VC 136,136,137
Zva 244,204,8
TR 198,19,26624
VC 137,138,139
Zva 252,204,12
VC 138,140,141
VC 139,142,143
s_waitcnt lgkmcnt(4)
Mva 224,208,16
TR 200,20,18432
Mva 232,208,20
TR 202,20,26624
Mva 240,208,24
s_mov_b32 s67, 16384
buffer_store_dwordx4 v[136:139], v21, s[52:55], s67 offen sc1
Mva 248,208,28
VC 144,144,145
VC 145,146,147
s_waitcnt lgkmcnt(4)
Mva 228,192,16
TR 204,19,19456
VC 146,148,149
Mva 236,192,20
TR 206,19,27648
VC 147,150,151
Mva 244,192,24
s_mov_b32 s67, 25600
buffer_store_dwordx4 v[144:147], v21, s[52:55], s67 offen sc1
Mva 252,192,28
TR 208,20,19456
VC 152,152,153
s_waitcnt lgkmcnt(5)
Mva 224,196,32
s_waitcnt vmcnt(3)
v_cmp_eq_u32 vcc, s100, v180
v_cmp_eq_u32 s[76:77], s100, v181
s_nop 0
s_and_b64 s[76:77], s[76:77], vcc
s_andn2_b32 s76, 0xffff, s76
s_cselect_b32 s38, 0, s54
s_mov_b64 s[36:37], s[52:53]
s_mov_b32 s39, s55
Mva 232,196,36
TR 210,20,27648
VC 153,154,155
Mva 240,196,40
s_mov_b32 s67, 0
buffer_load_dwordx4 a[192:195], v188, s[36:39], s67 offen sc1
Mva 248,196,44
VC 154,156,157
VC 155,158,159
s_waitcnt lgkmcnt(4)
Mva 228,200,32
ds_read_b128 v[192:195], v16 offset:32768
Mva 236,200,36
s_mov_b32 s67, 34816
buffer_store_dwordx4 v[152:155], v21, s[52:55], s67 offen sc1
Mva 244,200,40
s_mov_b32 s67, 294912
buffer_load_dwordx4 a[196:199], v188, s[36:39], s67 offen sc1
Mva 252,200,44
ds_read_b128 v[196:199], v16 offset:36864
s_waitcnt lgkmcnt(4)
Mva 224,204,48
Mva 232,204,52
Mva 240,204,56
s_mov_b32 s67, 589824
buffer_load_dwordx4 a[200:203], v188, s[36:39], s67 offen sc1
Mva 248,204,60
ds_read_b128 v[200:203], v17 offset:32768
s_waitcnt lgkmcnt(3)
)"
R"(Mva 228,208,48
Mva 236,208,52
Mva 244,208,56
s_mov_b32 s67, 884736
buffer_load_dwordx4 a[204:207], v188, s[36:39], s67 offen sc1
Mva 252,208,60
ds_read_b128 v[204:207], v17 offset:36864
s_barrier
s_waitcnt lgkmcnt(3)
Mva 224,192,96
Mva 232,192,100
Mva 240,192,104
s_mov_b32 s67, 1179648
buffer_load_dwordx4 a[208:211], v188, s[36:39], s67 offen sc1
Mva 248,192,108
ds_read_b128 v[208:211], v16 offset:33792
s_waitcnt lgkmcnt(3)
Mva 228,196,96
Mva 236,196,100
Mva 244,196,104
s_mov_b32 s67, 1474560
buffer_load_dwordx4 a[212:215], v188, s[36:39], s67 offen sc1
Mva 252,196,108
ds_read_b128 v[192:195], v16 offset:37888
s_waitcnt lgkmcnt(3)
Mva 224,200,112
Mva 232,200,116
Mva 240,200,120
s_mov_b32 s67, 1769472
buffer_load_dwordx4 a[216:219], v188, s[36:39], s67 offen sc1
Mva 248,200,124
ds_read_b128 v[196:199], v17 offset:33792
s_waitcnt lgkmcnt(3)
Mva 228,204,112
Mva 236,204,116
Mva 244,204,120
s_mov_b32 s67, 2064384
buffer_load_dwordx4 a[220:223], v188, s[36:39], s67 offen sc1
Mva 252,204,124
ds_read_b128 v[200:203], v17 offset:37888
s_waitcnt lgkmcnt(3)
Mva 224,208,128
Mva 232,208,132
Mva 240,208,136
s_mov_b32 s67, 2048
buffer_load_dwordx4 a[224:227], v188, s[36:39], s67 offen sc1
Mva 248,208,140
TR 204,19,49152
s_waitcnt lgkmcnt(3)
Mva 228,192,128
TR 206,19,57344
Mva 236,192,132
Mva 244,192,136
s_mov_b32 s67, 296960
buffer_load_dwordx4 a[228:231], v188, s[36:39], s67 offen sc1
Mva 252,192,140
TR 208,20,49152
s_waitcnt lgkmcnt(4)
Mva 224,196,144
buffer_load_dwordx2 v[54:55], v190, s[56:59], 0 offen sc1
Mva 232,196,148
TR 210,20,57344
Mva 240,196,152
s_mov_b32 s67, 591872
buffer_load_dwordx4 a[232:235], v188, s[36:39], s67 offen sc1
Mva 248,196,156
TR 192,19,50176
s_waitcnt lgkmcnt(5)
Mva 228,200,144
TR 194,19,58368
Mva 236,200,148
Mva 244,200,152
s_mov_b32 s67, 886784
buffer_load_dwordx4 a[236:239], v188, s[36:39], s67 offen sc1
Mva 252,200,156
TR 196,20,50176
s_waitcnt lgkmcnt(5)
Mva 224,204,64
TR 198,20,58368
Mva 232,204,68
Mva 240,204,72
s_mov_b32 s67, 1181696
buffer_load_dwordx4 a[240:243], v188, s[36:39], s67 offen sc1
Mva 248,204,76
TR 200,19,51200
s_waitcnt lgkmcnt(5)
Mva 228,208,64
TR 202,19,59392
Mva 236,208,68
Mva 244,208,72
s_mov_b32 s67, 1476608
buffer_load_dwordx4 a[244:247], v188, s[36:39], s67 offen sc1
Mva 252,208,76
TR 204,20,51200
s_waitcnt lgkmcnt(5)
Mva 224,192,80
TR 206,20,59392
Mva 232,192,84
Mva 240,192,88
s_mov_b32 s67, 1771520
buffer_load_dwordx4 a[248:251], v188, s[36:39], s67 offen sc1
Mva 248,192,92
TR 208,19,52224
s_waitcnt lgkmcnt(5)
Mva 228,196,80
TR 210,19,60416
Mva 236,196,84
Mva 244,196,88
s_mov_b32 s67, 2066432
buffer_load_dwordx4 a[252:255], v188, s[36:39], s67 offen sc1
Mva 252,196,92
TR 192,20,52224
s_waitcnt lgkmcnt(5)
Mva 224,200,160
TR 194,20,60416
Mva 232,200,164
Mva 240,200,168
Mva 248,200,172
s_waitcnt lgkmcnt(4)
Mva 228,204,160
Mva 236,204,164
Mva 244,204,168
Mva 252,204,172
s_waitcnt lgkmcnt(2)
Mva 224,208,176
Mva 232,208,180
Mva 240,208,184
Mva 248,208,188
s_waitcnt lgkmcnt(0)
Mva 228,192,176
Mva 236,192,180
Mva 244,192,184
Mva 252,192,188
VC 224,224,225
VC 225,226,227
s_nop 2
VC 226,228,229
VC 227,230,231
s_mov_b32 s67, 8192
buffer_store_dwordx4 v[224:227], v21, s[52:55], s67 offen sc1
VC 232,232,233
VC 233,234,235
VC 234,236,237
VC 235,238,239
s_mov_b32 s67, 17408
buffer_store_dwordx4 v[232:235], v21, s[52:55], s67 offen sc1
VC 240,240,241
VC 241,242,243
VC 242,244,245
VC 243,246,247
s_mov_b32 s67, 26624
buffer_store_dwordx4 v[240:243], v21, s[52:55], s67 offen sc1
VC 248,248,249
VC 249,250,251
VC 250,252,253
VC 251,254,255
s_mov_b32 s67, 35840
buffer_store_dwordx4 v[248:251], v21, s[52:55], s67 offen sc1
s_nop 0
s_waitcnt vmcnt(20)
buffer_store_dwordx2 v[186:187], v191, s[56:59], 0 offen offset:256 sc1
s_cmp_eq_u32 s38, 0
s_cbranch_scc1 LEA
LFA:
LCA1:
s_waitcnt vmcnt(11)
v_cmp_eq_u32 vcc, s100, v54
v_cmp_eq_u32 s[76:77], s100, v55
s_nop 0
s_and_b64 s[76:77], s[76:77], vcc
s_andn2_b32 s76, 0xffff0000, s76
s_cbranch_scc1 LRA1
LGA1:
s_mov_b32 s67, 4096
buffer_load_dwordx4 a[0:3], v188, s[52:55], s67 offen sc1
s_mov_b32 s67, 299008
buffer_load_dwordx4 a[4:7], v188, s[52:55], s67 offen sc1
s_mov_b32 s67, 593920
buffer_load_dwordx4 a[8:11], v188, s[52:55], s67 offen sc1
s_mov_b32 s67, 888832
buffer_load_dwordx4 a[12:15], v188, s[52:55], s67 offen sc1
s_mov_b32 s67, 1183744
buffer_load_dwordx4 a[16:19], v188, s[52:55], s67 offen sc1
s_mov_b32 s67, 1478656
buffer_load_dwordx4 a[20:23], v188, s[52:55], s67 offen sc1
s_mov_b32 s67, 1773568
buffer_load_dwordx4 a[24:27], v188, s[52:55], s67 offen sc1
s_mov_b32 s67, 2068480
buffer_load_dwordx4 a[28:31], v188, s[52:55], s67 offen sc1
s_waitcnt vmcnt(9)
buffer_store_dwordx2 v[186:187], v191, s[56:59], 0 offen offset:384 sc1
buffer_load_dwordx2 v[54:55], v190, s[56:59], 0 offen sc1
Mva 160,32,192
Mva 164,32,224
Mva 160,32,196
Mva 164,32,228
Mva 160,32,200
Mva 164,32,232
Mva 160,32,204
Mva 164,32,236
Mva 160,32,208
Mva 164,32,240
Mva 160,32,212
Mva 164,32,244
Mva 160,32,216
Mva 164,32,248
Mva 160,32,220
Mva 164,32,252
s_nop 6
buffer_store_dwordx4 v[160:163], v189, s[16:19], 0 offen offset:0 sc1
buffer_store_dwordx4 v[164:167], v189, s[16:19], 0 offen offset:512 sc1
LCA2:
s_waitcnt vmcnt(2)
v_cmp_eq_u32 vcc, s100, v54
v_cmp_eq_u32 s[76:77], s100, v55
s_nop 0
s_and_b64 s[76:77], s[76:77], vcc
s_andn2_b32 s76, 0xffff, s77
s_cbranch_scc1 LRA2
LGA2:
buffer_load_dwordx2 v[54:55], v190, s[56:59], 0 offen sc1
s_mov_b32 s67, 6144
buffer_load_dwordx4 a[32:35], v188, s[52:55], s67 offen sc1
s_mov_b32 s67, 301056
buffer_load_dwordx4 a[36:39], v188, s[52:55], s67 offen sc1
s_mov_b32 s67, 595968
buffer_load_dwordx4 a[40:43], v188, s[52:55], s67 offen sc1
s_mov_b32 s67, 890880
buffer_load_dwordx4 a[44:47], v188, s[52:55], s67 offen sc1
s_mov_b32 s67, 1185792
buffer_load_dwordx4 a[48:51], v188, s[52:55], s67 offen sc1
s_mov_b32 s67, 1480704
buffer_load_dwordx4 a[52:55], v188, s[52:55], s67 offen sc1
s_mov_b32 s67, 1775616
buffer_load_dwordx4 a[56:59], v188, s[52:55], s67 offen sc1
s_mov_b32 s67, 2070528
buffer_load_dwordx4 a[60:63], v188, s[52:55], s67 offen sc1
Mva 168,32,0
Mva 168,32,4
Mva 168,32,8
Mva 168,32,12
Mva 168,32,16
Mva 168,32,20
Mva 168,32,24
Mva 168,32,28
s_nop 7
buffer_store_dwordx4 v[168:171], v189, s[16:19], 0 offen offset:1024 sc1
LCA3:
s_waitcnt vmcnt(9)
v_cmp_eq_u32 vcc, s100, v54
v_cmp_eq_u32 s[76:77], s100, v55
s_nop 0
s_and_b64 s[76:77], s[76:77], vcc
s_andn2_b32 s76, 0xffff0000, s77
s_cbranch_scc1 LRA3
LGA3:
s_cmp_lg_u32 s65, 0
s_cselect_b32 s54, 0, s54
s_mov_b32 s67, 8192
buffer_load_dwordx4 a[64:67], v188, s[52:55], s67 offen sc1
s_mov_b32 s67, 303104
buffer_load_dwordx4 a[68:71], v188, s[52:55], s67 offen sc1
s_mov_b32 s67, 598016
buffer_load_dwordx4 a[72:75], v188, s[52:55], s67 offen sc1
s_mov_b32 s67, 892928
buffer_load_dwordx4 a[76:79], v188, s[52:55], s67 offen sc1
s_mov_b32 s67, 1187840
buffer_load_dwordx4 a[80:83], v188, s[52:55], s67 offen sc1
s_mov_b32 s67, 1482752
buffer_load_dwordx4 a[84:87], v188, s[52:55], s67 offen sc1
s_mov_b32 s67, 1777664
buffer_load_dwordx4 a[88:91], v188, s[52:55], s67 offen sc1
s_mov_b32 s67, 2072576
buffer_load_dwordx4 a[92:95], v188, s[52:55], s67 offen sc1
s_waitcnt vmcnt(16)
Mva 172,32,32
s_waitcnt vmcnt(15)
Mva 172,32,36
s_waitcnt vmcnt(14)
Mva 172,32,40
s_waitcnt vmcnt(13)
Mva 172,32,44
s_waitcnt vmcnt(12)
Mva 172,32,48
s_waitcnt vmcnt(11)
Mva 172,32,52
s_waitcnt vmcnt(10)
Mva 172,32,56
s_waitcnt vmcnt(9)
Mva 172,32,60
s_nop 7
buffer_store_dwordx4 v[172:175], v189, s[16:19], 0 offen offset:1536 sc1
s_waitcnt vmcnt(8)
Mva 176,32,64
s_waitcnt vmcnt(7)
Mva 176,32,68
s_waitcnt vmcnt(6)
Mva 176,32,72
s_waitcnt vmcnt(5)
Mva 176,32,76
s_waitcnt vmcnt(4)
Mva 176,32,80
s_waitcnt vmcnt(3)
Mva 176,32,84
s_waitcnt vmcnt(2)
Mva 176,32,88
s_waitcnt vmcnt(1)
Mva 176,32,92
s_cmp_lg_u32 s65, 0
s_cselect_b32 s18, 0, s18
s_nop 5
buffer_store_dwordx4 v[176:179], v189, s[16:19], 0 offen offset:2048 sc1
s_lshl_b32 s90, s14, 7
s_add_u32 s90, s90, 196608
s_add_u32 s91, s13, s11
s_cmp_eq_u32 s91, 0
s_cselect_b32 s90, s90, 0x80000000
v_mov_b32 v1, s90
buffer_store_dword v186, v1, s[56:59], 0 offen sc1
s_endpgm
LRETRY:
s_sleep 4
buffer_load_dwordx4 v[96:99], v212, s[56:59], 0 offen offset:0 sc1
buffer_load_dwordx4 v[100:103], v212, s[56:59], 0 offen offset:16 sc1
buffer_load_dwordx4 v[104:107], v213, s[56:59], 0 offen offset:0 sc1
buffer_load_dwordx4 v[108:111], v213, s[56:59], 0 offen offset:16 sc1
s_waitcnt vmcnt(0)
s_sub_u32 s92, s92, 1
s_cbranch_scc1 LGO
s_branch LCHK
LRA0:
s_sleep 8
buffer_load_dwordx2 v[54:55], v190, s[56:59], 0 offen sc1
s_waitcnt vmcnt(0)
s_sub_u32 s92, s92, 1
s_cbranch_scc1 LGA0
s_branch LCA0
LEA:
s_waitcnt vmcnt(0)
buffer_store_dwordx2 v[186:187], v191, s[56:59], 0 offen offset:384 sc1
LCA0:
v_cmp_eq_u32 vcc, s100, v54
v_cmp_eq_u32 s[76:77], s100, v55
s_nop 0
s_and_b64 s[76:77], s[76:77], vcc
s_andn2_b32 s76, 0xffff, s76
s_cbranch_scc1 LRA0
LGA0:
s_mov_b32 s67, 0
buffer_load_dwordx4 a[192:195], v188, s[52:55], s67 offen sc1
s_mov_b32 s67, 294912
buffer_load_dwordx4 a[196:199], v188, s[52:55], s67 offen sc1
s_mov_b32 s67, 589824
buffer_load_dwordx4 a[200:203], v188, s[52:55], s67 offen sc1
s_mov_b32 s67, 884736
buffer_load_dwordx4 a[204:207], v188, s[52:55], s67 offen sc1
s_mov_b32 s67, 1179648
buffer_load_dwordx4 a[208:211], v188, s[52:55], s67 offen sc1
s_mov_b32 s67, 1474560
buffer_load_dwordx4 a[212:215], v188, s[52:55], s67 offen sc1
s_mov_b32 s67, 1769472
buffer_load_dwordx4 a[216:219], v188, s[52:55], s67 offen sc1
s_mov_b32 s67, 2064384
buffer_load_dwordx4 a[220:223], v188, s[52:55], s67 offen sc1
s_mov_b32 s67, 2048
buffer_load_dwordx4 a[224:227], v188, s[52:55], s67 offen sc1
s_mov_b32 s67, 296960
buffer_load_dwordx4 a[228:231], v188, s[52:55], s67 offen sc1
s_mov_b32 s67, 591872
buffer_load_dwordx4 a[232:235], v188, s[52:55], s67 offen sc1
s_mov_b32 s67, 886784
buffer_load_dwordx4 a[236:239], v188, s[52:55], s67 offen sc1
s_mov_b32 s67, 1181696
buffer_load_dwordx4 a[240:243], v188, s[52:55], s67 offen sc1
s_mov_b32 s67, 1476608
buffer_load_dwordx4 a[244:247], v188, s[52:55], s67 offen sc1
s_mov_b32 s67, 1771520
buffer_load_dwordx4 a[248:251], v188, s[52:55], s67 offen sc1
s_mov_b32 s67, 2066432
buffer_load_dwordx4 a[252:255], v188, s[52:55], s67 offen sc1
buffer_load_dwordx2 v[54:55], v190, s[56:59], 0 offen sc1
s_waitcnt vmcnt(0)
s_branch LFA
LRA1:
s_sleep 4
buffer_load_dwordx2 v[54:55], v190, s[56:59], 0 offen sc1
s_waitcnt vmcnt(0)
s_sub_u32 s92, s92, 1
s_cbranch_scc1 LGA1
s_branch LCA1
LRA2:
s_sleep 2
buffer_load_dwordx2 v[54:55], v190, s[56:59], 0 offen sc1
s_waitcnt vmcnt(0)
s_sub_u32 s92, s92, 1
s_cbranch_scc1 LGA2
s_branch LCA2
LRA3:
s_sleep 1
buffer_load_dwordx2 v[54:55], v190, s[56:59], 0 offen sc1
s_waitcnt vmcnt(0)
s_sub_u32 s92, s92, 1
s_cbranch_scc1 LGA3
s_branch LCA3
LENDA:
PB5:
v_and_b32 v0, 63, v0
s_mov_b32 m0, 161792
s_and_b32 s72, s14, 3
s_lshl_b32 s72, s72, 2
s_add_u32 s72, s72, s11
s_mov_b32 s7, 0x20000
s_getpc_b64 s[4:5]
LPCB5:
s_mov_b32 s6, LENDB5-LPCB5
s_lshl_b32 s73, s72, 13
v_lshl_add_u32 v47, v0, 7, s73
buffer_load_dword v47, s[4:7], 0 offen lds
v_and_b32 v32, 15, v0
v_lshrrev_b32 v33, 4, v0
v_lshrrev_b32 v34, 2, v32
v_and_b32 v35, 3, v0
s_and_b32 s64, s11, 1
s_lshr_b32 s65, s11, 1
s_lshl_b32 s66, s64, 6
s_lshl_b32 s67, s65, 3
s_add_u32 s66, s66, s67
v_lshl_add_u32 v36, v33, 4, v34
v_add_u32 v36, s66, v36
s_lshl_b32 s67, s14, 7
v_add_u32 v37, s67, v36
v_mul_u32_u24 v38, 1152, v37
v_lshl_add_u32 v38, v35, 4, v38
v_add_u32 v39, 4608, v38
s_waitcnt lgkmcnt(0)
s_mov_b32 s48, s76
s_mov_b32 s49, s77
s_mov_b32 s50, 2359296
s_mov_b32 s51, 0x20000
buffer_load_dwordx4 a[0:3], v38, s[48:51], 0 offen offset:0
buffer_load_dwordx4 a[4:7], v39, s[48:51], 0 offen offset:0
buffer_load_dwordx4 a[16:19], v38, s[48:51], 0 offen offset:64
buffer_load_dwordx4 a[20:23], v39, s[48:51], 0 offen offset:64
buffer_load_dwordx4 a[32:35], v38, s[48:51], 0 offen offset:128
buffer_load_dwordx4 a[36:39], v39, s[48:51], 0 offen offset:128
buffer_load_dwordx4 a[48:51], v38, s[48:51], 0 offen offset:192
buffer_load_dwordx4 a[52:55], v39, s[48:51], 0 offen offset:192
buffer_load_dwordx4 a[64:67], v38, s[48:51], 0 offen offset:256
buffer_load_dwordx4 a[68:71], v39, s[48:51], 0 offen offset:256
buffer_load_dwordx4 a[80:83], v38, s[48:51], 0 offen offset:320
buffer_load_dwordx4 a[84:87], v39, s[48:51], 0 offen offset:320
s_mul_i32 s74, s13, 5
s_sub_u32 s74, s74, 9
s_lshl_b32 s75, s74, 7
s_add_u32 s16, s92, s75
s_addc_u32 s17, s93, 0
s_sub_u32 s16, s16, 0x1000
s_subb_u32 s17, s17, 0
s_sub_u32 s18, 5312512, s75
s_mov_b32 s19, 0x20000
s_add_u32 s20, s94, s75
s_addc_u32 s21, s95, 0
s_sub_u32 s20, s20, 0x1000
s_subb_u32 s21, s21, 0
s_sub_u32 s22, 5312512, s75
s_mov_b32 s23, 0x20000
s_mul_i32 s75, s74, 73728
s_add_u32 s24, s96, s75
s_addc_u32 s25, s97, 0
s_sub_u32 s24, s24, 0x1000
s_subb_u32 s25, s25, 0
s_sub_u32 s26, 5312512, s75
s_mov_b32 s27, 0x20000
v_lshrrev_b32 v40, 3, v0
v_and_b32 v41, 7, v0
v_and_b32 v42, 1, v0
v_lshlrev_b32 v42, 4, v42
v_lshrrev_b32 v43, 1, v41
s_lshl_b32 s67, s11, 3
v_add_u32 v44, s67, v40
s_lshl_b32 s68, s64, 1
v_and_b32 v45, 1, v33
v_or_b32 v46, s68, v45
v_xor_b32 v46, v46, v43
v_lshl_add_u32 v46, v46, 5, v42
v_mul_u32_u24 v1, 9216, v44
v_add_u32 v1, v1, v46
v_xor_b32 v46, v33, v43
v_lshl_add_u32 v46, v46, 5, v42
v_mul_u32_u24 v18, 1152, v44
v_add_u32 v18, v18, v46
v_add_u32 v44, s67, v44
v_xor_b32 v46, v33, v41
v_mul_u32_u24 v14, 1152, v44
v_lshl_add_u32 v14, v46, 4, v14
v_xor_b32 v15, 64, v14
v_add_u32 v15, 9216, v15
s_lshl_b32 s68, s11, 12
s_add_u32 s69, s68, 0x4000
s_add_u32 s70, s68, 0x8000
s_add_u32 s71, s68, 0xc000
v_lshrrev_b32 v46, 1, v34
v_lshl_or_b32 v46, v45, 1, v46
s_lshl_b32 s67, s65, 1
v_xor_b32 v46, s67, v46
v_lshlrev_b32 v46, 5, v46
v_lshl_add_u32 v46, v35, 3, v46
v_lshl_add_u32 v46, v34, 7, v46
v_lshl_add_u32 v2, v33, 12, v46
v_xor_b32 v3, 32, v2
v_lshl_add_u32 v19, v45, 9, v46
v_lshrrev_b32 v47, 1, v33
v_lshl_add_u32 v19, v47, 12, v19
v_xor_b32 v20, 32, v19
v_lshrrev_b32 v46, 1, v32
v_xor_b32 v46, v46, v33
v_lshlrev_b32 v46, 4, v46
v_and_b32 v47, 7, v32
v_lshl_add_u32 v46, v47, 7, v46
v_lshrrev_b32 v47, 3, v32
v_lshl_add_u32 v46, v47, 11, v46
s_lshl_b32 s67, s65, 13
v_add_u32 v16, s67, v46
v_xor_b32 v17, 64, v16
s_lshl_b32 s67, s64, 13
s_add_u32 s67, s67, 0x10000
v_lshl_add_u32 v4, v0, 4, s67
s_lshl_b32 s67, s65, 12
v_add_u32 v5, s67, v4
s_lshl_b32 s67, s64, 13
s_add_u32 s67, s67, 0x10000
v_and_b32 v46, 2, v33
v_lshl_or_b32 v46, v33, 2, v46
v_xor_b32 v46, v46, v0
v_lshl_add_u32 v157, v46, 4, s67
v_and_b32 v46, 2, v35
v_lshl_or_b32 v46, v35, 2, v46
v_lshrrev_b32 v47, 2, v0
v_xor_b32 v46, v46, v47
v_lshl_add_u32 v46, v35, 4, v46
s_lshl_b32 s72, s65, 11
s_add_u32 s67, s67, s72
v_lshl_add_u32 v156, v46, 4, s67
s_lshl_b32 s67, s14, 4
s_add_u32 s67, s67, s13
s_lshl_b32 s67, s67, 2
s_add_u32 s67, s67, s11
s_mul_i32 s67, s67, 36864
v_lshl_add_u32 v21, v0, 4, s67
s_add_u32 s52, s98, s67
s_addc_u32 s53, s99, 0x800000
s_mov_b32 s54, 64
s_mov_b32 s55, 0x20000
s_add_u32 s56, s98, 42991616
s_addc_u32 s57, s99, 0
s_mov_b32 s58, 262144
s_mov_b32 s59, 0x20000
s_lshl_b32 s67, s10, 10
s_add_u32 s67, s67, 41943040
s_add_u32 s60, s98, s67
s_addc_u32 s61, s99, 0
s_mov_b32 s62, 1024
s_mov_b32 s63, 0x20000
s_lshl_b32 s67, s14, 7
s_add_u32 s67, s67, 196608
s_load_dword s100, s[56:57], s67
s_lshl_b32 s67, s13, 7
s_add_u32 s67, s67, 200704
s_load_dword s101, s[56:57], s67
v_mov_b32 v96, 0x3f803f80
v_mov_b32 v97, 0x3f803f80
v_mov_b32 v98, 0x3f803f80
v_mov_b32 v99, 0x3f803f80
v_lshlrev_b32 v46, v42, v96
v_and_b32 v47, 0xffff, v96
v_cmp_eq_u32 vcc, 0, v42
s_nop 1
v_cndmask_b32 v46, v46, v47, vcc
v_cmp_eq_u32 vcc, v33, v34
s_nop 1
v_cndmask_b32 v46, 0, v46, vcc
v_cmp_gt_u32 vcc, 2, v35
v_mov_b32 v100, 0
v_mov_b32 v101, 0
v_cndmask_b32 v102, 0, v46, vcc
v_cndmask_b32 v103, v46, 0, vcc
v_mov_b32 v104, 0
v_mov_b32 v105, 0
v_cmp_eq_u32 s[72:73], 1, v35
v_cmp_eq_u32 s[74:75], 2, v35
v_cmp_eq_u32 s[76:77], 3, v35
s_mov_b32 m0, 161792
s_lshl_b32 s90, s14, 2
s_add_u32 s90, s90, s11
s_mov_b32 s7, 0x20000
s_add_u32 s91, s90, 90
s_mul_i32 s92, s91, 3641
s_lshr_b32 s92, s92, 15
s_mul_i32 s93, s92, 9
s_sub_u32 s93, s91, s93
)"
R"(s_bitcmp1_b32 s92, 0
s_cselect_b32 s4, s20, s16
s_cselect_b32 s5, s21, s17
s_cselect_b32 s32, s44, s40
s_cselect_b32 s33, s45, s41
s_lshr_b32 s92, s92, 1
s_lshl_b32 s92, s92, 7
s_mul_i32 s34, s93, 589824
s_add_u32 s92, s92, s34
s_or_b32 s5, s5, 603979776
s_or_b32 s33, s33, 75497472
s_mul_i32 s34, s93, 73728
s_sub_u32 s93, s91, 90
s_lshl_b32 s93, s93, 13
s_or_b32 s35, s25, 0x800000
s_cmp_ge_u32 s91, 90
s_cselect_b32 s4, s24, s4
s_cselect_b32 s5, s35, s5
s_cselect_b32 s92, s93, s92
s_add_u32 s92, s92, 0x1000
s_cmp_ge_u32 s91, 135
s_cselect_b32 s6, 0, 64
s_cselect_b32 s92, 0, s92
buffer_load_dwordx4 a[96:99], v38, s[48:51], 0 offen offset:384
buffer_load_dwordx4 a[100:103], v39, s[48:51], 0 offen offset:384
s_waitcnt vmcnt(13)
v_mfma_f32_4x4x4_16b_bf16 v[108:111], v[96:97], a[0:1], 0
v_mfma_f32_4x4x4_16b_bf16 v[116:119], a[0:1], a[0:1], 0
v_mfma_f32_4x4x4_16b_bf16 v[108:111], v[96:97], a[2:3], v[108:111]
v_mfma_f32_4x4x4_16b_bf16 v[116:119], a[2:3], a[2:3], v[116:119]
s_waitcnt vmcnt(12)
v_mfma_f32_4x4x4_16b_bf16 v[112:115], v[96:97], a[4:5], 0
v_mfma_f32_4x4x4_16b_bf16 v[120:123], a[4:5], a[4:5], 0
v_mfma_f32_4x4x4_16b_bf16 v[112:115], v[96:97], a[6:7], v[112:115]
v_mfma_f32_4x4x4_16b_bf16 v[120:123], a[6:7], a[6:7], v[120:123]
buffer_load_dwordx4 a[112:115], v38, s[48:51], 0 offen offset:448
buffer_load_dwordx4 a[116:119], v39, s[48:51], 0 offen offset:448
s_waitcnt vmcnt(13)
v_mfma_f32_4x4x4_16b_bf16 v[108:111], v[96:97], a[16:17], v[108:111]
v_mfma_f32_4x4x4_16b_bf16 v[116:119], a[16:17], a[16:17], v[116:119]
v_mfma_f32_4x4x4_16b_bf16 v[108:111], v[96:97], a[18:19], v[108:111]
v_mfma_f32_4x4x4_16b_bf16 v[116:119], a[18:19], a[18:19], v[116:119]
s_waitcnt vmcnt(12)
v_mfma_f32_4x4x4_16b_bf16 v[112:115], v[96:97], a[20:21], v[112:115]
v_mfma_f32_4x4x4_16b_bf16 v[120:123], a[20:21], a[20:21], v[120:123]
v_mfma_f32_4x4x4_16b_bf16 v[112:115], v[96:97], a[22:23], v[112:115]
v_mfma_f32_4x4x4_16b_bf16 v[120:123], a[22:23], a[22:23], v[120:123]
buffer_load_dwordx4 a[128:131], v38, s[48:51], 0 offen offset:512
buffer_load_dwordx4 a[132:135], v39, s[48:51], 0 offen offset:512
s_waitcnt vmcnt(13)
v_mfma_f32_4x4x4_16b_bf16 v[108:111], v[96:97], a[32:33], v[108:111]
v_mfma_f32_4x4x4_16b_bf16 v[116:119], a[32:33], a[32:33], v[116:119]
v_mfma_f32_4x4x4_16b_bf16 v[108:111], v[96:97], a[34:35], v[108:111]
v_mfma_f32_4x4x4_16b_bf16 v[116:119], a[34:35], a[34:35], v[116:119]
s_waitcnt vmcnt(12)
v_mfma_f32_4x4x4_16b_bf16 v[112:115], v[96:97], a[36:37], v[112:115]
v_mfma_f32_4x4x4_16b_bf16 v[120:123], a[36:37], a[36:37], v[120:123]
v_mfma_f32_4x4x4_16b_bf16 v[112:115], v[96:97], a[38:39], v[112:115]
v_mfma_f32_4x4x4_16b_bf16 v[120:123], a[38:39], a[38:39], v[120:123]
buffer_load_dwordx4 a[144:147], v38, s[48:51], 0 offen offset:576
buffer_load_dwordx4 a[148:151], v39, s[48:51], 0 offen offset:576
s_waitcnt vmcnt(13)
v_mfma_f32_4x4x4_16b_bf16 v[108:111], v[96:97], a[48:49], v[108:111]
v_mfma_f32_4x4x4_16b_bf16 v[116:119], a[48:49], a[48:49], v[116:119]
v_mfma_f32_4x4x4_16b_bf16 v[108:111], v[96:97], a[50:51], v[108:111]
v_mfma_f32_4x4x4_16b_bf16 v[116:119], a[50:51], a[50:51], v[116:119]
s_waitcnt vmcnt(12)
v_mfma_f32_4x4x4_16b_bf16 v[112:115], v[96:97], a[52:53], v[112:115]
v_mfma_f32_4x4x4_16b_bf16 v[120:123], a[52:53], a[52:53], v[120:123]
v_mfma_f32_4x4x4_16b_bf16 v[112:115], v[96:97], a[54:55], v[112:115]
v_mfma_f32_4x4x4_16b_bf16 v[120:123], a[54:55], a[54:55], v[120:123]
buffer_load_dwordx4 a[160:163], v38, s[48:51], 0 offen offset:640
buffer_load_dwordx4 a[164:167], v39, s[48:51], 0 offen offset:640
s_waitcnt vmcnt(13)
v_mfma_f32_4x4x4_16b_bf16 v[108:111], v[96:97], a[64:65], v[108:111]
v_mfma_f32_4x4x4_16b_bf16 v[116:119], a[64:65], a[64:65], v[116:119]
v_mfma_f32_4x4x4_16b_bf16 v[108:111], v[96:97], a[66:67], v[108:111]
v_mfma_f32_4x4x4_16b_bf16 v[116:119], a[66:67], a[66:67], v[116:119]
s_waitcnt vmcnt(12)
v_mfma_f32_4x4x4_16b_bf16 v[112:115], v[96:97], a[68:69], v[112:115]
v_mfma_f32_4x4x4_16b_bf16 v[120:123], a[68:69], a[68:69], v[120:123]
v_mfma_f32_4x4x4_16b_bf16 v[112:115], v[96:97], a[70:71], v[112:115]
v_mfma_f32_4x4x4_16b_bf16 v[120:123], a[70:71], a[70:71], v[120:123]
buffer_load_dwordx4 a[176:179], v38, s[48:51], 0 offen offset:704
buffer_load_dwordx4 a[180:183], v39, s[48:51], 0 offen offset:704
s_waitcnt vmcnt(13)
v_mfma_f32_4x4x4_16b_bf16 v[108:111], v[96:97], a[80:81], v[108:111]
v_mfma_f32_4x4x4_16b_bf16 v[116:119], a[80:81], a[80:81], v[116:119]
v_mfma_f32_4x4x4_16b_bf16 v[108:111], v[96:97], a[82:83], v[108:111]
v_mfma_f32_4x4x4_16b_bf16 v[116:119], a[82:83], a[82:83], v[116:119]
s_waitcnt vmcnt(12)
v_mfma_f32_4x4x4_16b_bf16 v[112:115], v[96:97], a[84:85], v[112:115]
v_mfma_f32_4x4x4_16b_bf16 v[120:123], a[84:85], a[84:85], v[120:123]
v_mfma_f32_4x4x4_16b_bf16 v[112:115], v[96:97], a[86:87], v[112:115]
v_mfma_f32_4x4x4_16b_bf16 v[120:123], a[86:87], a[86:87], v[120:123]
buffer_load_dwordx4 a[192:195], v38, s[48:51], 0 offen offset:768
buffer_load_dwordx4 a[196:199], v39, s[48:51], 0 offen offset:768
s_waitcnt vmcnt(13)
v_mfma_f32_4x4x4_16b_bf16 v[108:111], v[96:97], a[96:97], v[108:111]
v_mfma_f32_4x4x4_16b_bf16 v[116:119], a[96:97], a[96:97], v[116:119]
v_mfma_f32_4x4x4_16b_bf16 v[108:111], v[96:97], a[98:99], v[108:111]
v_mfma_f32_4x4x4_16b_bf16 v[116:119], a[98:99], a[98:99], v[116:119]
s_waitcnt vmcnt(12)
v_mfma_f32_4x4x4_16b_bf16 v[112:115], v[96:97], a[100:101], v[112:115]
v_mfma_f32_4x4x4_16b_bf16 v[120:123], a[100:101], a[100:101], v[120:123]
v_mfma_f32_4x4x4_16b_bf16 v[112:115], v[96:97], a[102:103], v[112:115]
v_mfma_f32_4x4x4_16b_bf16 v[120:123], a[102:103], a[102:103], v[120:123]
buffer_load_dwordx4 a[208:211], v38, s[48:51], 0 offen offset:832
buffer_load_dwordx4 a[212:215], v39, s[48:51], 0 offen offset:832
s_waitcnt vmcnt(13)
v_mfma_f32_4x4x4_16b_bf16 v[108:111], v[96:97], a[112:113], v[108:111]
v_mfma_f32_4x4x4_16b_bf16 v[116:119], a[112:113], a[112:113], v[116:119]
v_mfma_f32_4x4x4_16b_bf16 v[108:111], v[96:97], a[114:115], v[108:111]
v_mfma_f32_4x4x4_16b_bf16 v[116:119], a[114:115], a[114:115], v[116:119]
s_waitcnt vmcnt(12)
v_mfma_f32_4x4x4_16b_bf16 v[112:115], v[96:97], a[116:117], v[112:115]
v_mfma_f32_4x4x4_16b_bf16 v[120:123], a[116:117], a[116:117], v[120:123]
v_mfma_f32_4x4x4_16b_bf16 v[112:115], v[96:97], a[118:119], v[112:115]
v_mfma_f32_4x4x4_16b_bf16 v[120:123], a[118:119], a[118:119], v[120:123]
buffer_load_dwordx4 a[224:227], v38, s[48:51], 0 offen offset:896
buffer_load_dwordx4 a[228:231], v39, s[48:51], 0 offen offset:896
s_waitcnt vmcnt(13)
v_mfma_f32_4x4x4_16b_bf16 v[108:111], v[96:97], a[128:129], v[108:111]
v_mfma_f32_4x4x4_16b_bf16 v[116:119], a[128:129], a[128:129], v[116:119]
v_mfma_f32_4x4x4_16b_bf16 v[108:111], v[96:97], a[130:131], v[108:111]
v_mfma_f32_4x4x4_16b_bf16 v[116:119], a[130:131], a[130:131], v[116:119]
s_waitcnt vmcnt(12)
v_mfma_f32_4x4x4_16b_bf16 v[112:115], v[96:97], a[132:133], v[112:115]
v_mfma_f32_4x4x4_16b_bf16 v[120:123], a[132:133], a[132:133], v[120:123]
v_mfma_f32_4x4x4_16b_bf16 v[112:115], v[96:97], a[134:135], v[112:115]
v_mfma_f32_4x4x4_16b_bf16 v[120:123], a[134:135], a[134:135], v[120:123]
buffer_load_dwordx4 a[240:243], v38, s[48:51], 0 offen offset:960
buffer_load_dwordx4 a[244:247], v39, s[48:51], 0 offen offset:960
s_waitcnt vmcnt(13)
v_mfma_f32_4x4x4_16b_bf16 v[108:111], v[96:97], a[144:145], v[108:111]
v_mfma_f32_4x4x4_16b_bf16 v[116:119], a[144:145], a[144:145], v[116:119]
v_mfma_f32_4x4x4_16b_bf16 v[108:111], v[96:97], a[146:147], v[108:111]
v_mfma_f32_4x4x4_16b_bf16 v[116:119], a[146:147], a[146:147], v[116:119]
s_waitcnt vmcnt(12)
v_mfma_f32_4x4x4_16b_bf16 v[112:115], v[96:97], a[148:149], v[112:115]
v_mfma_f32_4x4x4_16b_bf16 v[120:123], a[148:149], a[148:149], v[120:123]
v_mfma_f32_4x4x4_16b_bf16 v[112:115], v[96:97], a[150:151], v[112:115]
v_mfma_f32_4x4x4_16b_bf16 v[120:123], a[150:151], a[150:151], v[120:123]
buffer_load_dwordx4 v[224:227], v38, s[48:51], 0 offen offset:1024
buffer_load_dwordx4 v[228:231], v39, s[48:51], 0 offen offset:1024
s_waitcnt vmcnt(13)
v_mfma_f32_4x4x4_16b_bf16 v[108:111], v[96:97], a[160:161], v[108:111]
v_mfma_f32_4x4x4_16b_bf16 v[116:119], a[160:161], a[160:161], v[116:119]
v_mfma_f32_4x4x4_16b_bf16 v[108:111], v[96:97], a[162:163], v[108:111]
v_mfma_f32_4x4x4_16b_bf16 v[116:119], a[162:163], a[162:163], v[116:119]
s_waitcnt vmcnt(12)
v_mfma_f32_4x4x4_16b_bf16 v[112:115], v[96:97], a[164:165], v[112:115]
v_mfma_f32_4x4x4_16b_bf16 v[120:123], a[164:165], a[164:165], v[120:123]
v_mfma_f32_4x4x4_16b_bf16 v[112:115], v[96:97], a[166:167], v[112:115]
v_mfma_f32_4x4x4_16b_bf16 v[120:123], a[166:167], a[166:167], v[120:123]
buffer_load_dwordx4 v[240:243], v38, s[48:51], 0 offen offset:1088
buffer_load_dwordx4 v[244:247], v39, s[48:51], 0 offen offset:1088
s_waitcnt vmcnt(13)
v_mfma_f32_4x4x4_16b_bf16 v[108:111], v[96:97], a[176:177], v[108:111]
v_mfma_f32_4x4x4_16b_bf16 v[116:119], a[176:177], a[176:177], v[116:119]
v_mfma_f32_4x4x4_16b_bf16 v[108:111], v[96:97], a[178:179], v[108:111]
v_mfma_f32_4x4x4_16b_bf16 v[116:119], a[178:179], a[178:179], v[116:119]
s_waitcnt vmcnt(12)
v_mfma_f32_4x4x4_16b_bf16 v[112:115], v[96:97], a[180:181], v[112:115]
v_mfma_f32_4x4x4_16b_bf16 v[120:123], a[180:181], a[180:181], v[120:123]
v_mfma_f32_4x4x4_16b_bf16 v[112:115], v[96:97], a[182:183], v[112:115]
v_mfma_f32_4x4x4_16b_bf16 v[120:123], a[182:183], a[182:183], v[120:123]
s_mov_b32 m0, s68
s_mov_b32 s15, 4096
buffer_load_dwordx4 v1, s[16:19], s15 offen offset:0 lds
s_mov_b32 s15, 3072
buffer_load_dwordx4 v1, s[20:23], s15 offen offset:1024 lds
s_mov_b32 s15, 296960
buffer_load_dwordx4 v1, s[16:19], s15 offen offset:2048 lds
s_mov_b32 s15, 295936
buffer_load_dwordx4 v1, s[20:23], s15 offen offset:3072 lds
s_mov_b32 m0, s69
s_mov_b32 s15, 593920
buffer_load_dwordx4 v1, s[16:19], s15 offen offset:0 lds
s_mov_b32 s15, 592896
buffer_load_dwordx4 v1, s[20:23], s15 offen offset:1024 lds
s_mov_b32 s15, 886784
buffer_load_dwordx4 v1, s[16:19], s15 offen offset:2048 lds
s_mov_b32 s15, 885760
buffer_load_dwordx4 v1, s[20:23], s15 offen offset:3072 lds
s_mov_b32 m0, s70
s_mov_b32 s15, 1183744
buffer_load_dwordx4 v1, s[16:19], s15 offen offset:0 lds
s_mov_b32 s15, 1182720
buffer_load_dwordx4 v1, s[20:23], s15 offen offset:1024 lds
s_mov_b32 s15, 1476608
buffer_load_dwordx4 v1, s[16:19], s15 offen offset:2048 lds
s_mov_b32 s15, 1475584
buffer_load_dwordx4 v1, s[20:23], s15 offen offset:3072 lds
s_mov_b32 m0, 161792
s_waitcnt vmcnt(23)
v_mfma_f32_4x4x4_16b_bf16 v[108:111], v[96:97], a[192:193], v[108:111]
v_mfma_f32_4x4x4_16b_bf16 v[116:119], a[192:193], a[192:193], v[116:119]
v_mfma_f32_4x4x4_16b_bf16 v[108:111], v[96:97], a[194:195], v[108:111]
v_mfma_f32_4x4x4_16b_bf16 v[116:119], a[194:195], a[194:195], v[116:119]
s_waitcnt vmcnt(22)
v_mfma_f32_4x4x4_16b_bf16 v[112:115], v[96:97], a[196:197], v[112:115]
v_mfma_f32_4x4x4_16b_bf16 v[120:123], a[196:197], a[196:197], v[120:123]
v_mfma_f32_4x4x4_16b_bf16 v[112:115], v[96:97], a[198:199], v[112:115]
v_mfma_f32_4x4x4_16b_bf16 v[120:123], a[198:199], a[198:199], v[120:123]
s_waitcnt vmcnt(21)
v_mfma_f32_4x4x4_16b_bf16 v[108:111], v[96:97], a[208:209], v[108:111]
v_mfma_f32_4x4x4_16b_bf16 v[116:119], a[208:209], a[208:209], v[116:119]
v_mfma_f32_4x4x4_16b_bf16 v[108:111], v[96:97], a[210:211], v[108:111]
v_mfma_f32_4x4x4_16b_bf16 v[116:119], a[210:211], a[210:211], v[116:119]
s_waitcnt vmcnt(20)
v_mfma_f32_4x4x4_16b_bf16 v[112:115], v[96:97], a[212:213], v[112:115]
v_mfma_f32_4x4x4_16b_bf16 v[120:123], a[212:213], a[212:213], v[120:123]
v_mfma_f32_4x4x4_16b_bf16 v[112:115], v[96:97], a[214:215], v[112:115]
v_mfma_f32_4x4x4_16b_bf16 v[120:123], a[214:215], a[214:215], v[120:123]
s_waitcnt vmcnt(19)
v_mfma_f32_4x4x4_16b_bf16 v[108:111], v[96:97], a[224:225], v[108:111]
v_mfma_f32_4x4x4_16b_bf16 v[116:119], a[224:225], a[224:225], v[116:119]
v_mfma_f32_4x4x4_16b_bf16 v[108:111], v[96:97], a[226:227], v[108:111]
v_mfma_f32_4x4x4_16b_bf16 v[116:119], a[226:227], a[226:227], v[116:119]
s_waitcnt vmcnt(18)
v_mfma_f32_4x4x4_16b_bf16 v[112:115], v[96:97], a[228:229], v[112:115]
v_mfma_f32_4x4x4_16b_bf16 v[120:123], a[228:229], a[228:229], v[120:123]
v_mfma_f32_4x4x4_16b_bf16 v[112:115], v[96:97], a[230:231], v[112:115]
v_mfma_f32_4x4x4_16b_bf16 v[120:123], a[230:231], a[230:231], v[120:123]
s_waitcnt vmcnt(17)
v_mfma_f32_4x4x4_16b_bf16 v[108:111], v[96:97], a[240:241], v[108:111]
v_mfma_f32_4x4x4_16b_bf16 v[116:119], a[240:241], a[240:241], v[116:119]
v_mfma_f32_4x4x4_16b_bf16 v[108:111], v[96:97], a[242:243], v[108:111]
v_mfma_f32_4x4x4_16b_bf16 v[116:119], a[242:243], a[242:243], v[116:119]
s_waitcnt vmcnt(16)
v_mfma_f32_4x4x4_16b_bf16 v[112:115], v[96:97], a[244:245], v[112:115]
v_mfma_f32_4x4x4_16b_bf16 v[120:123], a[244:245], a[244:245], v[120:123]
v_mfma_f32_4x4x4_16b_bf16 v[112:115], v[96:97], a[246:247], v[112:115]
v_mfma_f32_4x4x4_16b_bf16 v[120:123], a[246:247], a[246:247], v[120:123]
s_waitcnt vmcnt(15)
v_mfma_f32_4x4x4_16b_bf16 v[108:111], v[96:97], v[224:225], v[108:111]
v_mfma_f32_4x4x4_16b_bf16 v[116:119], v[224:225], v[224:225], v[116:119]
v_mfma_f32_4x4x4_16b_bf16 v[108:111], v[96:97], v[226:227], v[108:111]
v_mfma_f32_4x4x4_16b_bf16 v[116:119], v[226:227], v[226:227], v[116:119]
s_waitcnt vmcnt(14)
v_mfma_f32_4x4x4_16b_bf16 v[112:115], v[96:97], v[228:229], v[112:115]
v_mfma_f32_4x4x4_16b_bf16 v[120:123], v[228:229], v[228:229], v[120:123]
v_mfma_f32_4x4x4_16b_bf16 v[112:115], v[96:97], v[230:231], v[112:115]
v_mfma_f32_4x4x4_16b_bf16 v[120:123], v[230:231], v[230:231], v[120:123]
s_waitcnt vmcnt(13)
v_mfma_f32_4x4x4_16b_bf16 v[108:111], v[96:97], v[240:241], v[108:111]
v_mfma_f32_4x4x4_16b_bf16 v[116:119], v[240:241], v[240:241], v[116:119]
v_mfma_f32_4x4x4_16b_bf16 v[108:111], v[96:97], v[242:243], v[108:111]
v_mfma_f32_4x4x4_16b_bf16 v[116:119], v[242:243], v[242:243], v[116:119]
s_waitcnt vmcnt(12)
v_mfma_f32_4x4x4_16b_bf16 v[112:115], v[96:97], v[244:245], v[112:115]
v_mfma_f32_4x4x4_16b_bf16 v[120:123], v[244:245], v[244:245], v[120:123]
v_mfma_f32_4x4x4_16b_bf16 v[112:115], v[96:97], v[246:247], v[112:115]
v_mfma_f32_4x4x4_16b_bf16 v[120:123], v[246:247], v[246:247], v[120:123]
buffer_load_dword v0, s[4:7], s92 idxen  lds
s_nop 1
v_cndmask_b32 v116, v116, v117, s[72:73]
s_nop 3
v_cndmask_b32 v120, v120, v121, s[72:73]
v_cndmask_b32 v116, v116, v118, s[74:75]
v_cndmask_b32 v120, v120, v122, s[74:75]
v_cndmask_b32 v116, v116, v119, s[76:77]
v_cndmask_b32 v120, v120, v123, s[76:77]
v_add_f32_dpp v108, v108, v108 quad_perm:[1,0,3,2] row_mask:0xf bank_mask:0xf
v_add_f32_dpp v112, v112, v112 quad_perm:[1,0,3,2] row_mask:0xf bank_mask:0xf
v_add_f32_dpp v116, v116, v116 quad_perm:[1,0,3,2] row_mask:0xf bank_mask:0xf
v_add_f32_dpp v120, v120, v120 quad_perm:[1,0,3,2] row_mask:0xf bank_mask:0xf
v_add_f32_dpp v108, v108, v108 quad_perm:[2,3,0,1] row_mask:0xf bank_mask:0xf
v_add_f32_dpp v112, v112, v112 quad_perm:[2,3,0,1] row_mask:0xf bank_mask:0xf
v_add_f32_dpp v116, v116, v116 quad_perm:[2,3,0,1] row_mask:0xf bank_mask:0xf
v_add_f32_dpp v120, v120, v120 quad_perm:[2,3,0,1] row_mask:0xf bank_mask:0xf
v_mul_f32 v109, 0x3ae38e39, v108
v_mul_f32 v113, 0x3ae38e39, v112
VC 108,109,109
VC 112,113,113
v_and_b32 v50, 0xffff0000, v108
v_and_b32 v52, 0xffff0000, v112
VM 109,109,109
VM 113,113,113
v_lshlrev_b32 v54, 2, v36
v_lshlrev_b32 v55, 2, v36
v_add_u32 v54, 147456, v54
v_add_u32 v55, 147472, v55
v_lshl_add_u32 v56, v36, 3, 0
v_lshl_add_u32 v57, v36, 3, 32
v_mov_b32 v117, 0x3ae38e39
v_mov_b32 v121, 0x3ae38e39
v_fma_f32 v116, v116, v117, -v109
v_fma_f32 v120, v120, v121, -v113
v_max_f32 v116, 0, v116
v_max_f32 v120, 0, v120
VC 116,116,116
VC 120,120,120
v_and_b32 v116, 0xffff0000, v116
v_and_b32 v120, 0xffff0000, v120
v_add_f32 v116, 0x3727c5ac, v116
v_add_f32 v120, 0x3727c5ac, v120
VC 116,116,116
VC 120,120,120
v_and_b32 v116, 0xffff0000, v116
v_and_b32 v120, 0xffff0000, v120
v_sqrt_f32 v116, v116
v_sqrt_f32 v120, v120
v_xor_b32 v108, 0x80000000, v50
v_xor_b32 v112, 0x80000000, v52
VC 116,116,116
VC 120,120,120
v_and_b32 v51, 0xffff0000, v116
v_and_b32 v53, 0xffff0000, v120
v_rcp_f32 v116, v51
v_rcp_f32 v120, v53
ds_write_b32 v54, v51
ds_write_b32 v55, v53
v_mov_b32 v109, v108
v_mov_b32 v113, v112
v_mov_b32 v110, v108
v_mov_b32 v114, v112
v_mov_b32 v111, v108
v_mov_b32 v115, v112
ds_write_b32 v54, v116 offset:512
ds_write_b32 v55, v120 offset:512
buffer_store_dwordx2 v[50:51], v56, s[60:63], 0 offen sc1
buffer_store_dwordx2 v[52:53], v57, s[60:63], 0 offen sc1
s_waitcnt lgkmcnt(0)
s_add_u32 s100, s100, 1
s_add_u32 s101, s101, 1
v_mfma_f32_16x16x32_bf16 v[124:127], v[102:105], a[0:3], v[108:111]
v_mfma_f32_16x16x32_bf16 v[128:131], v[100:103], a[0:3], v[108:111]
v_mfma_f32_16x16x32_bf16 v[132:135], v[102:105], a[4:7], v[112:115]
v_mfma_f32_16x16x32_bf16 v[136:139], v[100:103], a[4:7], v[112:115]
s_nop 4
VC 140,124,125
VC 141,126,127
VC 142,128,129
VC 143,130,131
ds_write_b128 v156, v[140:143] offset:0
v_mfma_f32_16x16x32_bf16 v[124:127], v[102:105], a[16:19], v[108:111]
v_mfma_f32_16x16x32_bf16 v[128:131], v[100:103], a[16:19], v[108:111]
VC 144,132,133
VC 145,134,135
VC 146,136,137
VC 147,138,139
ds_write_b128 v156, v[144:147] offset:1024
v_mfma_f32_16x16x32_bf16 v[132:135], v[102:105], a[20:23], v[112:115]
v_mfma_f32_16x16x32_bf16 v[136:139], v[100:103], a[20:23], v[112:115]
VC 148,124,125
VC 149,126,127
VC 150,128,129
VC 151,130,131
ds_write_b128 v156, v[148:151] offset:4096
v_mfma_f32_16x16x32_bf16 v[124:127], v[102:105], a[32:35], v[108:111]
v_mfma_f32_16x16x32_bf16 v[128:131], v[100:103], a[32:35], v[108:111]
VC 152,132,133
VC 153,134,135
VC 154,136,137
VC 155,138,139
ds_write_b128 v156, v[152:155] offset:5120
v_mfma_f32_16x16x32_bf16 v[132:135], v[102:105], a[36:39], v[112:115]
v_mfma_f32_16x16x32_bf16 v[136:139], v[100:103], a[36:39], v[112:115]
VC 140,124,125
VC 141,126,127
VC 142,128,129
VC 143,130,131
ds_write_b128 v156, v[140:143] offset:16384
v_mfma_f32_16x16x32_bf16 v[124:127], v[102:105], a[48:51], v[108:111]
v_mfma_f32_16x16x32_bf16 v[128:131], v[100:103], a[48:51], v[108:111]
VC 144,132,133
VC 145,134,135
VC 146,136,137
VC 147,138,139
ds_write_b128 v156, v[144:147] offset:17408
v_mfma_f32_16x16x32_bf16 v[132:135], v[102:105], a[52:55], v[112:115]
v_mfma_f32_16x16x32_bf16 v[136:139], v[100:103], a[52:55], v[112:115]
VC 148,124,125
VC 149,126,127
VC 150,128,129
VC 151,130,131
ds_write_b128 v156, v[148:151] offset:20480
s_nop 1
VC 152,132,133
VC 153,134,135
VC 154,136,137
VC 155,138,139
ds_write_b128 v156, v[152:155] offset:21504
s_waitcnt lgkmcnt(0)
s_waitcnt vmcnt(11)
s_barrier
ds_read_b128 a[0:3], v157 offset:0
ds_read_b128 a[4:7], v157 offset:1024
ds_read_b128 a[8:11], v157 offset:2048
ds_read_b128 a[12:15], v157 offset:3072
ds_read_b128 a[16:19], v157 offset:4096
ds_read_b128 a[20:23], v157 offset:5120
ds_read_b128 a[24:27], v157 offset:6144
ds_read_b128 a[28:31], v157 offset:7168
v_lshl_add_u32 v54, v34, 4, v35
s_lshl_b32 s67, s64, 6
v_add_u32 v54, s67, v54
v_lshlrev_b32 v54, 2, v54
v_add_u32 v54, 147456, v54
ds_read_b32 v10, v54 offset:0
ds_read_b32 v6, v54 offset:512
ds_read_b32 v11, v54 offset:16
ds_read_b32 v7, v54 offset:528
ds_read_b32 v12, v54 offset:32
ds_read_b32 v8, v54 offset:544
ds_read_b32 v13, v54 offset:48
ds_read_b32 v9, v54 offset:560
TR 192,2,0
TR 194,2,512
TR 196,2,1024
TR 198,2,1536
s_waitcnt lgkmcnt(11)
VM 10,10,10
s_waitcnt lgkmcnt(10)
v_mul_f32 v6, 0xbfb8aa3b, v6
s_waitcnt lgkmcnt(9)
VM 11,11,11
s_waitcnt lgkmcnt(8)
v_mul_f32 v7, 0xbfb8aa3b, v7
s_waitcnt lgkmcnt(7)
VM 12,12,12
s_waitcnt lgkmcnt(6)
v_mul_f32 v8, 0xbfb8aa3b, v8
s_waitcnt lgkmcnt(5)
VM 13,13,13
s_waitcnt lgkmcnt(4)
v_mul_f32 v9, 0xbfb8aa3b, v9
s_waitcnt vmcnt(7)
s_barrier
s_waitcnt lgkmcnt(2)
Zva 32,192,0
s_mov_b32 m0, s71
ds_read_b128 a[32:35], v157 offset:16384
Zva 48,192,4
s_mov_b32 s15, 1773568
buffer_load_dwordx4 v1, s[16:19], s15 offen offset:0 lds
Zva 64,192,8
TR 200,3,0
Zva 80,192,12
TR 202,3,512
v_mfma_f32_16x16x32_bf16 v[124:127], v[102:105], a[64:67], v[108:111]
s_mov_b32 s15, 1772544
buffer_load_dwordx4 v1, s[20:23], s15 offen offset:1024 lds
v_mfma_f32_16x16x32_bf16 v[128:131], v[100:103], a[64:67], v[108:111]
ds_read_b128 a[36:39], v157 offset:17408
s_waitcnt lgkmcnt(4)
Zva 36,196,0
TR 204,3,1024
Zva 52,196,4
s_mov_b32 s15, 2066432
buffer_load_dwordx4 v1, s[16:19], s15 offen offset:2048 lds
Zva 68,196,8
TR 206,3,1536
VC 140,124,125
Zva 84,196,12
ds_read_b128 a[40:43], v157 offset:18432
VC 141,126,127
v_mfma_f32_16x16x32_bf16 v[132:135], v[102:105], a[68:71], v[112:115]
s_mov_b32 s15, 2065408
buffer_load_dwordx4 v1, s[20:23], s15 offen offset:3072 lds
v_mfma_f32_16x16x32_bf16 v[136:139], v[100:103], a[68:71], v[112:115]
VC 142,128,129
VC 143,130,131
s_waitcnt lgkmcnt(4)
Zva 40,200,0
TR 208,2,2048
Zva 56,200,4
TR 210,2,2560
Zva 72,200,8
ds_write_b128 v156, v[140:143] offset:32768
Zva 88,200,12
ds_read_b128 a[44:47], v157 offset:19456
VC 144,132,133
s_waitcnt lgkmcnt(5)
Zva 44,204,0
VC 145,134,135
VC 146,136,137
Zva 60,204,4
VC 147,138,139
TR 212,2,3072
Zva 76,204,8
ds_read_b128 a[48:51], v157 offset:20480
Zva 92,204,12
TR 214,2,3584
v_mfma_f32_16x16x32_bf16 v[124:127], v[102:105], a[80:83], v[108:111]
ds_write_b128 v156, v[144:147] offset:33792
v_mfma_f32_16x16x32_bf16 v[128:131], v[100:103], a[80:83], v[108:111]
TR 192,3,2048
s_waitcnt lgkmcnt(7)
Mva 32,208,16
ds_read_b128 a[52:55], v157 offset:21504
Mva 48,208,20
TR 194,3,2560
Mva 64,208,24
TR 196,3,3072
VC 148,124,125
Mva 80,208,28
ds_read_b128 a[56:59], v157 offset:22528
VC 149,126,127
v_mfma_f32_16x16x32_bf16 v[132:135], v[102:105], a[84:87], v[112:115]
TR 198,3,3584
VC 150,128,129
v_mfma_f32_16x16x32_bf16 v[136:139], v[100:103], a[84:87], v[112:115]
VC 151,130,131
TR 200,2,16384
s_waitcnt lgkmcnt(8)
Mva 36,212,16
ds_write_b128 v156, v[148:151] offset:36864
Mva 52,212,20
TR 202,2,16896
Mva 68,212,24
ds_read_b128 a[60:63], v157 offset:23552
VC 152,132,133
Mva 84,212,28
VC 153,134,135
VC 154,136,137
s_waitcnt lgkmcnt(7)
Mva 40,192,16
VC 155,138,139
TR 204,2,17408
Mva 56,192,20
ds_write_b128 v156, v[152:155] offset:37888
Mva 72,192,24
TR 206,2,17920
Mva 88,192,28
s_waitcnt lgkmcnt(7)
Mva 44,196,16
Mva 60,196,20
Mva 76,196,24
TR 208,3,16384
Mva 92,196,28
TR 210,3,16896
s_waitcnt lgkmcnt(3)
s_waitcnt vmcnt(7)
s_barrier
Mva 32,200,32
s_mov_b32 m0, s68
ds_read_b128 a[64:67], v157 offset:32768
Mva 48,200,36
s_mov_b32 s15, 2363392
buffer_load_dwordx4 v1, s[16:19], s15 offen offset:0 lds
Mva 64,200,40
ds_read_b128 a[68:71], v157 offset:33792
Mva 80,200,44
TR 212,3,17408
v_mfma_f32_16x16x32_bf16 v[124:127], v[102:105], a[96:99], v[108:111]
s_mov_b32 s15, 2362368
buffer_load_dwordx4 v1, s[20:23], s15 offen offset:1024 lds
v_mfma_f32_16x16x32_bf16 v[128:131], v[100:103], a[96:99], v[108:111]
TR 214,3,17920
s_waitcnt lgkmcnt(6)
Mva 36,204,32
ds_read_b128 a[72:75], v157 offset:34816
Mva 52,204,36
s_mov_b32 s15, 2656256
buffer_load_dwordx4 v1, s[16:19], s15 offen offset:2048 lds
Mva 68,204,40
VC 140,124,125
VC 141,126,127
Mva 84,204,44
VC 142,128,129
VC 143,130,131
v_mfma_f32_16x16x32_bf16 v[132:135], v[102:105], a[100:103], v[112:115]
s_mov_b32 s15, 2655232
buffer_load_dwordx4 v1, s[20:23], s15 offen offset:3072 lds
v_mfma_f32_16x16x32_bf16 v[136:139], v[100:103], a[100:103], v[112:115]
TR 192,2,18432
s_waitcnt lgkmcnt(6)
Mva 40,208,32
TR 194,2,18944
Mva 56,208,36
ds_write_b128 v156, v[140:143] offset:0
Mva 72,208,40
ds_read_b128 a[76:79], v157 offset:35840
VC 144,132,133
Mva 88,208,44
VC 145,134,135
VC 146,136,137
s_waitcnt lgkmcnt(5)
Mva 44,212,32
VC 147,138,139
TR 196,2,19456
Mva 60,212,36
ds_read_b128 a[80:83], v157 offset:36864
Mva 76,212,40
TR 198,2,19968
Mva 92,212,44
ds_write_b128 v156, v[144:147] offset:1024
v_mfma_f32_16x16x32_bf16 v[124:127], v[102:105], a[112:115], v[108:111]
TR 200,3,18432
v_mfma_f32_16x16x32_bf16 v[128:131], v[100:103], a[112:115], v[108:111]
ds_read_b128 a[84:87], v157 offset:37888
s_waitcnt lgkmcnt(8)
Mva 32,192,48
TR 202,3,18944
Mva 48,192,52
TR 204,3,19456
Mva 64,192,56
)"
R"(ds_read_b128 a[88:91], v157 offset:38912
VC 148,124,125
Mva 80,192,60
TR 206,3,19968
VC 149,126,127
v_mfma_f32_16x16x32_bf16 v[132:135], v[102:105], a[116:119], v[112:115]
VC 150,128,129
VC 151,130,131
v_mfma_f32_16x16x32_bf16 v[136:139], v[100:103], a[116:119], v[112:115]
TR 208,2,32768
s_waitcnt lgkmcnt(8)
Mva 36,196,48
ds_write_b128 v156, v[148:151] offset:4096
Mva 52,196,52
TR 210,2,33280
Mva 68,196,56
ds_read_b128 a[92:95], v157 offset:39936
VC 152,132,133
Mva 84,196,60
VC 153,134,135
VC 154,136,137
s_waitcnt lgkmcnt(7)
Mva 40,200,48
VC 155,138,139
TR 212,2,33792
Mva 56,200,52
ds_write_b128 v156, v[152:155] offset:5120
Mva 72,200,56
TR 214,2,34304
Mva 88,200,60
s_waitcnt lgkmcnt(7)
Mva 44,204,48
Mva 60,204,52
Mva 76,204,56
TR 192,3,32768
Mva 92,204,60
TR 194,3,33280
s_waitcnt lgkmcnt(3)
s_waitcnt vmcnt(4)
s_barrier
Mva 32,208,64
s_mov_b32 m0, s69
ds_read_b128 a[96:99], v157 offset:0
Mva 48,208,68
s_mov_b32 s15, 2953216
buffer_load_dwordx4 v1, s[16:19], s15 offen offset:0 lds
Mva 64,208,72
ds_read_b128 a[100:103], v157 offset:1024
Mva 80,208,76
TR 196,3,33792
v_mfma_f32_16x16x32_bf16 v[124:127], v[102:105], a[128:131], v[108:111]
s_mov_b32 s15, 2952192
buffer_load_dwordx4 v1, s[20:23], s15 offen offset:1024 lds
v_mfma_f32_16x16x32_bf16 v[128:131], v[100:103], a[128:131], v[108:111]
TR 198,3,34304
s_waitcnt lgkmcnt(6)
Mva 36,212,64
ds_read_b128 a[104:107], v157 offset:2048
Mva 52,212,68
s_mov_b32 s15, 3246080
buffer_load_dwordx4 v1, s[16:19], s15 offen offset:2048 lds
Mva 68,212,72
VC 140,124,125
VC 141,126,127
Mva 84,212,76
VC 142,128,129
VC 143,130,131
v_mfma_f32_16x16x32_bf16 v[132:135], v[102:105], a[132:135], v[112:115]
s_mov_b32 s15, 3245056
buffer_load_dwordx4 v1, s[20:23], s15 offen offset:3072 lds
v_mfma_f32_16x16x32_bf16 v[136:139], v[100:103], a[132:135], v[112:115]
TR 200,2,34816
s_waitcnt lgkmcnt(6)
Mva 40,192,64
TR 202,2,35328
Mva 56,192,68
ds_write_b128 v156, v[140:143] offset:16384
Mva 72,192,72
ds_read_b128 a[108:111], v157 offset:3072
VC 144,132,133
Mva 88,192,76
VC 145,134,135
VC 146,136,137
s_waitcnt lgkmcnt(5)
Mva 44,196,64
VC 147,138,139
TR 204,2,35840
Mva 60,196,68
ds_read_b128 a[112:115], v157 offset:4096
Mva 76,196,72
TR 206,2,36352
Mva 92,196,76
ds_write_b128 v156, v[144:147] offset:17408
v_mfma_f32_16x16x32_bf16 v[124:127], v[102:105], a[144:147], v[108:111]
TR 208,3,34816
v_mfma_f32_16x16x32_bf16 v[128:131], v[100:103], a[144:147], v[108:111]
ds_read_b128 a[116:119], v157 offset:5120
s_waitcnt lgkmcnt(8)
Mva 32,200,80
TR 210,3,35328
Mva 48,200,84
TR 212,3,35840
Mva 64,200,88
ds_read_b128 a[120:123], v157 offset:6144
VC 148,124,125
Mva 80,200,92
TR 214,3,36352
VC 149,126,127
v_mfma_f32_16x16x32_bf16 v[132:135], v[102:105], a[148:151], v[112:115]
VC 150,128,129
VC 151,130,131
v_mfma_f32_16x16x32_bf16 v[136:139], v[100:103], a[148:151], v[112:115]
TR 192,2,49152
s_waitcnt lgkmcnt(8)
Mva 36,204,80
ds_write_b128 v156, v[148:151] offset:20480
Mva 52,204,84
TR 194,2,49664
Mva 68,204,88
ds_read_b128 a[124:127], v157 offset:7168
VC 152,132,133
Mva 84,204,92
VC 153,134,135
VC 154,136,137
s_waitcnt lgkmcnt(7)
Mva 40,208,80
VC 155,138,139
TR 196,2,50176
Mva 56,208,84
ds_write_b128 v156, v[152:155] offset:21504
Mva 72,208,88
TR 198,2,50688
Mva 88,208,92
s_waitcnt lgkmcnt(7)
Mva 44,212,80
Mva 60,212,84
Mva 76,212,88
TR 200,3,49152
Mva 92,212,92
TR 202,3,49664
s_waitcnt lgkmcnt(3)
s_waitcnt vmcnt(4)
s_barrier
Mva 32,192,96
s_mov_b32 m0, s70
ds_read_b128 a[128:131], v157 offset:16384
Mva 48,192,100
s_mov_b32 s15, 3543040
buffer_load_dwordx4 v1, s[16:19], s15 offen offset:0 lds
Mva 64,192,104
ds_read_b128 a[132:135], v157 offset:17408
Mva 80,192,108
TR 204,3,50176
v_mfma_f32_16x16x32_bf16 v[124:127], v[102:105], a[160:163], v[108:111]
s_mov_b32 s15, 3542016
buffer_load_dwordx4 v1, s[20:23], s15 offen offset:1024 lds
v_mfma_f32_16x16x32_bf16 v[128:131], v[100:103], a[160:163], v[108:111]
TR 206,3,50688
s_waitcnt lgkmcnt(6)
Mva 36,196,96
ds_read_b128 a[136:139], v157 offset:18432
Mva 52,196,100
s_mov_b32 s15, 3835904
buffer_load_dwordx4 v1, s[16:19], s15 offen offset:2048 lds
Mva 68,196,104
VC 140,124,125
VC 141,126,127
Mva 84,196,108
VC 142,128,129
VC 143,130,131
v_mfma_f32_16x16x32_bf16 v[132:135], v[102:105], a[164:167], v[112:115]
s_mov_b32 s15, 3834880
buffer_load_dwordx4 v1, s[20:23], s15 offen offset:3072 lds
v_mfma_f32_16x16x32_bf16 v[136:139], v[100:103], a[164:167], v[112:115]
TR 208,2,51200
s_waitcnt lgkmcnt(6)
Mva 40,200,96
TR 210,2,51712
Mva 56,200,100
ds_write_b128 v156, v[140:143] offset:32768
Mva 72,200,104
ds_read_b128 a[140:143], v157 offset:19456
VC 144,132,133
Mva 88,200,108
VC 145,134,135
VC 146,136,137
s_waitcnt lgkmcnt(5)
Mva 44,204,96
VC 147,138,139
TR 212,2,52224
Mva 60,204,100
ds_read_b128 a[144:147], v157 offset:20480
Mva 76,204,104
TR 214,2,52736
Mva 92,204,108
ds_write_b128 v156, v[144:147] offset:33792
v_mfma_f32_16x16x32_bf16 v[124:127], v[102:105], a[176:179], v[108:111]
TR 192,3,51200
v_mfma_f32_16x16x32_bf16 v[128:131], v[100:103], a[176:179], v[108:111]
ds_read_b128 a[148:151], v157 offset:21504
s_waitcnt lgkmcnt(8)
Mva 32,208,112
TR 194,3,51712
Mva 48,208,116
TR 196,3,52224
Mva 64,208,120
ds_read_b128 a[152:155], v157 offset:22528
VC 148,124,125
Mva 80,208,124
TR 198,3,52736
VC 149,126,127
v_mfma_f32_16x16x32_bf16 v[132:135], v[102:105], a[180:183], v[112:115]
VC 150,128,129
VC 151,130,131
v_mfma_f32_16x16x32_bf16 v[136:139], v[100:103], a[180:183], v[112:115]
TR 200,2,0
s_waitcnt lgkmcnt(8)
Mva 36,212,112
ds_write_b128 v156, v[148:151] offset:36864
Mva 52,212,116
TR 202,2,512
Mva 68,212,120
ds_read_b128 a[156:159], v157 offset:23552
VC 152,132,133
Mva 84,212,124
VC 153,134,135
VC 154,136,137
s_waitcnt lgkmcnt(7)
Mva 40,192,112
VC 155,138,139
TR 204,2,1024
Mva 56,192,116
ds_write_b128 v156, v[152:155] offset:37888
Mva 72,192,120
TR 206,2,1536
Mva 88,192,124
s_waitcnt lgkmcnt(7)
Mva 44,196,112
Mva 60,196,116
Mva 76,196,120
TR 208,3,0
Mva 92,196,124
TR 210,3,512
s_waitcnt lgkmcnt(3)
s_waitcnt vmcnt(4)
s_barrier
Mva 32,200,128
s_mov_b32 m0, s71
ds_read_b128 a[160:163], v157 offset:32768
Mva 48,200,132
s_mov_b32 s15, 4132864
buffer_load_dwordx4 v1, s[16:19], s15 offen offset:0 lds
Mva 64,200,136
ds_read_b128 a[164:167], v157 offset:33792
Mva 80,200,140
TR 212,3,1024
v_mfma_f32_16x16x32_bf16 v[124:127], v[102:105], a[192:195], v[108:111]
s_mov_b32 s15, 4131840
buffer_load_dwordx4 v1, s[20:23], s15 offen offset:1024 lds
v_mfma_f32_16x16x32_bf16 v[128:131], v[100:103], a[192:195], v[108:111]
TR 214,3,1536
s_waitcnt lgkmcnt(6)
Mva 36,204,128
ds_read_b128 a[168:171], v157 offset:34816
Mva 52,204,132
s_mov_b32 s15, 4425728
buffer_load_dwordx4 v1, s[16:19], s15 offen offset:2048 lds
Mva 68,204,136
VC 140,124,125
VC 141,126,127
Mva 84,204,140
VC 142,128,129
VC 143,130,131
v_mfma_f32_16x16x32_bf16 v[132:135], v[102:105], a[196:199], v[112:115]
s_mov_b32 s15, 4424704
buffer_load_dwordx4 v1, s[20:23], s15 offen offset:3072 lds
v_mfma_f32_16x16x32_bf16 v[136:139], v[100:103], a[196:199], v[112:115]
TR 192,2,2048
s_waitcnt lgkmcnt(6)
Mva 40,208,128
TR 194,2,2560
Mva 56,208,132
ds_write_b128 v156, v[140:143] offset:0
Mva 72,208,136
ds_read_b128 a[172:175], v157 offset:35840
VC 144,132,133
Mva 88,208,140
VC 145,134,135
VC 146,136,137
s_waitcnt lgkmcnt(5)
Mva 44,212,128
VC 147,138,139
TR 196,2,3072
Mva 60,212,132
ds_read_b128 a[176:179], v157 offset:36864
Mva 76,212,136
TR 198,2,3584
Mva 92,212,140
ds_write_b128 v156, v[144:147] offset:1024
v_mfma_f32_16x16x32_bf16 v[124:127], v[102:105], a[208:211], v[108:111]
TR 200,3,2048
v_mfma_f32_16x16x32_bf16 v[128:131], v[100:103], a[208:211], v[108:111]
ds_read_b128 a[180:183], v157 offset:37888
s_waitcnt lgkmcnt(8)
Mva 32,192,144
TR 202,3,2560
Mva 48,192,148
TR 204,3,3072
Mva 64,192,152
ds_read_b128 a[184:187], v157 offset:38912
VC 148,124,125
Mva 80,192,156
TR 206,3,3584
VC 149,126,127
v_mfma_f32_16x16x32_bf16 v[132:135], v[102:105], a[212:215], v[112:115]
VC 150,128,129
VC 151,130,131
v_mfma_f32_16x16x32_bf16 v[136:139], v[100:103], a[212:215], v[112:115]
TR 208,2,16384
s_waitcnt lgkmcnt(8)
Mva 36,196,144
ds_write_b128 v156, v[148:151] offset:4096
Mva 52,196,148
TR 210,2,16896
Mva 68,196,152
ds_read_b128 a[188:191], v157 offset:39936
VC 152,132,133
Mva 84,196,156
VC 153,134,135
VC 154,136,137
s_waitcnt lgkmcnt(7)
Mva 40,200,144
VC 155,138,139
TR 212,2,17408
Mva 56,200,148
ds_write_b128 v156, v[152:155] offset:5120
Mva 72,200,152
TR 214,2,17920
Mva 88,200,156
s_waitcnt lgkmcnt(7)
Mva 44,204,144
Mva 60,204,148
Mva 76,204,152
TR 192,3,16384
Mva 92,204,156
TR 194,3,16896
s_waitcnt lgkmcnt(3)
s_waitcnt vmcnt(4)
s_barrier
Mva 32,208,160
s_mov_b32 m0, s68
ds_read_b128 a[192:195], v157 offset:0
Mva 48,208,164
s_mov_b32 s15, 4722688
buffer_load_dwordx4 v1, s[16:19], s15 offen offset:0 lds
Mva 64,208,168
ds_read_b128 a[196:199], v157 offset:1024
Mva 80,208,172
TR 196,3,17408
v_mfma_f32_16x16x32_bf16 v[124:127], v[102:105], a[224:227], v[108:111]
s_mov_b32 s15, 4721664
buffer_load_dwordx4 v1, s[20:23], s15 offen offset:1024 lds
v_mfma_f32_16x16x32_bf16 v[128:131], v[100:103], a[224:227], v[108:111]
TR 198,3,17920
s_waitcnt lgkmcnt(6)
Mva 36,212,160
ds_read_b128 a[200:203], v157 offset:2048
Mva 52,212,164
s_mov_b32 s15, 5015552
buffer_load_dwordx4 v1, s[16:19], s15 offen offset:2048 lds
Mva 68,212,168
VC 140,124,125
VC 141,126,127
Mva 84,212,172
VC 142,128,129
VC 143,130,131
v_mfma_f32_16x16x32_bf16 v[132:135], v[102:105], a[228:231], v[112:115]
s_mov_b32 s15, 5014528
buffer_load_dwordx4 v1, s[20:23], s15 offen offset:3072 lds
v_mfma_f32_16x16x32_bf16 v[136:139], v[100:103], a[228:231], v[112:115]
TR 200,2,18432
s_waitcnt lgkmcnt(6)
Mva 40,192,160
TR 202,2,18944
Mva 56,192,164
ds_write_b128 v156, v[140:143] offset:16384
Mva 72,192,168
ds_read_b128 a[204:207], v157 offset:3072
VC 144,132,133
Mva 88,192,172
VC 145,134,135
VC 146,136,137
s_waitcnt lgkmcnt(5)
Mva 44,196,160
VC 147,138,139
TR 204,2,19456
Mva 60,196,164
ds_read_b128 a[208:211], v157 offset:4096
Mva 76,196,168
TR 206,2,19968
Mva 92,196,172
ds_write_b128 v156, v[144:147] offset:17408
v_mfma_f32_16x16x32_bf16 v[124:127], v[102:105], a[240:243], v[108:111]
TR 208,3,18432
v_mfma_f32_16x16x32_bf16 v[128:131], v[100:103], a[240:243], v[108:111]
ds_read_b128 a[212:215], v157 offset:5120
s_waitcnt lgkmcnt(8)
Mva 32,200,176
TR 210,3,18944
Mva 48,200,180
TR 212,3,19456
Mva 64,200,184
ds_read_b128 a[216:219], v157 offset:6144
VC 148,124,125
Mva 80,200,188
TR 214,3,19968
VC 149,126,127
v_mfma_f32_16x16x32_bf16 v[132:135], v[102:105], a[244:247], v[112:115]
VC 150,128,129
VC 151,130,131
v_mfma_f32_16x16x32_bf16 v[136:139], v[100:103], a[244:247], v[112:115]
TR 192,2,32768
s_waitcnt lgkmcnt(8)
Mva 36,204,176
ds_write_b128 v156, v[148:151] offset:20480
Mva 52,204,180
TR 194,2,33280
Mva 68,204,184
ds_read_b128 a[220:223], v157 offset:7168
VC 152,132,133
Mva 84,204,188
VC 153,134,135
VC 154,136,137
s_waitcnt lgkmcnt(7)
Mva 40,208,176
VC 155,138,139
TR 196,2,33792
Mva 56,208,180
ds_write_b128 v156, v[152:155] offset:21504
Mva 72,208,184
TR 198,2,34304
Mva 88,208,188
s_waitcnt lgkmcnt(7)
Mva 44,212,176
Mva 60,212,180
Mva 76,212,184
TR 200,3,32768
Mva 92,212,188
TR 202,3,33280
s_waitcnt lgkmcnt(3)
s_waitcnt vmcnt(4)
s_barrier
Mva 32,192,192
s_mov_b32 m0, s69
ds_read_b128 a[224:227], v157 offset:16384
Mva 48,192,196
s_mov_b32 s15, 4224
buffer_load_dwordx4 v1, s[16:19], s15 offen offset:0 lds
Mva 64,192,200
ds_read_b128 a[228:231], v157 offset:17408
Mva 80,192,204
TR 204,3,33792
v_mfma_f32_16x16x32_bf16 v[124:127], v[102:105], v[224:227], v[108:111]
s_mov_b32 s15, 3200
buffer_load_dwordx4 v1, s[20:23], s15 offen offset:1024 lds
v_mfma_f32_16x16x32_bf16 v[128:131], v[100:103], v[224:227], v[108:111]
TR 206,3,34304
s_waitcnt lgkmcnt(6)
Mva 36,196,192
ds_read_b128 a[232:235], v157 offset:18432
Mva 52,196,196
s_mov_b32 s15, 297088
buffer_load_dwordx4 v1, s[16:19], s15 offen offset:2048 lds
Mva 68,196,200
VC 140,124,125
VC 141,126,127
Mva 84,196,204
VC 142,128,129
VC 143,130,131
v_mfma_f32_16x16x32_bf16 v[132:135], v[102:105], v[228:231], v[112:115]
s_mov_b32 s15, 296064
buffer_load_dwordx4 v1, s[20:23], s15 offen offset:3072 lds
v_mfma_f32_16x16x32_bf16 v[136:139], v[100:103], v[228:231], v[112:115]
TR 208,2,34816
s_waitcnt lgkmcnt(6)
Mva 40,200,192
TR 210,2,35328
Mva 56,200,196
ds_write_b128 v156, v[140:143] offset:32768
Mva 72,200,200
ds_read_b128 a[236:239], v157 offset:19456
VC 144,132,133
Mva 88,200,204
VC 145,134,135
VC 146,136,137
s_waitcnt lgkmcnt(5)
Mva 44,204,192
VC 147,138,139
TR 212,2,35840
Mva 60,204,196
ds_read_b128 a[240:243], v157 offset:20480
Mva 76,204,200
TR 214,2,36352
Mva 92,204,204
ds_write_b128 v156, v[144:147] offset:33792
v_mfma_f32_16x16x32_bf16 v[124:127], v[102:105], v[240:243], v[108:111]
TR 192,3,34816
v_mfma_f32_16x16x32_bf16 v[128:131], v[100:103], v[240:243], v[108:111]
ds_read_b128 a[244:247], v157 offset:21504
s_waitcnt lgkmcnt(8)
Mva 32,208,208
TR 194,3,35328
Mva 48,208,212
TR 196,3,35840
Mva 64,208,216
ds_read_b128 a[248:251], v157 offset:22528
VC 148,124,125
Mva 80,208,220
TR 198,3,36352
VC 149,126,127
v_mfma_f32_16x16x32_bf16 v[132:135], v[102:105], v[244:247], v[112:115]
VC 150,128,129
VC 151,130,131
v_mfma_f32_16x16x32_bf16 v[136:139], v[100:103], v[244:247], v[112:115]
TR 200,2,49152
s_waitcnt lgkmcnt(8)
Mva 36,212,208
ds_write_b128 v156, v[148:151] offset:36864
Mva 52,212,212
TR 202,2,49664
Mva 68,212,216
ds_read_b128 a[252:255], v157 offset:23552
VC 152,132,133
Mva 84,212,220
VC 153,134,135
VC 154,136,137
s_waitcnt lgkmcnt(7)
Mva 40,192,208
VC 155,138,139
TR 204,2,50176
Mva 56,192,212
TR 206,2,50688
Mva 72,192,216
ds_write_b128 v156, v[152:155] offset:37888
Mva 88,192,220
TR 208,3,49152
s_waitcnt lgkmcnt(8)
Mva 44,196,208
TR 210,3,49664
Mva 60,196,212
Mva 76,196,216
TR 212,3,50176
Mva 92,196,220
TR 214,3,50688
s_waitcnt lgkmcnt(4)
s_waitcnt vmcnt(4)
s_barrier
Mva 32,200,224
s_mov_b32 m0, s70
ds_read_b128 v[224:227], v157 offset:32768
Mva 48,200,228
s_mov_b32 s15, 594048
buffer_load_dwordx4 v1, s[16:19], s15 offen offset:0 lds
Mva 64,200,232
ds_read_b128 v[228:231], v157 offset:33792
Mva 80,200,236
TR 192,2,51200
Mva 36,204,224
s_mov_b32 s15, 593024
buffer_load_dwordx4 v1, s[20:23], s15 offen offset:1024 lds
Mva 52,204,228
ds_read_b128 v[232:235], v157 offset:34816
Mva 68,204,232
TR 194,2,51712
Mva 84,204,236
s_mov_b32 s15, 886912
buffer_load_dwordx4 v1, s[16:19], s15 offen offset:2048 lds
s_waitcnt lgkmcnt(7)
Mva 40,208,224
TR 196,2,52224
Mva 56,208,228
ds_read_b128 v[236:239], v157 offset:35840
Mva 72,208,232
s_mov_b32 s15, 885888
buffer_load_dwordx4 v1, s[20:23], s15 offen offset:3072 lds
Mva 88,208,236
TR 198,2,52736
s_waitcnt lgkmcnt(8)
Mva 44,212,224
ds_read_b128 v[240:243], v157 offset:36864
Mva 60,212,228
TR 200,3,51200
Mva 76,212,232
TR 202,3,51712
Mva 92,212,236
ds_read_b128 v[244:247], v157 offset:37888
s_waitcnt lgkmcnt(7)
Mva 32,192,240
TR 204,3,52224
Mva 48,192,244
)"
R"(TR 206,3,52736
Mva 64,192,248
ds_read_b128 v[248:251], v157 offset:38912
Mva 80,192,252
TR 208,2,0
s_waitcnt lgkmcnt(8)
Mva 36,196,240
TR 210,2,512
Mva 52,196,244
ds_read_b128 v[252:255], v157 offset:39936
Mva 68,196,248
TR 212,2,1024
Mva 84,196,252
TR 214,2,1536
s_waitcnt lgkmcnt(9)
Mva 40,200,240
Mva 56,200,244
Mva 72,200,248
TR 192,3,0
Mva 88,200,252
TR 194,3,512
s_waitcnt lgkmcnt(8)
Mva 44,204,240
Mva 60,204,244
Mva 76,204,248
TR 196,3,1024
Mva 92,204,252
TR 198,3,1536
s_waitcnt vmcnt(4)
s_barrier
s_waitcnt lgkmcnt(7)
Mvv 32,208,224
s_mov_b32 m0, s71
Mvv 48,208,228
s_mov_b32 s15, 1183872
buffer_load_dwordx4 v1, s[16:19], s15 offen offset:0 lds
Mvv 64,208,232
TR 200,2,2048
Mvv 80,208,236
TR 202,2,2560
s_waitcnt lgkmcnt(6)
Mvv 36,212,224
s_mov_b32 s15, 1182848
buffer_load_dwordx4 v1, s[20:23], s15 offen offset:1024 lds
Mvv 52,212,228
Mvv 68,212,232
TR 204,2,3072
Mvv 84,212,236
s_mov_b32 s15, 1476736
buffer_load_dwordx4 v1, s[16:19], s15 offen offset:2048 lds
s_waitcnt lgkmcnt(5)
Mvv 40,192,224
TR 206,2,3584
Mvv 56,192,228
Mvv 72,192,232
s_mov_b32 s15, 1475712
buffer_load_dwordx4 v1, s[20:23], s15 offen offset:3072 lds
Mvv 88,192,236
TR 208,3,2048
s_waitcnt lgkmcnt(5)
Mvv 44,196,224
TR 210,3,2560
Mvv 60,196,228
Mvv 76,196,232
TR 212,3,3072
Mvv 92,196,236
TR 214,3,3584
s_waitcnt lgkmcnt(6)
Mvv 32,200,240
Mvv 48,200,244
Mvv 64,200,248
TR 192,2,16384
Mvv 80,200,252
TR 194,2,16896
s_waitcnt lgkmcnt(6)
Mvv 36,204,240
Mvv 52,204,244
Mvv 68,204,248
TR 196,2,17408
Mvv 84,204,252
TR 198,2,17920
s_waitcnt lgkmcnt(6)
Mvv 40,208,240
Mvv 56,208,244
Mvv 72,208,248
TR 200,3,16384
Mvv 88,208,252
TR 202,3,16896
s_waitcnt lgkmcnt(6)
Mvv 44,212,240
Mvv 60,212,244
Mvv 76,212,248
TR 204,3,17408
Mvv 92,212,252
TR 206,3,17920
s_waitcnt vmcnt(4)
s_barrier
s_waitcnt lgkmcnt(6)
Zva 96,192,0
s_mov_b32 m0, s68
Zva 112,192,4
s_mov_b32 s15, 1773696
buffer_load_dwordx4 v1, s[16:19], s15 offen offset:0 lds
Zva 128,192,8
VM 36,32,36
VM 37,33,37
Zva 144,192,12
VM 38,34,38
VM 32,6,32
s_waitcnt lgkmcnt(4)
Zva 100,196,0
s_mov_b32 s15, 1772672
buffer_load_dwordx4 v1, s[20:23], s15 offen offset:1024 lds
Zva 116,196,4
VM 39,35,39
VM 33,6,33
Zva 132,196,8
TR 208,2,18432
VM 44,40,44
Zva 148,196,12
s_mov_b32 s15, 2066560
buffer_load_dwordx4 v1, s[16:19], s15 offen offset:2048 lds
s_waitcnt lgkmcnt(3)
Zva 104,200,0
TR 210,2,18944
VM 34,6,34
Zva 120,200,4
v_exp_f32 v32, v32
Zva 136,200,8
s_mov_b32 s15, 2065536
buffer_load_dwordx4 v1, s[20:23], s15 offen offset:3072 lds
Zva 152,200,12
TR 212,2,19456
VM 45,41,45
s_waitcnt lgkmcnt(3)
Zva 108,204,0
TR 214,2,19968
VM 35,6,35
Zva 124,204,4
v_exp_f32 v33, v33
Zva 140,204,8
TR 192,3,18432
VM 46,42,46
Zva 156,204,12
TR 194,3,18944
VM 40,6,40
s_waitcnt lgkmcnt(4)
Mva 96,208,16
v_exp_f32 v34, v34
Mva 112,208,20
TR 196,3,19456
VF 32,32,10,10
Mva 128,208,24
TR 198,3,19968
VM 47,43,47
Mva 144,208,28
VM 41,6,41
TR 200,2,32768
s_waitcnt lgkmcnt(5)
Mva 100,212,16
v_exp_f32 v35, v35
Mva 116,212,20
TR 202,2,33280
VF 33,33,10,10
Mva 132,212,24
VM 42,6,42
TR 204,2,33792
Mva 148,212,28
v_exp_f32 v40, v40
s_waitcnt lgkmcnt(5)
Mva 104,192,16
TR 206,2,34304
VF 34,34,10,10
Mva 120,192,20
v_rcp_f32 v32, v32
Mva 136,192,24
TR 208,3,32768
VM 43,6,43
Mva 152,192,28
TR 210,3,33280
s_waitcnt lgkmcnt(6)
Mva 108,196,16
v_exp_f32 v41, v41
Mva 124,196,20
VF 35,35,10,10
Mva 140,196,24
TR 212,3,33792
Mva 156,196,28
TR 214,3,34304
s_waitcnt vmcnt(4)
s_barrier
s_waitcnt lgkmcnt(6)
Mva 96,200,32
s_mov_b32 m0, s69
Mva 112,200,36
s_mov_b32 s15, 2363520
buffer_load_dwordx4 v1, s[16:19], s15 offen offset:0 lds
Mva 128,200,40
v_rcp_f32 v33, v33
Mva 144,200,44
v_exp_f32 v42, v42
s_waitcnt lgkmcnt(4)
Mva 100,204,32
s_mov_b32 s15, 2362496
buffer_load_dwordx4 v1, s[20:23], s15 offen offset:1024 lds
Mva 116,204,36
TR 192,2,34816
VF 40,40,10,10
Mva 132,204,40
TR 194,2,35328
Mva 148,204,44
s_mov_b32 s15, 2656384
buffer_load_dwordx4 v1, s[16:19], s15 offen offset:2048 lds
s_waitcnt lgkmcnt(4)
Mva 104,208,32
v_rcp_f32 v34, v34
Mva 120,208,36
VM 36,36,32
TR 196,2,35840
Mva 136,208,40
s_mov_b32 s15, 2655360
buffer_load_dwordx4 v1, s[20:23], s15 offen offset:3072 lds
Mva 152,208,44
TR 198,2,36352
s_waitcnt lgkmcnt(4)
Mva 108,212,32
v_exp_f32 v43, v43
Mva 124,212,36
VF 41,41,10,10
TR 200,3,34816
Mva 140,212,40
v_rcp_f32 v35, v35
Mva 156,212,44
TR 202,3,35328
VM 37,37,33
s_waitcnt lgkmcnt(4)
Mva 96,192,48
VF 42,42,10,10
TR 204,3,35840
Mva 112,192,52
v_rcp_f32 v40, v40
Mva 128,192,56
TR 206,3,36352
VM 38,38,34
Mva 144,192,60
VF 43,43,10,10
TR 208,2,49152
s_waitcnt lgkmcnt(5)
Mva 100,196,48
v_rcp_f32 v41, v41
Mva 116,196,52
TR 210,2,49664
VM 39,39,35
Mva 132,196,56
v_rcp_f32 v42, v42
Mva 148,196,60
TR 212,2,50176
VM 44,44,40
s_waitcnt lgkmcnt(5)
Mva 104,200,48
TR 214,2,50688
Mva 120,200,52
v_rcp_f32 v43, v43
Mva 136,200,56
VM 45,45,41
TR 192,3,49152
Mva 152,200,60
VM 46,46,42
TR 194,3,49664
s_waitcnt lgkmcnt(6)
Mva 108,204,48
VM 47,47,43
VC 32,36,37
Mva 124,204,52
VC 33,38,39
VC 34,44,45
Mva 140,204,56
TR 196,3,50176
VC 35,46,47
Mva 156,204,60
TR 198,3,50688
s_waitcnt vmcnt(4)
s_barrier
s_waitcnt lgkmcnt(6)
Mva 96,208,64
s_mov_b32 m0, s70
Mva 112,208,68
s_mov_b32 s15, 2953344
buffer_load_dwordx4 v1, s[16:19], s15 offen offset:0 lds
Mva 128,208,72
TR 200,2,51200
Mva 144,208,76
ds_write_b128 v5, v[32:35] offset:0
s_waitcnt lgkmcnt(6)
Mva 100,212,64
s_mov_b32 s15, 2952320
buffer_load_dwordx4 v1, s[20:23], s15 offen offset:1024 lds
Mva 116,212,68
TR 202,2,51712
VM 52,48,52
Mva 132,212,72
VM 53,49,53
VM 54,50,54
Mva 148,212,76
s_mov_b32 s15, 3246208
buffer_load_dwordx4 v1, s[16:19], s15 offen offset:2048 lds
s_waitcnt lgkmcnt(5)
Mva 104,192,64
TR 204,2,52224
VM 48,7,48
Mva 120,192,68
TR 206,2,52736
VM 55,51,55
Mva 136,192,72
s_mov_b32 s15, 3245184
buffer_load_dwordx4 v1, s[20:23], s15 offen offset:3072 lds
Mva 152,192,76
VM 49,7,49
VM 60,56,60
s_waitcnt lgkmcnt(5)
Mva 108,196,64
TR 208,3,51200
VM 50,7,50
Mva 124,196,68
TR 210,3,51712
Mva 140,196,72
v_exp_f32 v48, v48
Mva 156,196,76
VM 61,57,61
TR 212,3,52224
s_waitcnt lgkmcnt(5)
Mva 96,200,80
VM 51,7,51
TR 214,3,52736
Mva 112,200,84
v_exp_f32 v49, v49
Mva 128,200,88
VM 62,58,62
TR 192,2,0
Mva 144,200,92
VM 56,7,56
TR 194,2,512
s_waitcnt lgkmcnt(6)
Mva 100,204,80
v_exp_f32 v50, v50
Mva 116,204,84
VF 48,48,11,11
VM 63,59,63
Mva 132,204,88
TR 196,2,1024
VM 57,7,57
Mva 148,204,92
TR 198,2,1536
s_waitcnt lgkmcnt(6)
Mva 104,208,80
v_exp_f32 v51, v51
Mva 120,208,84
VF 49,49,11,11
VM 58,7,58
Mva 136,208,88
TR 200,3,0
Mva 152,208,92
TR 202,3,512
s_waitcnt lgkmcnt(6)
Mva 108,212,80
v_exp_f32 v56, v56
Mva 124,212,84
VF 50,50,11,11
Mva 140,212,88
TR 204,3,1024
Mva 156,212,92
TR 206,3,1536
s_waitcnt vmcnt(4)
s_barrier
s_waitcnt lgkmcnt(6)
Mva 96,192,96
s_mov_b32 m0, s71
Mva 112,192,100
s_mov_b32 s15, 3543168
buffer_load_dwordx4 v1, s[16:19], s15 offen offset:0 lds
Mva 128,192,104
v_rcp_f32 v48, v48
Mva 144,192,108
VM 59,7,59
TR 208,2,2048
s_waitcnt lgkmcnt(5)
Mva 100,196,96
s_mov_b32 s15, 3542144
buffer_load_dwordx4 v1, s[20:23], s15 offen offset:1024 lds
Mva 116,196,100
TR 210,2,2560
Mva 132,196,104
v_exp_f32 v57, v57
Mva 148,196,108
s_mov_b32 s15, 3836032
buffer_load_dwordx4 v1, s[16:19], s15 offen offset:2048 lds
s_waitcnt lgkmcnt(4)
Mva 104,200,96
VF 51,51,11,11
TR 212,2,3072
Mva 120,200,100
v_rcp_f32 v49, v49
Mva 136,200,104
s_mov_b32 s15, 3835008
buffer_load_dwordx4 v1, s[20:23], s15 offen offset:3072 lds
Mva 152,200,108
TR 214,2,3584
s_waitcnt lgkmcnt(4)
Mva 108,204,96
v_exp_f32 v58, v58
Mva 124,204,100
VF 56,56,11,11
TR 192,3,2048
Mva 140,204,104
TR 194,3,2560
Mva 156,204,108
v_rcp_f32 v50, v50
s_waitcnt lgkmcnt(4)
Mva 96,208,112
VM 52,52,48
TR 196,3,3072
Mva 112,208,116
v_exp_f32 v59, v59
Mva 128,208,120
TR 198,3,3584
VF 57,57,11,11
Mva 144,208,124
v_rcp_f32 v51, v51
s_waitcnt lgkmcnt(4)
Mva 100,212,112
TR 200,2,16384
VM 53,53,49
Mva 116,212,116
TR 202,2,16896
VF 58,58,11,11
Mva 132,212,120
v_rcp_f32 v56, v56
Mva 148,212,124
TR 204,2,17408
VM 54,54,50
s_waitcnt lgkmcnt(5)
Mva 104,192,112
TR 206,2,17920
VF 59,59,11,11
Mva 120,192,116
v_rcp_f32 v57, v57
Mva 136,192,120
TR 208,3,16384
VM 55,55,51
Mva 152,192,124
TR 210,3,16896
s_waitcnt lgkmcnt(6)
Mva 108,196,112
v_rcp_f32 v58, v58
Mva 124,196,116
VM 60,60,56
Mva 140,196,120
TR 212,3,17408
Mva 156,196,124
TR 214,3,17920
s_waitcnt vmcnt(4)
s_barrier
s_waitcnt lgkmcnt(6)
Mva 96,200,128
s_mov_b32 m0, s68
Mva 112,200,132
s_mov_b32 s15, 4132992
buffer_load_dwordx4 v1, s[16:19], s15 offen offset:0 lds
Mva 128,200,136
v_rcp_f32 v59, v59
Mva 144,200,140
VM 61,61,57
VM 62,62,58
s_waitcnt lgkmcnt(4)
Mva 100,204,128
s_mov_b32 s15, 4131968
buffer_load_dwordx4 v1, s[20:23], s15 offen offset:1024 lds
Mva 116,204,132
TR 192,2,18432
VM 63,63,59
Mva 132,204,136
TR 194,2,18944
VC 48,52,53
Mva 148,204,140
s_mov_b32 s15, 4425856
buffer_load_dwordx4 v1, s[16:19], s15 offen offset:2048 lds
s_waitcnt lgkmcnt(4)
Mva 104,208,128
VC 49,54,55
VC 50,60,61
Mva 120,208,132
TR 196,2,19456
VC 51,62,63
Mva 136,208,136
s_mov_b32 s15, 4424832
buffer_load_dwordx4 v1, s[20:23], s15 offen offset:3072 lds
Mva 152,208,140
TR 198,2,19968
s_waitcnt lgkmcnt(4)
Mva 108,212,128
ds_write_b128 v5, v[48:51] offset:1024
Mva 124,212,132
TR 200,3,18432
VM 68,64,68
Mva 140,212,136
TR 202,3,18944
VM 69,65,69
Mva 156,212,140
VM 70,66,70
VM 64,8,64
s_waitcnt lgkmcnt(5)
Mva 96,192,144
TR 204,3,19456
VM 71,67,71
Mva 112,192,148
TR 206,3,19968
VM 65,8,65
Mva 128,192,152
VM 76,72,76
VM 66,8,66
Mva 144,192,156
TR 208,2,32768
s_waitcnt lgkmcnt(6)
Mva 100,196,144
TR 210,2,33280
Mva 116,196,148
v_exp_f32 v64, v64
Mva 132,196,152
VM 77,73,77
VM 67,8,67
Mva 148,196,156
TR 212,2,33792
s_waitcnt lgkmcnt(5)
Mva 104,200,144
TR 214,2,34304
Mva 120,200,148
v_exp_f32 v65, v65
Mva 136,200,152
VM 78,74,78
VM 72,8,72
Mva 152,200,156
TR 192,3,32768
s_waitcnt lgkmcnt(5)
Mva 108,204,144
TR 194,3,33280
Mva 124,204,148
v_exp_f32 v66, v66
Mva 140,204,152
VF 64,64,12,12
VM 79,75,79
Mva 156,204,156
TR 196,3,33792
VM 73,8,73
s_waitcnt vmcnt(4)
s_barrier
s_waitcnt lgkmcnt(5)
Mva 96,208,160
s_mov_b32 m0, s69
TR 198,3,34304
Mva 112,208,164
s_mov_b32 s15, 4722816
buffer_load_dwordx4 v1, s[16:19], s15 offen offset:0 lds
Mva 128,208,168
v_exp_f32 v67, v67
Mva 144,208,172
VF 65,65,12,12
TR 200,2,34816
s_waitcnt lgkmcnt(5)
Mva 100,212,160
s_mov_b32 s15, 4721792
buffer_load_dwordx4 v1, s[20:23], s15 offen offset:1024 lds
Mva 116,212,164
VM 74,8,74
TR 202,2,35328
Mva 132,212,168
v_exp_f32 v72, v72
Mva 148,212,172
s_mov_b32 s15, 5015680
buffer_load_dwordx4 v1, s[16:19], s15 offen offset:2048 lds
s_waitcnt lgkmcnt(4)
Mva 104,192,160
VF 66,66,12,12
TR 204,2,35840
Mva 120,192,164
TR 206,2,36352
Mva 136,192,168
s_mov_b32 s15, 5014656
buffer_load_dwordx4 v1, s[20:23], s15 offen offset:3072 lds
Mva 152,192,172
v_rcp_f32 v64, v64
s_waitcnt lgkmcnt(4)
Mva 108,196,160
VM 75,8,75
TR 208,3,34816
Mva 124,196,164
v_exp_f32 v73, v73
Mva 140,196,168
TR 210,3,35328
VF 67,67,12,12
Mva 156,196,172
)"
R"(v_rcp_f32 v65, v65
s_waitcnt lgkmcnt(4)
Mva 96,200,176
TR 212,3,35840
Mva 112,200,180
TR 214,3,36352
Mva 128,200,184
v_exp_f32 v74, v74
Mva 144,200,188
VF 72,72,12,12
TR 192,2,49152
s_waitcnt lgkmcnt(5)
Mva 100,204,176
v_rcp_f32 v66, v66
Mva 116,204,180
TR 194,2,49664
VM 68,68,64
Mva 132,204,184
v_exp_f32 v75, v75
Mva 148,204,188
TR 196,2,50176
VF 73,73,12,12
s_waitcnt lgkmcnt(5)
Mva 104,208,176
TR 198,2,50688
Mva 120,208,180
v_rcp_f32 v67, v67
Mva 136,208,184
VM 69,69,65
TR 200,3,49152
Mva 152,208,188
VF 74,74,12,12
TR 202,3,49664
s_waitcnt lgkmcnt(6)
Mva 108,212,176
v_rcp_f32 v72, v72
Mva 124,212,180
VM 70,70,66
VF 75,75,12,12
Mva 140,212,184
TR 204,3,50176
Mva 156,212,188
TR 206,3,50688
s_waitcnt vmcnt(4)
s_barrier
s_waitcnt lgkmcnt(6)
Mva 96,192,192
s_mov_b32 m0, s70
Mva 112,192,196
s_mov_b32 s15, 4352
buffer_load_dwordx4 v1, s[16:19], s15 offen offset:0 lds
Mva 128,192,200
v_rcp_f32 v73, v73
Mva 144,192,204
VM 71,71,67
TR 208,2,51200
s_waitcnt lgkmcnt(5)
Mva 100,196,192
s_mov_b32 s15, 3328
buffer_load_dwordx4 v1, s[20:23], s15 offen offset:1024 lds
Mva 116,196,196
TR 210,2,51712
Mva 132,196,200
v_rcp_f32 v74, v74
Mva 148,196,204
s_mov_b32 s15, 297216
buffer_load_dwordx4 v1, s[16:19], s15 offen offset:2048 lds
s_waitcnt lgkmcnt(4)
Mva 104,200,192
VM 76,76,72
TR 212,2,52224
Mva 120,200,196
v_rcp_f32 v75, v75
Mva 136,200,200
s_mov_b32 s15, 296192
buffer_load_dwordx4 v1, s[20:23], s15 offen offset:3072 lds
Mva 152,200,204
TR 214,2,52736
VM 77,77,73
s_waitcnt lgkmcnt(4)
Mva 108,204,192
VM 78,78,74
VM 79,79,75
Mva 124,204,196
TR 192,3,51200
VC 64,68,69
Mva 140,204,200
TR 194,3,51712
VC 65,70,71
Mva 156,204,204
VC 66,76,77
VC 67,78,79
s_waitcnt lgkmcnt(4)
Mva 96,208,208
TR 196,3,52224
Mva 112,208,212
TR 198,3,52736
Mva 128,208,216
ds_write_b128 v5, v[64:67] offset:2048
Mva 144,208,220
VM 84,80,84
TR 200,2,0
s_waitcnt lgkmcnt(6)
Mva 100,212,208
VM 85,81,85
TR 202,2,512
Mva 116,212,212
VM 86,82,86
VM 80,9,80
Mva 132,212,216
VM 87,83,87
TR 204,2,1024
Mva 148,212,220
VM 81,9,81
TR 206,2,1536
s_waitcnt lgkmcnt(7)
Mva 104,192,208
VM 92,88,92
VM 82,9,82
Mva 120,192,212
v_exp_f32 v80, v80
Mva 136,192,216
TR 208,3,0
VM 93,89,93
Mva 152,192,220
TR 210,3,512
VM 83,9,83
s_waitcnt lgkmcnt(7)
Mva 108,196,208
v_exp_f32 v81, v81
Mva 124,196,212
VM 94,90,94
VM 88,9,88
Mva 140,196,216
TR 212,3,1024
Mva 156,196,220
TR 214,3,1536
s_waitcnt vmcnt(4)
s_barrier
s_waitcnt lgkmcnt(6)
Mva 96,200,224
s_mov_b32 m0, s71
Mva 112,200,228
s_mov_b32 s15, 594176
buffer_load_dwordx4 v1, s[16:19], s15 offen offset:0 lds
Mva 128,200,232
TR 192,2,2048
Mva 144,200,236
v_exp_f32 v82, v82
s_waitcnt lgkmcnt(5)
Mva 100,204,224
s_mov_b32 s15, 593152
buffer_load_dwordx4 v1, s[20:23], s15 offen offset:1024 lds
Mva 116,204,228
TR 194,2,2560
VF 80,80,13,13
Mva 132,204,232
VM 95,91,95
VM 89,9,89
Mva 148,204,236
s_mov_b32 s15, 887040
buffer_load_dwordx4 v1, s[16:19], s15 offen offset:2048 lds
s_waitcnt lgkmcnt(4)
Mva 104,208,224
TR 196,2,3072
Mva 120,208,228
TR 198,2,3584
Mva 136,208,232
s_mov_b32 s15, 886016
buffer_load_dwordx4 v1, s[20:23], s15 offen offset:3072 lds
Mva 152,208,236
v_exp_f32 v83, v83
s_waitcnt lgkmcnt(4)
Mva 108,212,224
VF 81,81,13,13
VM 90,9,90
Mva 124,212,228
TR 200,3,2048
Mva 140,212,232
TR 202,3,2560
Mva 156,212,236
v_exp_f32 v88, v88
s_waitcnt lgkmcnt(4)
Mva 96,192,240
VF 82,82,13,13
TR 204,3,3072
Mva 112,192,244
v_rcp_f32 v80, v80
Mva 128,192,248
TR 206,3,3584
VM 91,9,91
Mva 144,192,252
v_exp_f32 v89, v89
s_waitcnt lgkmcnt(4)
Mva 100,196,240
TR 208,2,16384
VF 83,83,13,13
Mva 116,196,244
TR 210,2,16896
Mva 132,196,248
v_rcp_f32 v81, v81
Mva 148,196,252
TR 212,2,17408
s_waitcnt lgkmcnt(5)
Mva 104,200,240
v_exp_f32 v90, v90
Mva 120,200,244
TR 214,2,17920
VF 88,88,13,13
Mva 136,200,248
v_rcp_f32 v82, v82
Mva 152,200,252
TR 192,3,16384
VM 84,84,80
s_waitcnt lgkmcnt(5)
Mva 108,204,240
TR 194,3,16896
Mva 124,204,244
v_exp_f32 v91, v91
Mva 140,204,248
VF 89,89,13,13
TR 196,3,17408
Mva 156,204,252
TR 198,3,17920
s_waitcnt vmcnt(4)
s_barrier
s_waitcnt lgkmcnt(6)
Mvv 96,208,224
s_mov_b32 m0, s68
Mvv 112,208,228
s_mov_b32 s15, 1184000
buffer_load_dwordx4 v1, s[16:19], s15 offen offset:0 lds
Mvv 128,208,232
v_rcp_f32 v83, v83
Mvv 144,208,236
VM 85,85,81
VF 90,90,13,13
s_waitcnt lgkmcnt(4)
Mvv 100,212,224
s_mov_b32 s15, 1182976
buffer_load_dwordx4 v1, s[20:23], s15 offen offset:1024 lds
Mvv 116,212,228
TR 200,2,18432
Mvv 132,212,232
TR 202,2,18944
Mvv 148,212,236
s_mov_b32 s15, 1476864
buffer_load_dwordx4 v1, s[16:19], s15 offen offset:2048 lds
s_waitcnt lgkmcnt(4)
Mvv 104,192,224
v_rcp_f32 v88, v88
Mvv 120,192,228
VM 86,86,82
VF 91,91,13,13
Mvv 136,192,232
s_mov_b32 s15, 1475840
buffer_load_dwordx4 v1, s[20:23], s15 offen offset:3072 lds
Mvv 152,192,236
TR 204,2,19456
s_waitcnt lgkmcnt(3)
Mvv 108,196,224
TR 206,2,19968
Mvv 124,196,228
v_rcp_f32 v89, v89
Mvv 140,196,232
VM 87,87,83
TR 208,3,18432
Mvv 156,196,236
v_rcp_f32 v90, v90
s_waitcnt lgkmcnt(3)
Mvv 96,200,240
TR 210,3,18944
VM 92,92,88
Mvv 112,200,244
v_rcp_f32 v91, v91
Mvv 128,200,248
TR 212,3,19456
VM 93,93,89
Mvv 144,200,252
TR 214,3,19968
VM 94,94,90
s_waitcnt lgkmcnt(4)
Mvv 100,204,240
VM 95,95,91
VC 80,84,85
Mvv 116,204,244
TR 192,2,32768
VC 81,86,87
Mvv 132,204,248
TR 194,2,33280
VC 82,92,93
Mvv 148,204,252
VC 83,94,95
TR 196,2,33792
s_waitcnt lgkmcnt(5)
Mvv 104,208,240
TR 198,2,34304
Mvv 120,208,244
ds_write_b128 v5, v[80:83] offset:3072
Mvv 136,208,248
TR 200,3,32768
Mvv 152,208,252
TR 202,3,33280
s_waitcnt lgkmcnt(7)
Mvv 108,212,240
Mvv 124,212,244
Mvv 140,212,248
TR 204,3,33792
Mvv 156,212,252
TR 206,3,34304
s_waitcnt lgkmcnt(4)
s_waitcnt vmcnt(4)
s_barrier
Zva 32,192,0
s_mov_b32 m0, s69
Zva 48,192,4
s_mov_b32 s15, 1773824
buffer_load_dwordx4 v1, s[16:19], s15 offen offset:0 lds
Zva 64,192,8
VM 100,96,100
VM 101,97,101
Zva 80,192,12
VM 102,98,102
VM 96,6,96
Zva 36,196,0
s_mov_b32 s15, 1772800
buffer_load_dwordx4 v1, s[20:23], s15 offen offset:1024 lds
Zva 52,196,4
VM 103,99,103
VM 97,6,97
Zva 68,196,8
TR 208,2,34816
VM 108,104,108
Zva 84,196,12
s_mov_b32 s15, 2066688
buffer_load_dwordx4 v1, s[16:19], s15 offen offset:2048 lds
s_waitcnt lgkmcnt(3)
Zva 40,200,0
TR 210,2,35328
VM 98,6,98
Zva 56,200,4
v_exp_f32 v96, v96
Zva 72,200,8
s_mov_b32 s15, 2065664
buffer_load_dwordx4 v1, s[20:23], s15 offen offset:3072 lds
Zva 88,200,12
TR 212,2,35840
VM 109,105,109
s_waitcnt lgkmcnt(3)
Zva 44,204,0
TR 214,2,36352
VM 99,6,99
Zva 60,204,4
v_exp_f32 v97, v97
Zva 76,204,8
TR 192,3,34816
VM 110,106,110
Zva 92,204,12
TR 194,3,35328
VM 104,6,104
s_waitcnt lgkmcnt(4)
Mva 32,208,16
v_exp_f32 v98, v98
Mva 48,208,20
TR 196,3,35840
VF 96,96,10,10
Mva 64,208,24
TR 198,3,36352
VM 111,107,111
Mva 80,208,28
VM 105,6,105
TR 200,2,49152
s_waitcnt lgkmcnt(5)
Mva 36,212,16
v_exp_f32 v99, v99
Mva 52,212,20
TR 202,2,49664
VF 97,97,10,10
Mva 68,212,24
VM 106,6,106
TR 204,2,50176
Mva 84,212,28
v_exp_f32 v104, v104
s_waitcnt lgkmcnt(5)
Mva 40,192,16
TR 206,2,50688
VF 98,98,10,10
Mva 56,192,20
v_rcp_f32 v96, v96
Mva 72,192,24
TR 208,3,49152
VM 107,6,107
Mva 88,192,28
TR 210,3,49664
s_waitcnt lgkmcnt(6)
Mva 44,196,16
v_exp_f32 v105, v105
Mva 60,196,20
VF 99,99,10,10
Mva 76,196,24
TR 212,3,50176
Mva 92,196,28
TR 214,3,50688
s_waitcnt vmcnt(4)
s_barrier
s_waitcnt lgkmcnt(6)
Mva 32,200,32
s_mov_b32 m0, s70
Mva 48,200,36
s_mov_b32 s15, 2363648
buffer_load_dwordx4 v1, s[16:19], s15 offen offset:0 lds
Mva 64,200,40
v_rcp_f32 v97, v97
Mva 80,200,44
v_exp_f32 v106, v106
s_waitcnt lgkmcnt(4)
Mva 36,204,32
s_mov_b32 s15, 2362624
buffer_load_dwordx4 v1, s[20:23], s15 offen offset:1024 lds
Mva 52,204,36
TR 192,2,51200
VF 104,104,10,10
Mva 68,204,40
TR 194,2,51712
Mva 84,204,44
s_mov_b32 s15, 2656512
buffer_load_dwordx4 v1, s[16:19], s15 offen offset:2048 lds
s_waitcnt lgkmcnt(4)
Mva 40,208,32
v_rcp_f32 v98, v98
Mva 56,208,36
VM 100,100,96
TR 196,2,52224
Mva 72,208,40
s_mov_b32 s15, 2655488
buffer_load_dwordx4 v1, s[20:23], s15 offen offset:3072 lds
Mva 88,208,44
TR 198,2,52736
s_waitcnt lgkmcnt(4)
Mva 44,212,32
v_exp_f32 v107, v107
Mva 60,212,36
VF 105,105,10,10
TR 200,3,51200
Mva 76,212,40
v_rcp_f32 v99, v99
Mva 92,212,44
TR 202,3,51712
VM 101,101,97
s_waitcnt lgkmcnt(4)
Mva 32,192,48
VF 106,106,10,10
TR 204,3,52224
Mva 48,192,52
v_rcp_f32 v104, v104
Mva 64,192,56
TR 206,3,52736
VM 102,102,98
Mva 80,192,60
VF 107,107,10,10
TR 208,2,0
s_waitcnt lgkmcnt(5)
Mva 36,196,48
v_rcp_f32 v105, v105
Mva 52,196,52
TR 210,2,512
VM 103,103,99
Mva 68,196,56
v_rcp_f32 v106, v106
Mva 84,196,60
TR 212,2,1024
VM 108,108,104
s_waitcnt lgkmcnt(5)
Mva 40,200,48
TR 214,2,1536
Mva 56,200,52
v_rcp_f32 v107, v107
Mva 72,200,56
VM 109,109,105
TR 192,3,0
Mva 88,200,60
VM 110,110,106
TR 194,3,512
s_waitcnt lgkmcnt(6)
Mva 44,204,48
VM 111,111,107
VC 96,100,101
Mva 60,204,52
VC 97,102,103
VC 98,108,109
Mva 76,204,56
TR 196,3,1024
VC 99,110,111
Mva 92,204,60
TR 198,3,1536
s_waitcnt vmcnt(4)
s_barrier
s_waitcnt lgkmcnt(6)
Mva 32,208,64
s_mov_b32 m0, s71
Mva 48,208,68
s_mov_b32 s15, 2953472
buffer_load_dwordx4 v1, s[16:19], s15 offen offset:0 lds
Mva 64,208,72
TR 200,2,2048
Mva 80,208,76
ds_write_b128 v5, v[96:99] offset:16384
s_waitcnt lgkmcnt(6)
Mva 36,212,64
s_mov_b32 s15, 2952448
buffer_load_dwordx4 v1, s[20:23], s15 offen offset:1024 lds
Mva 52,212,68
TR 202,2,2560
VM 116,112,116
Mva 68,212,72
VM 117,113,117
VM 118,114,118
Mva 84,212,76
s_mov_b32 s15, 3246336
buffer_load_dwordx4 v1, s[16:19], s15 offen offset:2048 lds
s_waitcnt lgkmcnt(5)
Mva 40,192,64
TR 204,2,3072
VM 112,7,112
Mva 56,192,68
TR 206,2,3584
VM 119,115,119
Mva 72,192,72
s_mov_b32 s15, 3245312
buffer_load_dwordx4 v1, s[20:23], s15 offen offset:3072 lds
Mva 88,192,76
VM 113,7,113
VM 124,120,124
s_waitcnt lgkmcnt(5)
Mva 44,196,64
TR 208,3,2048
VM 114,7,114
Mva 60,196,68
TR 210,3,2560
Mva 76,196,72
v_exp_f32 v112, v112
Mva 92,196,76
VM 125,121,125
TR 212,3,3072
s_waitcnt lgkmcnt(5)
Mva 32,200,80
VM 115,7,115
TR 214,3,3584
Mva 48,200,84
v_exp_f32 v113, v113
Mva 64,200,88
VM 126,122,126
TR 192,2,16384
Mva 80,200,92
VM 120,7,120
TR 194,2,16896
s_waitcnt lgkmcnt(6)
Mva 36,204,80
v_exp_f32 v114, v114
Mva 52,204,84
VF 112,112,11,11
VM 127,123,127
Mva 68,204,88
TR 196,2,17408
VM 121,7,121
Mva 84,204,92
TR 198,2,17920
s_waitcnt lgkmcnt(6)
Mva 40,208,80
v_exp_f32 v115, v115
Mva 56,208,84
VF 113,113,11,11
VM 122,7,122
Mva 72,208,88
TR 200,3,16384
Mva 88,208,92
TR 202,3,16896
s_waitcnt lgkmcnt(6)
Mva 44,212,80
v_exp_f32 v120, v120
Mva 60,212,84
VF 114,114,11,11
Mva 76,212,88
TR 204,3,17408
Mva 92,212,92
TR 206,3,17920
s_waitcnt vmcnt(4)
s_barrier
s_waitcnt lgkmcnt(6)
Mva 32,192,96
s_mov_b32 m0, s68
Mva 48,192,100
s_mov_b32 s15, 3543296
buffer_load_dwordx4 v1, s[16:19], s15 offen offset:0 lds
Mva 64,192,104
v_rcp_f32 v112, v112
Mva 80,192,108
VM 123,7,123
TR 208,2,18432
s_waitcnt lgkmcnt(5)
)"
R"(Mva 36,196,96
s_mov_b32 s15, 3542272
buffer_load_dwordx4 v1, s[20:23], s15 offen offset:1024 lds
Mva 52,196,100
TR 210,2,18944
Mva 68,196,104
v_exp_f32 v121, v121
Mva 84,196,108
s_mov_b32 s15, 3836160
buffer_load_dwordx4 v1, s[16:19], s15 offen offset:2048 lds
s_waitcnt lgkmcnt(4)
Mva 40,200,96
VF 115,115,11,11
TR 212,2,19456
Mva 56,200,100
v_rcp_f32 v113, v113
Mva 72,200,104
s_mov_b32 s15, 3835136
buffer_load_dwordx4 v1, s[20:23], s15 offen offset:3072 lds
Mva 88,200,108
TR 214,2,19968
s_waitcnt lgkmcnt(4)
Mva 44,204,96
v_exp_f32 v122, v122
Mva 60,204,100
VF 120,120,11,11
TR 192,3,18432
Mva 76,204,104
TR 194,3,18944
Mva 92,204,108
v_rcp_f32 v114, v114
s_waitcnt lgkmcnt(4)
Mva 32,208,112
VM 116,116,112
TR 196,3,19456
Mva 48,208,116
v_exp_f32 v123, v123
Mva 64,208,120
TR 198,3,19968
VF 121,121,11,11
Mva 80,208,124
v_rcp_f32 v115, v115
s_waitcnt lgkmcnt(4)
Mva 36,212,112
TR 200,2,32768
VM 117,117,113
Mva 52,212,116
TR 202,2,33280
VF 122,122,11,11
Mva 68,212,120
v_rcp_f32 v120, v120
Mva 84,212,124
TR 204,2,33792
VM 118,118,114
s_waitcnt lgkmcnt(5)
Mva 40,192,112
TR 206,2,34304
VF 123,123,11,11
Mva 56,192,116
v_rcp_f32 v121, v121
Mva 72,192,120
TR 208,3,32768
VM 119,119,115
Mva 88,192,124
TR 210,3,33280
s_waitcnt lgkmcnt(6)
Mva 44,196,112
v_rcp_f32 v122, v122
Mva 60,196,116
VM 124,124,120
Mva 76,196,120
TR 212,3,33792
Mva 92,196,124
TR 214,3,34304
s_waitcnt vmcnt(4)
s_barrier
s_waitcnt lgkmcnt(6)
Mva 32,200,128
s_mov_b32 m0, s69
Mva 48,200,132
s_mov_b32 s15, 4133120
buffer_load_dwordx4 v1, s[16:19], s15 offen offset:0 lds
Mva 64,200,136
v_rcp_f32 v123, v123
Mva 80,200,140
VM 125,125,121
VM 126,126,122
s_waitcnt lgkmcnt(4)
Mva 36,204,128
s_mov_b32 s15, 4132096
buffer_load_dwordx4 v1, s[20:23], s15 offen offset:1024 lds
Mva 52,204,132
TR 192,2,34816
VM 127,127,123
Mva 68,204,136
TR 194,2,35328
VC 112,116,117
Mva 84,204,140
s_mov_b32 s15, 4425984
buffer_load_dwordx4 v1, s[16:19], s15 offen offset:2048 lds
s_waitcnt lgkmcnt(4)
Mva 40,208,128
VC 113,118,119
VC 114,124,125
Mva 56,208,132
TR 196,2,35840
VC 115,126,127
Mva 72,208,136
s_mov_b32 s15, 4424960
buffer_load_dwordx4 v1, s[20:23], s15 offen offset:3072 lds
Mva 88,208,140
TR 198,2,36352
s_waitcnt lgkmcnt(4)
Mva 44,212,128
ds_write_b128 v5, v[112:115] offset:17408
Mva 60,212,132
TR 200,3,34816
VM 132,128,132
Mva 76,212,136
TR 202,3,35328
VM 133,129,133
Mva 92,212,140
VM 134,130,134
VM 128,8,128
s_waitcnt lgkmcnt(5)
Mva 32,192,144
TR 204,3,35840
VM 135,131,135
Mva 48,192,148
TR 206,3,36352
VM 129,8,129
Mva 64,192,152
VM 140,136,140
VM 130,8,130
Mva 80,192,156
TR 208,2,49152
s_waitcnt lgkmcnt(6)
Mva 36,196,144
TR 210,2,49664
Mva 52,196,148
v_exp_f32 v128, v128
Mva 68,196,152
VM 141,137,141
VM 131,8,131
Mva 84,196,156
TR 212,2,50176
s_waitcnt lgkmcnt(5)
Mva 40,200,144
TR 214,2,50688
Mva 56,200,148
v_exp_f32 v129, v129
Mva 72,200,152
VM 142,138,142
VM 136,8,136
Mva 88,200,156
TR 192,3,49152
s_waitcnt lgkmcnt(5)
Mva 44,204,144
TR 194,3,49664
Mva 60,204,148
v_exp_f32 v130, v130
Mva 76,204,152
VF 128,128,12,12
VM 143,139,143
Mva 92,204,156
TR 196,3,50176
VM 137,8,137
s_waitcnt vmcnt(4)
s_barrier
s_waitcnt lgkmcnt(5)
Mva 32,208,160
s_mov_b32 m0, s70
TR 198,3,50688
Mva 48,208,164
s_mov_b32 s15, 4722944
buffer_load_dwordx4 v1, s[16:19], s15 offen offset:0 lds
Mva 64,208,168
v_exp_f32 v131, v131
Mva 80,208,172
VF 129,129,12,12
TR 200,2,51200
s_waitcnt lgkmcnt(5)
Mva 36,212,160
s_mov_b32 s15, 4721920
buffer_load_dwordx4 v1, s[20:23], s15 offen offset:1024 lds
Mva 52,212,164
VM 138,8,138
TR 202,2,51712
Mva 68,212,168
v_exp_f32 v136, v136
Mva 84,212,172
s_mov_b32 s15, 5015808
buffer_load_dwordx4 v1, s[16:19], s15 offen offset:2048 lds
s_waitcnt lgkmcnt(4)
Mva 40,192,160
VF 130,130,12,12
TR 204,2,52224
Mva 56,192,164
TR 206,2,52736
Mva 72,192,168
s_mov_b32 s15, 5014784
buffer_load_dwordx4 v1, s[20:23], s15 offen offset:3072 lds
Mva 88,192,172
v_rcp_f32 v128, v128
s_waitcnt lgkmcnt(4)
Mva 44,196,160
VM 139,8,139
TR 208,3,51200
Mva 60,196,164
v_exp_f32 v137, v137
Mva 76,196,168
TR 210,3,51712
VF 131,131,12,12
Mva 92,196,172
v_rcp_f32 v129, v129
s_waitcnt lgkmcnt(4)
Mva 32,200,176
TR 212,3,52224
Mva 48,200,180
TR 214,3,52736
Mva 64,200,184
v_exp_f32 v138, v138
Mva 80,200,188
VF 136,136,12,12
TR 192,2,0
s_waitcnt lgkmcnt(5)
Mva 36,204,176
v_rcp_f32 v130, v130
Mva 52,204,180
TR 194,2,512
VM 132,132,128
Mva 68,204,184
v_exp_f32 v139, v139
Mva 84,204,188
TR 196,2,1024
VF 137,137,12,12
s_waitcnt lgkmcnt(5)
Mva 40,208,176
TR 198,2,1536
Mva 56,208,180
v_rcp_f32 v131, v131
Mva 72,208,184
VM 133,133,129
TR 200,3,0
Mva 88,208,188
VF 138,138,12,12
TR 202,3,512
s_waitcnt lgkmcnt(6)
Mva 44,212,176
v_rcp_f32 v136, v136
Mva 60,212,180
VM 134,134,130
VF 139,139,12,12
Mva 76,212,184
TR 204,3,1024
Mva 92,212,188
TR 206,3,1536
s_waitcnt vmcnt(4)
s_barrier
s_waitcnt lgkmcnt(6)
Mva 32,192,192
s_mov_b32 m0, s71
Mva 48,192,196
s_mov_b32 s15, 4480
buffer_load_dwordx4 v1, s[16:19], s15 offen offset:0 lds
Mva 64,192,200
v_rcp_f32 v137, v137
Mva 80,192,204
VM 135,135,131
TR 208,2,2048
s_waitcnt lgkmcnt(5)
Mva 36,196,192
s_mov_b32 s15, 3456
buffer_load_dwordx4 v1, s[20:23], s15 offen offset:1024 lds
Mva 52,196,196
TR 210,2,2560
Mva 68,196,200
v_rcp_f32 v138, v138
Mva 84,196,204
s_mov_b32 s15, 297344
buffer_load_dwordx4 v1, s[16:19], s15 offen offset:2048 lds
s_waitcnt lgkmcnt(4)
Mva 40,200,192
VM 140,140,136
TR 212,2,3072
Mva 56,200,196
v_rcp_f32 v139, v139
Mva 72,200,200
s_mov_b32 s15, 296320
buffer_load_dwordx4 v1, s[20:23], s15 offen offset:3072 lds
Mva 88,200,204
TR 214,2,3584
VM 141,141,137
s_waitcnt lgkmcnt(4)
Mva 44,204,192
VM 142,142,138
VM 143,143,139
Mva 60,204,196
TR 192,3,2048
VC 128,132,133
Mva 76,204,200
TR 194,3,2560
VC 129,134,135
Mva 92,204,204
VC 130,140,141
VC 131,142,143
s_waitcnt lgkmcnt(4)
Mva 32,208,208
TR 196,3,3072
Mva 48,208,212
TR 198,3,3584
Mva 64,208,216
ds_write_b128 v5, v[128:131] offset:18432
Mva 80,208,220
VM 148,144,148
TR 200,2,16384
s_waitcnt lgkmcnt(6)
Mva 36,212,208
VM 149,145,149
TR 202,2,16896
Mva 52,212,212
VM 150,146,150
VM 144,9,144
Mva 68,212,216
VM 151,147,151
TR 204,2,17408
Mva 84,212,220
VM 145,9,145
TR 206,2,17920
s_waitcnt lgkmcnt(7)
Mva 40,192,208
VM 156,152,156
VM 146,9,146
Mva 56,192,212
v_exp_f32 v144, v144
Mva 72,192,216
TR 208,3,16384
VM 157,153,157
Mva 88,192,220
TR 210,3,16896
VM 147,9,147
s_waitcnt lgkmcnt(7)
Mva 44,196,208
v_exp_f32 v145, v145
Mva 60,196,212
VM 158,154,158
VM 152,9,152
Mva 76,196,216
TR 212,3,17408
Mva 92,196,220
TR 214,3,17920
s_waitcnt vmcnt(4)
s_barrier
s_waitcnt lgkmcnt(6)
Mva 32,200,224
s_mov_b32 m0, s68
Mva 48,200,228
s_mov_b32 s15, 594304
buffer_load_dwordx4 v1, s[16:19], s15 offen offset:0 lds
Mva 64,200,232
TR 192,2,18432
Mva 80,200,236
v_exp_f32 v146, v146
s_waitcnt lgkmcnt(5)
Mva 36,204,224
s_mov_b32 s15, 593280
buffer_load_dwordx4 v1, s[20:23], s15 offen offset:1024 lds
Mva 52,204,228
TR 194,2,18944
VF 144,144,13,13
Mva 68,204,232
VM 159,155,159
VM 153,9,153
Mva 84,204,236
s_mov_b32 s15, 887168
buffer_load_dwordx4 v1, s[16:19], s15 offen offset:2048 lds
s_waitcnt lgkmcnt(4)
Mva 40,208,224
TR 196,2,19456
Mva 56,208,228
TR 198,2,19968
Mva 72,208,232
s_mov_b32 s15, 886144
buffer_load_dwordx4 v1, s[20:23], s15 offen offset:3072 lds
Mva 88,208,236
v_exp_f32 v147, v147
s_waitcnt lgkmcnt(4)
Mva 44,212,224
VF 145,145,13,13
VM 154,9,154
Mva 60,212,228
TR 200,3,18432
Mva 76,212,232
TR 202,3,18944
Mva 92,212,236
v_exp_f32 v152, v152
s_waitcnt lgkmcnt(4)
Mva 32,192,240
VF 146,146,13,13
TR 204,3,19456
Mva 48,192,244
v_rcp_f32 v144, v144
Mva 64,192,248
TR 206,3,19968
VM 155,9,155
Mva 80,192,252
v_exp_f32 v153, v153
s_waitcnt lgkmcnt(4)
Mva 36,196,240
TR 208,2,32768
VF 147,147,13,13
Mva 52,196,244
TR 210,2,33280
Mva 68,196,248
v_rcp_f32 v145, v145
Mva 84,196,252
TR 212,2,33792
s_waitcnt lgkmcnt(5)
Mva 40,200,240
v_exp_f32 v154, v154
Mva 56,200,244
TR 214,2,34304
VF 152,152,13,13
Mva 72,200,248
v_rcp_f32 v146, v146
Mva 88,200,252
TR 192,3,32768
VM 148,148,144
s_waitcnt lgkmcnt(5)
Mva 44,204,240
TR 194,3,33280
Mva 60,204,244
v_exp_f32 v155, v155
Mva 76,204,248
VF 153,153,13,13
TR 196,3,33792
Mva 92,204,252
TR 198,3,34304
s_waitcnt vmcnt(4)
s_barrier
s_waitcnt lgkmcnt(6)
Mvv 32,208,224
s_mov_b32 m0, s69
Mvv 48,208,228
s_mov_b32 s15, 1184128
buffer_load_dwordx4 v1, s[16:19], s15 offen offset:0 lds
Mvv 64,208,232
v_rcp_f32 v147, v147
Mvv 80,208,236
VM 149,149,145
VF 154,154,13,13
s_waitcnt lgkmcnt(4)
Mvv 36,212,224
s_mov_b32 s15, 1183104
buffer_load_dwordx4 v1, s[20:23], s15 offen offset:1024 lds
Mvv 52,212,228
TR 200,2,34816
Mvv 68,212,232
TR 202,2,35328
Mvv 84,212,236
s_mov_b32 s15, 1476992
buffer_load_dwordx4 v1, s[16:19], s15 offen offset:2048 lds
s_waitcnt lgkmcnt(4)
Mvv 40,192,224
v_rcp_f32 v152, v152
Mvv 56,192,228
VM 150,150,146
VF 155,155,13,13
Mvv 72,192,232
s_mov_b32 s15, 1475968
buffer_load_dwordx4 v1, s[20:23], s15 offen offset:3072 lds
Mvv 88,192,236
TR 204,2,35840
s_waitcnt lgkmcnt(3)
Mvv 44,196,224
TR 206,2,36352
Mvv 60,196,228
v_rcp_f32 v153, v153
Mvv 76,196,232
VM 151,151,147
TR 208,3,34816
Mvv 92,196,236
v_rcp_f32 v154, v154
s_waitcnt lgkmcnt(3)
Mvv 32,200,240
TR 210,3,35328
VM 156,156,152
Mvv 48,200,244
v_rcp_f32 v155, v155
Mvv 64,200,248
TR 212,3,35840
VM 157,157,153
Mvv 80,200,252
TR 214,3,36352
VM 158,158,154
s_waitcnt lgkmcnt(4)
Mvv 36,204,240
VM 159,159,155
VC 144,148,149
Mvv 52,204,244
TR 192,2,49152
VC 145,150,151
Mvv 68,204,248
TR 194,2,49664
VC 146,156,157
Mvv 84,204,252
VC 147,158,159
TR 196,2,50176
s_waitcnt lgkmcnt(5)
Mvv 40,208,240
TR 198,2,50688
Mvv 56,208,244
ds_write_b128 v5, v[144:147] offset:19456
Mvv 72,208,248
TR 200,3,49152
Mvv 88,208,252
TR 202,3,49664
s_waitcnt lgkmcnt(7)
Mvv 44,212,240
Mvv 60,212,244
Mvv 76,212,248
TR 204,3,50176
Mvv 92,212,252
TR 206,3,50688
s_waitcnt lgkmcnt(4)
s_waitcnt vmcnt(4)
s_barrier
Zva 96,192,0
s_mov_b32 m0, s70
Zva 112,192,4
s_mov_b32 s15, 1773952
buffer_load_dwordx4 v1, s[16:19], s15 offen offset:0 lds
Zva 128,192,8
VM 36,32,36
VM 37,33,37
Zva 144,192,12
VM 38,34,38
VM 32,6,32
Zva 100,196,0
s_mov_b32 s15, 1772928
buffer_load_dwordx4 v1, s[20:23], s15 offen offset:1024 lds
Zva 116,196,4
VM 39,35,39
VM 33,6,33
Zva 132,196,8
TR 208,2,51200
VM 44,40,44
Zva 148,196,12
s_mov_b32 s15, 2066816
buffer_load_dwordx4 v1, s[16:19], s15 offen offset:2048 lds
s_waitcnt lgkmcnt(3)
Zva 104,200,0
TR 210,2,51712
VM 34,6,34
Zva 120,200,4
v_exp_f32 v32, v32
Zva 136,200,8
s_mov_b32 s15, 2065792
buffer_load_dwordx4 v1, s[20:23], s15 offen offset:3072 lds
Zva 152,200,12
TR 212,2,52224
VM 45,41,45
s_waitcnt lgkmcnt(3)
Zva 108,204,0
TR 214,2,52736
VM 35,6,35
Zva 124,204,4
v_exp_f32 v33, v33
Zva 140,204,8
TR 192,3,51200
VM 46,42,46
Zva 156,204,12
TR 194,3,51712
VM 40,6,40
s_waitcnt lgkmcnt(4)
Mva 96,208,16
v_exp_f32 v34, v34
Mva 112,208,20
TR 196,3,52224
VF 32,32,10,10
Mva 128,208,24
TR 198,3,52736
VM 47,43,47
Mva 144,208,28
VM 41,6,41
TR 200,2,0
s_waitcnt lgkmcnt(5)
Mva 100,212,16
v_exp_f32 v35, v35
Mva 116,212,20
TR 202,2,512
VF 33,33,10,10
Mva 132,212,24
VM 42,6,42
TR 204,2,1024
Mva 148,212,28
v_exp_f32 v40, v40
s_waitcnt lgkmcnt(5)
Mva 104,192,16
TR 206,2,1536
)"
R"(VF 34,34,10,10
Mva 120,192,20
v_rcp_f32 v32, v32
Mva 136,192,24
TR 208,3,0
VM 43,6,43
Mva 152,192,28
TR 210,3,512
s_waitcnt lgkmcnt(6)
Mva 108,196,16
v_exp_f32 v41, v41
Mva 124,196,20
VF 35,35,10,10
Mva 140,196,24
TR 212,3,1024
Mva 156,196,28
TR 214,3,1536
s_waitcnt vmcnt(4)
s_barrier
s_waitcnt lgkmcnt(6)
Mva 96,200,32
s_mov_b32 m0, s71
Mva 112,200,36
s_mov_b32 s15, 2363776
buffer_load_dwordx4 v1, s[16:19], s15 offen offset:0 lds
Mva 128,200,40
v_rcp_f32 v33, v33
Mva 144,200,44
v_exp_f32 v42, v42
s_waitcnt lgkmcnt(4)
Mva 100,204,32
s_mov_b32 s15, 2362752
buffer_load_dwordx4 v1, s[20:23], s15 offen offset:1024 lds
Mva 116,204,36
TR 192,2,2048
VF 40,40,10,10
Mva 132,204,40
TR 194,2,2560
Mva 148,204,44
s_mov_b32 s15, 2656640
buffer_load_dwordx4 v1, s[16:19], s15 offen offset:2048 lds
s_waitcnt lgkmcnt(4)
Mva 104,208,32
v_rcp_f32 v34, v34
Mva 120,208,36
VM 36,36,32
TR 196,2,3072
Mva 136,208,40
s_mov_b32 s15, 2655616
buffer_load_dwordx4 v1, s[20:23], s15 offen offset:3072 lds
Mva 152,208,44
TR 198,2,3584
s_waitcnt lgkmcnt(4)
Mva 108,212,32
v_exp_f32 v43, v43
Mva 124,212,36
VF 41,41,10,10
TR 200,3,2048
Mva 140,212,40
v_rcp_f32 v35, v35
Mva 156,212,44
TR 202,3,2560
VM 37,37,33
s_waitcnt lgkmcnt(4)
Mva 96,192,48
VF 42,42,10,10
TR 204,3,3072
Mva 112,192,52
v_rcp_f32 v40, v40
Mva 128,192,56
TR 206,3,3584
VM 38,38,34
Mva 144,192,60
VF 43,43,10,10
TR 208,2,16384
s_waitcnt lgkmcnt(5)
Mva 100,196,48
v_rcp_f32 v41, v41
Mva 116,196,52
TR 210,2,16896
VM 39,39,35
Mva 132,196,56
v_rcp_f32 v42, v42
Mva 148,196,60
TR 212,2,17408
VM 44,44,40
s_waitcnt lgkmcnt(5)
Mva 104,200,48
TR 214,2,17920
Mva 120,200,52
v_rcp_f32 v43, v43
Mva 136,200,56
VM 45,45,41
TR 192,3,16384
Mva 152,200,60
VM 46,46,42
TR 194,3,16896
s_waitcnt lgkmcnt(6)
Mva 108,204,48
VM 47,47,43
VC 32,36,37
Mva 124,204,52
VC 33,38,39
VC 34,44,45
Mva 140,204,56
TR 196,3,17408
VC 35,46,47
Mva 156,204,60
TR 198,3,17920
s_waitcnt vmcnt(4)
s_barrier
s_waitcnt lgkmcnt(6)
Mva 96,208,64
s_mov_b32 m0, s68
Mva 112,208,68
s_mov_b32 s15, 2953600
buffer_load_dwordx4 v1, s[16:19], s15 offen offset:0 lds
Mva 128,208,72
TR 200,2,18432
Mva 144,208,76
ds_write_b128 v5, v[32:35] offset:32768
s_waitcnt lgkmcnt(6)
Mva 100,212,64
s_mov_b32 s15, 2952576
buffer_load_dwordx4 v1, s[20:23], s15 offen offset:1024 lds
Mva 116,212,68
TR 202,2,18944
VM 52,48,52
Mva 132,212,72
VM 53,49,53
VM 54,50,54
Mva 148,212,76
s_mov_b32 s15, 3246464
buffer_load_dwordx4 v1, s[16:19], s15 offen offset:2048 lds
s_waitcnt lgkmcnt(5)
Mva 104,192,64
TR 204,2,19456
VM 48,7,48
Mva 120,192,68
TR 206,2,19968
VM 55,51,55
Mva 136,192,72
s_mov_b32 s15, 3245440
buffer_load_dwordx4 v1, s[20:23], s15 offen offset:3072 lds
Mva 152,192,76
VM 49,7,49
VM 60,56,60
s_waitcnt lgkmcnt(5)
Mva 108,196,64
TR 208,3,18432
VM 50,7,50
Mva 124,196,68
TR 210,3,18944
Mva 140,196,72
v_exp_f32 v48, v48
Mva 156,196,76
VM 61,57,61
TR 212,3,19456
s_waitcnt lgkmcnt(5)
Mva 96,200,80
VM 51,7,51
TR 214,3,19968
Mva 112,200,84
v_exp_f32 v49, v49
Mva 128,200,88
VM 62,58,62
TR 192,2,32768
Mva 144,200,92
VM 56,7,56
TR 194,2,33280
s_waitcnt lgkmcnt(6)
Mva 100,204,80
v_exp_f32 v50, v50
Mva 116,204,84
VF 48,48,11,11
VM 63,59,63
Mva 132,204,88
TR 196,2,33792
VM 57,7,57
Mva 148,204,92
TR 198,2,34304
s_waitcnt lgkmcnt(6)
Mva 104,208,80
v_exp_f32 v51, v51
Mva 120,208,84
VF 49,49,11,11
VM 58,7,58
Mva 136,208,88
TR 200,3,32768
Mva 152,208,92
TR 202,3,33280
s_waitcnt lgkmcnt(6)
Mva 108,212,80
v_exp_f32 v56, v56
Mva 124,212,84
VF 50,50,11,11
Mva 140,212,88
TR 204,3,33792
Mva 156,212,92
TR 206,3,34304
s_waitcnt vmcnt(4)
s_barrier
s_waitcnt lgkmcnt(6)
Mva 96,192,96
s_mov_b32 m0, s69
Mva 112,192,100
s_mov_b32 s15, 3543424
buffer_load_dwordx4 v1, s[16:19], s15 offen offset:0 lds
Mva 128,192,104
v_rcp_f32 v48, v48
Mva 144,192,108
VM 59,7,59
TR 208,2,34816
s_waitcnt lgkmcnt(5)
Mva 100,196,96
s_mov_b32 s15, 3542400
buffer_load_dwordx4 v1, s[20:23], s15 offen offset:1024 lds
Mva 116,196,100
TR 210,2,35328
Mva 132,196,104
v_exp_f32 v57, v57
Mva 148,196,108
s_mov_b32 s15, 3836288
buffer_load_dwordx4 v1, s[16:19], s15 offen offset:2048 lds
s_waitcnt lgkmcnt(4)
Mva 104,200,96
VF 51,51,11,11
TR 212,2,35840
Mva 120,200,100
v_rcp_f32 v49, v49
Mva 136,200,104
s_mov_b32 s15, 3835264
buffer_load_dwordx4 v1, s[20:23], s15 offen offset:3072 lds
Mva 152,200,108
TR 214,2,36352
s_waitcnt lgkmcnt(4)
Mva 108,204,96
v_exp_f32 v58, v58
Mva 124,204,100
VF 56,56,11,11
TR 192,3,34816
Mva 140,204,104
TR 194,3,35328
Mva 156,204,108
v_rcp_f32 v50, v50
s_waitcnt lgkmcnt(4)
Mva 96,208,112
VM 52,52,48
TR 196,3,35840
Mva 112,208,116
v_exp_f32 v59, v59
Mva 128,208,120
TR 198,3,36352
VF 57,57,11,11
Mva 144,208,124
v_rcp_f32 v51, v51
s_waitcnt lgkmcnt(4)
Mva 100,212,112
TR 200,2,49152
VM 53,53,49
Mva 116,212,116
TR 202,2,49664
VF 58,58,11,11
Mva 132,212,120
v_rcp_f32 v56, v56
Mva 148,212,124
TR 204,2,50176
VM 54,54,50
s_waitcnt lgkmcnt(5)
Mva 104,192,112
TR 206,2,50688
VF 59,59,11,11
Mva 120,192,116
v_rcp_f32 v57, v57
Mva 136,192,120
TR 208,3,49152
VM 55,55,51
Mva 152,192,124
TR 210,3,49664
s_waitcnt lgkmcnt(6)
Mva 108,196,112
v_rcp_f32 v58, v58
Mva 124,196,116
VM 60,60,56
Mva 140,196,120
TR 212,3,50176
Mva 156,196,124
TR 214,3,50688
s_waitcnt vmcnt(4)
s_barrier
s_waitcnt lgkmcnt(6)
Mva 96,200,128
s_mov_b32 m0, s70
Mva 112,200,132
s_mov_b32 s15, 4133248
buffer_load_dwordx4 v1, s[16:19], s15 offen offset:0 lds
Mva 128,200,136
v_rcp_f32 v59, v59
Mva 144,200,140
VM 61,61,57
VM 62,62,58
s_waitcnt lgkmcnt(4)
Mva 100,204,128
s_mov_b32 s15, 4132224
buffer_load_dwordx4 v1, s[20:23], s15 offen offset:1024 lds
Mva 116,204,132
TR 192,2,51200
VM 63,63,59
Mva 132,204,136
TR 194,2,51712
VC 48,52,53
Mva 148,204,140
s_mov_b32 s15, 4426112
buffer_load_dwordx4 v1, s[16:19], s15 offen offset:2048 lds
s_waitcnt lgkmcnt(4)
Mva 104,208,128
VC 49,54,55
VC 50,60,61
Mva 120,208,132
TR 196,2,52224
VC 51,62,63
Mva 136,208,136
s_mov_b32 s15, 4425088
buffer_load_dwordx4 v1, s[20:23], s15 offen offset:3072 lds
Mva 152,208,140
TR 198,2,52736
s_waitcnt lgkmcnt(4)
Mva 108,212,128
ds_write_b128 v5, v[48:51] offset:33792
Mva 124,212,132
TR 200,3,51200
VM 68,64,68
Mva 140,212,136
TR 202,3,51712
VM 69,65,69
Mva 156,212,140
VM 70,66,70
VM 64,8,64
s_waitcnt lgkmcnt(5)
Mva 96,192,144
TR 204,3,52224
VM 71,67,71
Mva 112,192,148
TR 206,3,52736
VM 65,8,65
Mva 128,192,152
VM 76,72,76
VM 66,8,66
Mva 144,192,156
TR 208,2,0
s_waitcnt lgkmcnt(6)
Mva 100,196,144
TR 210,2,512
Mva 116,196,148
v_exp_f32 v64, v64
Mva 132,196,152
VM 77,73,77
VM 67,8,67
Mva 148,196,156
TR 212,2,1024
s_waitcnt lgkmcnt(5)
Mva 104,200,144
TR 214,2,1536
Mva 120,200,148
v_exp_f32 v65, v65
Mva 136,200,152
VM 78,74,78
VM 72,8,72
Mva 152,200,156
TR 192,3,0
s_waitcnt lgkmcnt(5)
Mva 108,204,144
TR 194,3,512
Mva 124,204,148
v_exp_f32 v66, v66
Mva 140,204,152
VF 64,64,12,12
VM 79,75,79
Mva 156,204,156
TR 196,3,1024
VM 73,8,73
s_waitcnt vmcnt(4)
s_barrier
s_waitcnt lgkmcnt(5)
Mva 96,208,160
s_mov_b32 m0, s71
TR 198,3,1536
Mva 112,208,164
s_mov_b32 s15, 4723072
buffer_load_dwordx4 v1, s[16:19], s15 offen offset:0 lds
Mva 128,208,168
v_exp_f32 v67, v67
Mva 144,208,172
VF 65,65,12,12
TR 200,2,2048
s_waitcnt lgkmcnt(5)
Mva 100,212,160
s_mov_b32 s15, 4722048
buffer_load_dwordx4 v1, s[20:23], s15 offen offset:1024 lds
Mva 116,212,164
VM 74,8,74
TR 202,2,2560
Mva 132,212,168
v_exp_f32 v72, v72
Mva 148,212,172
s_mov_b32 s15, 5015936
buffer_load_dwordx4 v1, s[16:19], s15 offen offset:2048 lds
s_waitcnt lgkmcnt(4)
Mva 104,192,160
VF 66,66,12,12
TR 204,2,3072
Mva 120,192,164
TR 206,2,3584
Mva 136,192,168
s_mov_b32 s15, 5014912
buffer_load_dwordx4 v1, s[20:23], s15 offen offset:3072 lds
Mva 152,192,172
v_rcp_f32 v64, v64
s_waitcnt lgkmcnt(4)
Mva 108,196,160
VM 75,8,75
TR 208,3,2048
Mva 124,196,164
v_exp_f32 v73, v73
Mva 140,196,168
TR 210,3,2560
VF 67,67,12,12
Mva 156,196,172
v_rcp_f32 v65, v65
s_waitcnt lgkmcnt(4)
Mva 96,200,176
TR 212,3,3072
Mva 112,200,180
TR 214,3,3584
Mva 128,200,184
v_exp_f32 v74, v74
Mva 144,200,188
VF 72,72,12,12
TR 192,2,16384
s_waitcnt lgkmcnt(5)
Mva 100,204,176
v_rcp_f32 v66, v66
Mva 116,204,180
TR 194,2,16896
VM 68,68,64
Mva 132,204,184
v_exp_f32 v75, v75
Mva 148,204,188
TR 196,2,17408
VF 73,73,12,12
s_waitcnt lgkmcnt(5)
Mva 104,208,176
TR 198,2,17920
Mva 120,208,180
v_rcp_f32 v67, v67
Mva 136,208,184
VM 69,69,65
TR 200,3,16384
Mva 152,208,188
VF 74,74,12,12
TR 202,3,16896
s_waitcnt lgkmcnt(6)
Mva 108,212,176
v_rcp_f32 v72, v72
Mva 124,212,180
VM 70,70,66
VF 75,75,12,12
Mva 140,212,184
TR 204,3,17408
Mva 156,212,188
TR 206,3,17920
s_waitcnt vmcnt(4)
s_barrier
s_waitcnt lgkmcnt(6)
Mva 96,192,192
s_mov_b32 m0, s68
Mva 112,192,196
s_mov_b32 s15, 4608
buffer_load_dwordx4 v1, s[16:19], s15 offen offset:0 lds
Mva 128,192,200
v_rcp_f32 v73, v73
Mva 144,192,204
VM 71,71,67
TR 208,2,18432
s_waitcnt lgkmcnt(5)
Mva 100,196,192
s_mov_b32 s15, 3584
buffer_load_dwordx4 v1, s[20:23], s15 offen offset:1024 lds
Mva 116,196,196
TR 210,2,18944
Mva 132,196,200
v_rcp_f32 v74, v74
Mva 148,196,204
s_mov_b32 s15, 297472
buffer_load_dwordx4 v1, s[16:19], s15 offen offset:2048 lds
s_waitcnt lgkmcnt(4)
Mva 104,200,192
VM 76,76,72
TR 212,2,19456
Mva 120,200,196
v_rcp_f32 v75, v75
Mva 136,200,200
s_mov_b32 s15, 296448
buffer_load_dwordx4 v1, s[20:23], s15 offen offset:3072 lds
Mva 152,200,204
TR 214,2,19968
VM 77,77,73
s_waitcnt lgkmcnt(4)
Mva 108,204,192
VM 78,78,74
VM 79,79,75
Mva 124,204,196
TR 192,3,18432
VC 64,68,69
Mva 140,204,200
TR 194,3,18944
VC 65,70,71
Mva 156,204,204
VC 66,76,77
VC 67,78,79
s_waitcnt lgkmcnt(4)
Mva 96,208,208
TR 196,3,19456
Mva 112,208,212
TR 198,3,19968
Mva 128,208,216
ds_write_b128 v5, v[64:67] offset:34816
Mva 144,208,220
VM 84,80,84
TR 200,2,32768
s_waitcnt lgkmcnt(6)
Mva 100,212,208
VM 85,81,85
TR 202,2,33280
Mva 116,212,212
VM 86,82,86
VM 80,9,80
Mva 132,212,216
VM 87,83,87
TR 204,2,33792
Mva 148,212,220
VM 81,9,81
TR 206,2,34304
s_waitcnt lgkmcnt(7)
Mva 104,192,208
VM 92,88,92
VM 82,9,82
Mva 120,192,212
v_exp_f32 v80, v80
Mva 136,192,216
TR 208,3,32768
VM 93,89,93
Mva 152,192,220
TR 210,3,33280
VM 83,9,83
s_waitcnt lgkmcnt(7)
Mva 108,196,208
v_exp_f32 v81, v81
Mva 124,196,212
VM 94,90,94
VM 88,9,88
Mva 140,196,216
TR 212,3,33792
Mva 156,196,220
TR 214,3,34304
s_waitcnt vmcnt(4)
s_barrier
s_waitcnt lgkmcnt(6)
Mva 96,200,224
s_mov_b32 m0, s69
Mva 112,200,228
s_mov_b32 s15, 594432
buffer_load_dwordx4 v1, s[16:19], s15 offen offset:0 lds
Mva 128,200,232
TR 192,2,34816
Mva 144,200,236
v_exp_f32 v82, v82
s_waitcnt lgkmcnt(5)
Mva 100,204,224
s_mov_b32 s15, 593408
buffer_load_dwordx4 v1, s[20:23], s15 offen offset:1024 lds
Mva 116,204,228
TR 194,2,35328
VF 80,80,13,13
Mva 132,204,232
VM 95,91,95
VM 89,9,89
Mva 148,204,236
s_mov_b32 s15, 887296
buffer_load_dwordx4 v1, s[16:19], s15 offen offset:2048 lds
s_waitcnt lgkmcnt(4)
Mva 104,208,224
TR 196,2,35840
Mva 120,208,228
TR 198,2,36352
Mva 136,208,232
s_mov_b32 s15, 886272
buffer_load_dwordx4 v1, s[20:23], s15 offen offset:3072 lds
Mva 152,208,236
v_exp_f32 v83, v83
)"
R"(s_waitcnt lgkmcnt(4)
Mva 108,212,224
VF 81,81,13,13
VM 90,9,90
Mva 124,212,228
TR 200,3,34816
Mva 140,212,232
TR 202,3,35328
Mva 156,212,236
v_exp_f32 v88, v88
s_waitcnt lgkmcnt(4)
Mva 96,192,240
VF 82,82,13,13
TR 204,3,35840
Mva 112,192,244
v_rcp_f32 v80, v80
Mva 128,192,248
TR 206,3,36352
VM 91,9,91
Mva 144,192,252
v_exp_f32 v89, v89
s_waitcnt lgkmcnt(4)
Mva 100,196,240
TR 208,2,49152
VF 83,83,13,13
Mva 116,196,244
TR 210,2,49664
Mva 132,196,248
v_rcp_f32 v81, v81
Mva 148,196,252
TR 212,2,50176
s_waitcnt lgkmcnt(5)
Mva 104,200,240
v_exp_f32 v90, v90
Mva 120,200,244
TR 214,2,50688
VF 88,88,13,13
Mva 136,200,248
v_rcp_f32 v82, v82
Mva 152,200,252
TR 192,3,49152
VM 84,84,80
s_waitcnt lgkmcnt(5)
Mva 108,204,240
TR 194,3,49664
Mva 124,204,244
v_exp_f32 v91, v91
Mva 140,204,248
VF 89,89,13,13
TR 196,3,50176
Mva 156,204,252
TR 198,3,50688
s_waitcnt vmcnt(4)
s_barrier
s_waitcnt lgkmcnt(6)
Mvv 96,208,224
s_mov_b32 m0, s70
Mvv 112,208,228
s_mov_b32 s15, 1184256
buffer_load_dwordx4 v1, s[16:19], s15 offen offset:0 lds
Mvv 128,208,232
v_rcp_f32 v83, v83
Mvv 144,208,236
VM 85,85,81
VF 90,90,13,13
s_waitcnt lgkmcnt(4)
Mvv 100,212,224
s_mov_b32 s15, 1183232
buffer_load_dwordx4 v1, s[20:23], s15 offen offset:1024 lds
Mvv 116,212,228
TR 200,2,51200
Mvv 132,212,232
TR 202,2,51712
Mvv 148,212,236
s_mov_b32 s15, 1477120
buffer_load_dwordx4 v1, s[16:19], s15 offen offset:2048 lds
s_waitcnt lgkmcnt(4)
Mvv 104,192,224
v_rcp_f32 v88, v88
Mvv 120,192,228
VM 86,86,82
VF 91,91,13,13
Mvv 136,192,232
s_mov_b32 s15, 1476096
buffer_load_dwordx4 v1, s[20:23], s15 offen offset:3072 lds
Mvv 152,192,236
TR 204,2,52224
s_waitcnt lgkmcnt(3)
Mvv 108,196,224
TR 206,2,52736
Mvv 124,196,228
v_rcp_f32 v89, v89
Mvv 140,196,232
VM 87,87,83
TR 208,3,51200
Mvv 156,196,236
v_rcp_f32 v90, v90
s_waitcnt lgkmcnt(3)
Mvv 96,200,240
TR 210,3,51712
VM 92,92,88
Mvv 112,200,244
v_rcp_f32 v91, v91
Mvv 128,200,248
TR 212,3,52224
VM 93,93,89
Mvv 144,200,252
TR 214,3,52736
VM 94,94,90
s_waitcnt lgkmcnt(4)
Mvv 100,204,240
VM 95,95,91
VC 80,84,85
Mvv 116,204,244
TR 192,2,0
VC 81,86,87
Mvv 132,204,248
TR 194,2,512
VC 82,92,93
Mvv 148,204,252
VC 83,94,95
TR 196,2,1024
s_waitcnt lgkmcnt(5)
Mvv 104,208,240
TR 198,2,1536
Mvv 120,208,244
ds_write_b128 v5, v[80:83] offset:35840
Mvv 136,208,248
TR 200,3,0
Mvv 152,208,252
TR 202,3,512
s_waitcnt lgkmcnt(7)
Mvv 108,212,240
Mvv 124,212,244
Mvv 140,212,248
TR 204,3,1024
Mvv 156,212,252
TR 206,3,1536
s_waitcnt lgkmcnt(4)
s_waitcnt vmcnt(4)
s_barrier
Zva 32,192,0
s_mov_b32 m0, s71
Zva 48,192,4
s_mov_b32 s15, 1774080
buffer_load_dwordx4 v1, s[16:19], s15 offen offset:0 lds
Zva 64,192,8
VM 100,96,100
VM 101,97,101
Zva 80,192,12
VM 102,98,102
VM 96,6,96
Zva 36,196,0
s_mov_b32 s15, 1773056
buffer_load_dwordx4 v1, s[20:23], s15 offen offset:1024 lds
Zva 52,196,4
VM 103,99,103
VM 97,6,97
Zva 68,196,8
TR 208,2,2048
VM 108,104,108
Zva 84,196,12
s_mov_b32 s15, 2066944
buffer_load_dwordx4 v1, s[16:19], s15 offen offset:2048 lds
s_waitcnt lgkmcnt(3)
Zva 40,200,0
TR 210,2,2560
VM 98,6,98
Zva 56,200,4
v_exp_f32 v96, v96
Zva 72,200,8
s_mov_b32 s15, 2065920
buffer_load_dwordx4 v1, s[20:23], s15 offen offset:3072 lds
Zva 88,200,12
TR 212,2,3072
VM 109,105,109
s_waitcnt lgkmcnt(3)
Zva 44,204,0
TR 214,2,3584
VM 99,6,99
Zva 60,204,4
v_exp_f32 v97, v97
Zva 76,204,8
TR 192,3,2048
VM 110,106,110
Zva 92,204,12
TR 194,3,2560
VM 104,6,104
s_waitcnt lgkmcnt(4)
Mva 32,208,16
v_exp_f32 v98, v98
Mva 48,208,20
TR 196,3,3072
VF 96,96,10,10
Mva 64,208,24
TR 198,3,3584
VM 111,107,111
Mva 80,208,28
VM 105,6,105
TR 200,2,16384
s_waitcnt lgkmcnt(5)
Mva 36,212,16
v_exp_f32 v99, v99
Mva 52,212,20
TR 202,2,16896
VF 97,97,10,10
Mva 68,212,24
VM 106,6,106
TR 204,2,17408
Mva 84,212,28
v_exp_f32 v104, v104
s_waitcnt lgkmcnt(5)
Mva 40,192,16
TR 206,2,17920
VF 98,98,10,10
Mva 56,192,20
v_rcp_f32 v96, v96
Mva 72,192,24
TR 208,3,16384
VM 107,6,107
Mva 88,192,28
TR 210,3,16896
s_waitcnt lgkmcnt(6)
Mva 44,196,16
v_exp_f32 v105, v105
Mva 60,196,20
VF 99,99,10,10
ds_read_b128 a[0:3], v4 offset:0
Mva 76,196,24
TR 212,3,17408
Mva 92,196,28
TR 214,3,17920
s_waitcnt vmcnt(4)
s_barrier
s_waitcnt lgkmcnt(7)
Mva 32,200,32
s_mov_b32 m0, s68
ds_read_b128 a[4:7], v4 offset:1024
Mva 48,200,36
s_mov_b32 s15, 2363904
buffer_load_dwordx4 v1, s[16:19], s15 offen offset:0 lds
Mva 64,200,40
v_rcp_f32 v97, v97
Mva 80,200,44
ds_read_b128 a[8:11], v4 offset:2048
s_waitcnt lgkmcnt(7)
Mva 36,204,32
s_mov_b32 s15, 2362880
buffer_load_dwordx4 v1, s[20:23], s15 offen offset:1024 lds
Mva 52,204,36
ds_read_b128 a[12:15], v4 offset:3072
Mva 68,204,40
v_exp_f32 v106, v106
Mva 84,204,44
s_mov_b32 s15, 2656768
buffer_load_dwordx4 v1, s[16:19], s15 offen offset:2048 lds
s_waitcnt lgkmcnt(6)
Mva 40,208,32
TR 192,2,18432
VF 104,104,10,10
Mva 56,208,36
TR 194,2,18944
Mva 72,208,40
s_mov_b32 s15, 2655744
buffer_load_dwordx4 v1, s[20:23], s15 offen offset:3072 lds
Mva 88,208,44
v_rcp_f32 v98, v98
s_waitcnt lgkmcnt(5)
Mva 44,212,32
VM 100,100,96
TR 196,2,19456
Mva 60,212,36
TR 198,2,19968
Mva 76,212,40
v_exp_f32 v107, v107
Mva 92,212,44
VF 105,105,10,10
TR 200,3,18432
s_waitcnt lgkmcnt(3)
Mva 32,192,48
v_rcp_f32 v99, v99
Mva 48,192,52
TR 202,3,18944
VM 101,101,97
Mva 64,192,56
VF 106,106,10,10
TR 204,3,19456
Mva 80,192,60
v_rcp_f32 v104, v104
s_waitcnt lgkmcnt(3)
Mva 36,196,48
TR 206,3,19968
VM 102,102,98
Mva 52,196,52
VF 107,107,10,10
ds_read_b128 a[16:19], v4 offset:4096
Mva 68,196,56
ds_read_b128 a[20:23], v4 offset:5120
ds_read_b128 a[24:27], v4 offset:6144
Mva 84,196,60
ds_read_b128 a[28:31], v4 offset:7168
TR 208,2,32768
s_waitcnt lgkmcnt(7)
Mva 40,200,48
v_rcp_f32 v105, v105
Mva 56,200,52
TR 210,2,33280
VM 103,103,99
Mva 72,200,56
v_rcp_f32 v106, v106
Mva 88,200,60
TR 212,2,33792
VM 108,108,104
s_waitcnt lgkmcnt(7)
Mva 44,204,48
TR 214,2,34304
Mva 60,204,52
v_rcp_f32 v107, v107
Mva 76,204,56
VM 109,109,105
TR 192,3,32768
Mva 92,204,60
VM 110,110,106
TR 194,3,33280
s_waitcnt vmcnt(4)
s_barrier
s_waitcnt lgkmcnt(4)
Mva 32,208,64
s_mov_b32 m0, s69
VM 111,111,107
Mva 48,208,68
s_mov_b32 s15, 2953728
buffer_load_dwordx4 v1, s[16:19], s15 offen offset:0 lds
Mva 64,208,72
VC 96,100,101
VC 97,102,103
Mva 80,208,76
TR 196,3,33792
VC 98,108,109
s_waitcnt lgkmcnt(3)
Mva 36,212,64
s_mov_b32 s15, 2952704
buffer_load_dwordx4 v1, s[20:23], s15 offen offset:1024 lds
Mva 52,212,68
TR 198,3,34304
VC 99,110,111
Mva 68,212,72
ds_read_b128 a[32:35], v4 offset:16384
ds_read_b128 a[36:39], v4 offset:17408
Mva 84,212,76
ds_read_b128 a[40:43], v4 offset:18432
ds_read_b128 a[44:47], v4 offset:19456
s_waitcnt lgkmcnt(6)
Mva 40,192,64
s_mov_b32 s15, 3246592
buffer_load_dwordx4 v1, s[16:19], s15 offen offset:2048 lds
Mva 56,192,68
TR 200,2,34816
ds_write_b128 v5, v[96:99] offset:49152
Mva 72,192,72
TR 202,2,35328
VM 116,112,116
Mva 88,192,76
s_mov_b32 s15, 3245568
buffer_load_dwordx4 v1, s[20:23], s15 offen offset:3072 lds
VM 117,113,117
s_waitcnt lgkmcnt(7)
Mva 44,196,64
VM 118,114,118
TR 204,2,35840
Mva 60,196,68
VM 112,7,112
TR 206,2,36352
Mva 76,196,72
VM 119,115,119
VM 113,7,113
Mva 92,196,76
VM 124,120,124
TR 208,3,34816
s_waitcnt lgkmcnt(3)
Mva 32,200,80
VM 114,7,114
TR 210,3,35328
Mva 48,200,84
v_exp_f32 v112, v112
Mva 64,200,88
VM 125,121,125
TR 212,3,35840
Mva 80,200,92
VM 115,7,115
TR 214,3,36352
s_waitcnt lgkmcnt(4)
Mva 36,204,80
v_exp_f32 v113, v113
Mva 52,204,84
ds_read_b128 a[48:51], v4 offset:20480
ds_read_b128 a[52:55], v4 offset:21504
Mva 68,204,88
ds_read_b128 a[56:59], v4 offset:22528
ds_read_b128 a[60:63], v4 offset:23552
Mva 84,204,92
VM 126,122,126
TR 192,2,49152
s_waitcnt lgkmcnt(7)
Mva 40,208,80
VM 120,7,120
TR 194,2,49664
Mva 56,208,84
v_exp_f32 v114, v114
Mva 72,208,88
VF 112,112,11,11
TR 196,2,50176
Mva 88,208,92
VM 127,123,127
TR 198,2,50688
s_waitcnt lgkmcnt(8)
Mva 44,212,80
VM 121,7,121
TR 200,3,49152
Mva 60,212,84
v_exp_f32 v115, v115
Mva 76,212,88
VF 113,113,11,11
TR 202,3,49664
Mva 92,212,92
VM 122,7,122
TR 204,3,50176
s_waitcnt vmcnt(4)
s_barrier
s_waitcnt lgkmcnt(5)
Mva 32,192,96
s_mov_b32 m0, s70
TR 206,3,50688
Mva 48,192,100
s_mov_b32 s15, 3543552
buffer_load_dwordx4 v1, s[16:19], s15 offen offset:0 lds
Mva 64,192,104
v_exp_f32 v120, v120
Mva 80,192,108
VF 114,114,11,11
ds_read_b128 a[64:67], v4 offset:32768
s_waitcnt lgkmcnt(5)
Mva 36,196,96
s_mov_b32 s15, 3542528
buffer_load_dwordx4 v1, s[20:23], s15 offen offset:1024 lds
Mva 52,196,100
v_rcp_f32 v112, v112
Mva 68,196,104
ds_read_b128 a[68:71], v4 offset:33792
ds_read_b128 a[72:75], v4 offset:34816
Mva 84,196,108
ds_read_b128 a[76:79], v4 offset:35840
VM 123,7,123
s_waitcnt lgkmcnt(6)
Mva 40,200,96
s_mov_b32 s15, 3836416
buffer_load_dwordx4 v1, s[16:19], s15 offen offset:2048 lds
Mva 56,200,100
TR 208,2,51200
TR 210,2,51712
Mva 72,200,104
v_exp_f32 v121, v121
Mva 88,200,108
s_mov_b32 s15, 3835392
buffer_load_dwordx4 v1, s[20:23], s15 offen offset:3072 lds
VF 115,115,11,11
s_waitcnt lgkmcnt(6)
Mva 44,204,96
TR 212,2,52224
TR 214,2,52736
Mva 60,204,100
v_rcp_f32 v113, v113
Mva 76,204,104
v_exp_f32 v122, v122
Mva 92,204,108
VF 120,120,11,11
TR 192,3,51200
s_waitcnt lgkmcnt(3)
Mva 32,208,112
TR 194,3,51712
Mva 48,208,116
v_rcp_f32 v114, v114
Mva 64,208,120
VM 116,116,112
TR 196,3,52224
Mva 80,208,124
v_exp_f32 v123, v123
s_waitcnt lgkmcnt(3)
Mva 36,212,112
TR 198,3,52736
VF 121,121,11,11
Mva 52,212,116
ds_read_b128 a[80:83], v4 offset:36864
ds_read_b128 a[84:87], v4 offset:37888
Mva 68,212,120
ds_read_b128 a[88:91], v4 offset:38912
ds_read_b128 a[92:95], v4 offset:39936
Mva 84,212,124
v_rcp_f32 v115, v115
s_waitcnt lgkmcnt(6)
Mva 40,192,112
TR 200,2,0
VM 117,117,113
Mva 56,192,116
TR 202,2,512
VF 122,122,11,11
Mva 72,192,120
v_rcp_f32 v120, v120
Mva 88,192,124
TR 204,2,1024
VM 118,118,114
s_waitcnt lgkmcnt(7)
Mva 44,196,112
TR 206,2,1536
VF 123,123,11,11
Mva 60,196,116
v_rcp_f32 v121, v121
Mva 76,196,120
TR 208,3,0
VM 119,119,115
Mva 92,196,124
TR 210,3,512
s_waitcnt vmcnt(4)
s_barrier
s_waitcnt lgkmcnt(4)
Mva 32,200,128
s_mov_b32 m0, s71
TR 212,3,1024
Mva 48,200,132
s_mov_b32 s15, 4133376
buffer_load_dwordx4 v1, s[16:19], s15 offen offset:0 lds
Mva 64,200,136
v_rcp_f32 v122, v122
Mva 80,200,140
VM 124,124,120
TR 214,3,1536
s_waitcnt lgkmcnt(4)
Mva 36,204,128
s_mov_b32 s15, 4132352
buffer_load_dwordx4 v1, s[20:23], s15 offen offset:1024 lds
Mva 52,204,132
v_rcp_f32 v123, v123
Mva 68,204,136
VM 125,125,121
VM 126,126,122
Mva 84,204,140
s_mov_b32 s15, 4426240
buffer_load_dwordx4 v1, s[16:19], s15 offen offset:2048 lds
s_waitcnt lgkmcnt(2)
Mva 40,208,128
TR 192,2,2048
VM 127,127,123
Mva 56,208,132
TR 194,2,2560
VC 112,116,117
Mva 72,208,136
s_mov_b32 s15, 4425216
buffer_load_dwordx4 v1, s[20:23], s15 offen offset:3072 lds
Mva 88,208,140
VC 113,118,119
VC 114,124,125
s_waitcnt lgkmcnt(2)
Mva 44,212,128
TR 196,2,3072
VC 115,126,127
Mva 60,212,132
TR 198,2,3584
Mva 76,212,136
ds_write_b128 v5, v[112:115] offset:50176
Mva 92,212,140
TR 200,3,2048
VM 132,128,132
s_waitcnt lgkmcnt(4)
Mva 32,192,144
TR 202,3,2560
VM 133,129,133
Mva 48,192,148
VM 134,130,134
VM 128,8,128
Mva 64,192,152
TR 204,3,3072
VM 135,131,135
Mva 80,192,156
TR 206,3,3584
VM 129,8,129
s_waitcnt lgkmcnt(5)
Mva 36,196,144
VM 140,136,140
VM 130,8,130
Mva 52,196,148
TR 208,2,16384
Mva 68,196,152
TR 210,2,16896
Mva 84,196,156
v_exp_f32 v128, v128
s_waitcnt lgkmcnt(4)
Mva 40,200,144
VM 141,137,141
VM 131,8,131
Mva 56,200,148
)"
R"(TR 212,2,17408
Mva 72,200,152
TR 214,2,17920
Mva 88,200,156
v_exp_f32 v129, v129
s_waitcnt lgkmcnt(4)
Mva 44,204,144
VM 142,138,142
VM 136,8,136
Mva 60,204,148
TR 192,3,16384
Mva 76,204,152
TR 194,3,16896
Mva 92,204,156
v_exp_f32 v130, v130
s_waitcnt vmcnt(4)
s_barrier
s_waitcnt lgkmcnt(4)
Mva 32,208,160
s_mov_b32 m0, s68
VF 128,128,12,12
Mva 48,208,164
s_mov_b32 s15, 4723200
buffer_load_dwordx4 v1, s[16:19], s15 offen offset:0 lds
Mva 64,208,168
VM 143,139,143
TR 196,3,17408
Mva 80,208,172
VM 137,8,137
TR 198,3,17920
s_waitcnt lgkmcnt(4)
Mva 36,212,160
s_mov_b32 s15, 4722176
buffer_load_dwordx4 v1, s[20:23], s15 offen offset:1024 lds
Mva 52,212,164
v_exp_f32 v131, v131
Mva 68,212,168
VF 129,129,12,12
TR 200,2,18432
Mva 84,212,172
s_mov_b32 s15, 5016064
buffer_load_dwordx4 v1, s[16:19], s15 offen offset:2048 lds
s_waitcnt lgkmcnt(3)
Mva 40,192,160
VM 138,8,138
TR 202,2,18944
Mva 56,192,164
v_exp_f32 v136, v136
Mva 72,192,168
s_mov_b32 s15, 5015040
buffer_load_dwordx4 v1, s[20:23], s15 offen offset:3072 lds
Mva 88,192,172
VF 130,130,12,12
TR 204,2,19456
s_waitcnt lgkmcnt(3)
Mva 44,196,160
TR 206,2,19968
Mva 60,196,164
v_rcp_f32 v128, v128
Mva 76,196,168
VM 139,8,139
TR 208,3,18432
Mva 92,196,172
v_exp_f32 v137, v137
s_waitcnt lgkmcnt(3)
Mva 32,200,176
TR 210,3,18944
VF 131,131,12,12
Mva 48,200,180
v_rcp_f32 v129, v129
Mva 64,200,184
TR 212,3,19456
Mva 80,200,188
TR 214,3,19968
s_waitcnt lgkmcnt(4)
Mva 36,204,176
v_exp_f32 v138, v138
Mva 52,204,180
VF 136,136,12,12
TR 192,2,32768
Mva 68,204,184
v_rcp_f32 v130, v130
Mva 84,204,188
TR 194,2,33280
VM 132,132,128
s_waitcnt lgkmcnt(4)
Mva 40,208,176
v_exp_f32 v139, v139
Mva 56,208,180
TR 196,2,33792
VF 137,137,12,12
Mva 72,208,184
TR 198,2,34304
Mva 88,208,188
v_rcp_f32 v131, v131
s_waitcnt lgkmcnt(4)
Mva 44,212,176
VM 133,133,129
TR 200,3,32768
Mva 60,212,180
VF 138,138,12,12
TR 202,3,33280
Mva 76,212,184
v_rcp_f32 v136, v136
Mva 92,212,188
VM 134,134,130
TR 204,3,33792
s_waitcnt vmcnt(4)
s_barrier
s_waitcnt lgkmcnt(5)
Mva 32,192,192
s_mov_b32 m0, s69
VF 139,139,12,12
Mva 48,192,196
s_mov_b32 s15, 4096
buffer_load_dwordx4 v18, s[24:27], s15 offen offset:0 lds
Mva 64,192,200
TR 206,3,34304
Mva 80,192,204
v_rcp_f32 v137, v137
s_waitcnt lgkmcnt(4)
Mva 36,196,192
s_mov_b32 s15, 39936
buffer_load_dwordx4 v18, s[24:27], s15 offen offset:1024 lds
Mva 52,196,196
VM 135,135,131
TR 208,2,34816
Mva 68,196,200
TR 210,2,35328
Mva 84,196,204
s_mov_b32 s15, 75776
buffer_load_dwordx4 v18, s[24:27], s15 offen offset:2048 lds
s_waitcnt lgkmcnt(4)
Mva 40,200,192
v_rcp_f32 v138, v138
Mva 56,200,196
VM 140,140,136
TR 212,2,35840
Mva 72,200,200
s_mov_b32 s15, 111616
buffer_load_dwordx4 v18, s[24:27], s15 offen offset:3072 lds
Mva 88,200,204
v_rcp_f32 v139, v139
s_waitcnt lgkmcnt(3)
Mva 44,204,192
TR 214,2,36352
VM 141,141,137
Mva 60,204,196
VM 142,142,138
VM 143,143,139
Mva 76,204,200
TR 192,3,34816
VC 128,132,133
Mva 92,204,204
TR 194,3,35328
VC 129,134,135
s_waitcnt lgkmcnt(4)
Mva 32,208,208
VC 130,140,141
VC 131,142,143
Mva 48,208,212
TR 196,3,35840
Mva 64,208,216
TR 198,3,36352
Mva 80,208,220
ds_write_b128 v5, v[128:131] offset:51200
s_waitcnt lgkmcnt(5)
Mva 36,212,208
VM 148,144,148
TR 200,2,49152
Mva 52,212,212
VM 149,145,149
TR 202,2,49664
Mva 68,212,216
VM 150,146,150
VM 144,9,144
Mva 84,212,220
VM 151,147,151
TR 204,2,50176
s_waitcnt lgkmcnt(6)
Mva 40,192,208
VM 145,9,145
TR 206,2,50688
Mva 56,192,212
VM 156,152,156
VM 146,9,146
Mva 72,192,216
TR 208,3,49152
Mva 88,192,220
v_exp_f32 v144, v144
s_waitcnt lgkmcnt(6)
Mva 44,196,208
TR 210,3,49664
VM 157,153,157
Mva 60,196,212
VM 147,9,147
Mva 76,196,216
TR 212,3,50176
Mva 92,196,220
v_exp_f32 v145, v145
s_waitcnt vmcnt(4)
s_barrier
s_waitcnt lgkmcnt(5)
Mva 32,200,224
s_mov_b32 m0, s70
TR 214,3,50688
Mva 48,200,228
s_mov_b32 s15, 151552
buffer_load_dwordx4 v18, s[24:27], s15 offen offset:0 lds
Mva 64,200,232
VM 158,154,158
VM 152,9,152
Mva 80,200,236
TR 192,2,51200
s_waitcnt lgkmcnt(5)
Mva 36,204,224
s_mov_b32 s15, 187392
buffer_load_dwordx4 v18, s[24:27], s15 offen offset:1024 lds
Mva 52,204,228
v_exp_f32 v146, v146
Mva 68,204,232
TR 194,2,51712
VF 144,144,13,13
Mva 84,204,236
s_mov_b32 s15, 223232
buffer_load_dwordx4 v18, s[24:27], s15 offen offset:2048 lds
s_waitcnt lgkmcnt(4)
Mva 40,208,224
VM 159,155,159
VM 153,9,153
Mva 56,208,228
TR 196,2,52224
Mva 72,208,232
s_mov_b32 s15, 259072
buffer_load_dwordx4 v18, s[24:27], s15 offen offset:3072 lds
Mva 88,208,236
TR 198,2,52736
s_waitcnt lgkmcnt(4)
Mva 44,212,224
v_exp_f32 v147, v147
Mva 60,212,228
VF 145,145,13,13
VM 154,9,154
Mva 76,212,232
TR 200,3,51200
Mva 92,212,236
TR 202,3,51712
s_waitcnt lgkmcnt(4)
Mva 32,192,240
v_exp_f32 v152, v152
Mva 48,192,244
VF 146,146,13,13
TR 204,3,52224
Mva 64,192,248
v_rcp_f32 v144, v144
Mva 80,192,252
TR 206,3,52736
VM 155,9,155
s_waitcnt lgkmcnt(4)
Mva 36,196,240
v_exp_f32 v153, v153
Mva 52,196,244
TR 208,2,0
VF 147,147,13,13
Mva 68,196,248
TR 210,2,512
Mva 84,196,252
v_rcp_f32 v145, v145
s_waitcnt lgkmcnt(4)
Mva 40,200,240
TR 212,2,1024
Mva 56,200,244
v_exp_f32 v154, v154
Mva 72,200,248
TR 214,2,1536
VF 152,152,13,13
Mva 88,200,252
v_rcp_f32 v146, v146
s_waitcnt lgkmcnt(4)
Mva 44,204,240
TR 192,3,0
VM 148,148,144
Mva 60,204,244
TR 194,3,512
Mva 76,204,248
v_exp_f32 v155, v155
Mva 92,204,252
VF 153,153,13,13
TR 196,3,1024
s_waitcnt vmcnt(4)
s_barrier
s_waitcnt lgkmcnt(5)
Mvv 32,208,224
s_mov_b32 m0, s71
TR 198,3,1536
Mvv 48,208,228
s_mov_b32 s15, 4224
buffer_load_dwordx4 v18, s[24:27], s15 offen offset:0 lds
Mvv 64,208,232
v_rcp_f32 v147, v147
Mvv 80,208,236
VM 149,149,145
VF 154,154,13,13
s_waitcnt lgkmcnt(4)
Mvv 36,212,224
s_mov_b32 s15, 40064
buffer_load_dwordx4 v18, s[24:27], s15 offen offset:1024 lds
Mvv 52,212,228
TR 200,2,2048
Mvv 68,212,232
TR 202,2,2560
Mvv 84,212,236
s_mov_b32 s15, 75904
buffer_load_dwordx4 v18, s[24:27], s15 offen offset:2048 lds
s_waitcnt lgkmcnt(4)
Mvv 40,192,224
v_rcp_f32 v152, v152
Mvv 56,192,228
VM 150,150,146
VF 155,155,13,13
Mvv 72,192,232
s_mov_b32 s15, 111744
buffer_load_dwordx4 v18, s[24:27], s15 offen offset:3072 lds
Mvv 88,192,236
TR 204,2,3072
s_waitcnt lgkmcnt(3)
Mvv 44,196,224
TR 206,2,3584
Mvv 60,196,228
v_rcp_f32 v153, v153
Mvv 76,196,232
VM 151,151,147
TR 208,3,2048
Mvv 92,196,236
v_rcp_f32 v154, v154
s_waitcnt lgkmcnt(3)
Mvv 32,200,240
TR 210,3,2560
VM 156,156,152
Mvv 48,200,244
v_rcp_f32 v155, v155
Mvv 64,200,248
TR 212,3,3072
VM 157,157,153
Mvv 80,200,252
TR 214,3,3584
VM 158,158,154
s_waitcnt lgkmcnt(4)
Mvv 36,204,240
VM 159,159,155
VC 144,148,149
Mvv 52,204,244
v_add_u32 v2, 0x10000, v19
v_add_u32 v3, 0x10000, v20
Mvv 68,204,248
TR 192,19,16384
VC 145,150,151
Mvv 84,204,252
TR 194,19,24576
VC 146,156,157
s_waitcnt lgkmcnt(4)
Mvv 40,208,240
VC 147,158,159
TR 196,20,16384
Mvv 56,208,244
TR 198,20,24576
Mvv 72,208,248
ds_write_b128 v5, v[144:147] offset:52224
Mvv 88,208,252
TR 200,19,17408
s_waitcnt lgkmcnt(6)
Mvv 44,212,240
TR 202,19,25600
Mvv 60,212,244
Mvv 76,212,248
TR 204,20,17408
Mvv 92,212,252
TR 206,20,25600
s_waitcnt lgkmcnt(4)
s_waitcnt vmcnt(4)
s_barrier
Zva 160,192,0
s_mov_b32 m0, s68
ds_read_b128 a[96:99], v4 offset:49152
Zva 168,192,4
s_mov_b32 s15, 151680
buffer_load_dwordx4 v18, s[24:27], s15 offen offset:0 lds
Zva 176,192,8
v_add_u32 v5, 0x10000, v5
VM 36,32,36
Zva 184,192,12
VM 37,33,37
VM 38,34,38
Zva 164,196,0
s_mov_b32 s15, 187520
buffer_load_dwordx4 v18, s[24:27], s15 offen offset:1024 lds
Zva 172,196,4
VM 32,6,32
VM 39,35,39
Zva 180,196,8
VM 33,6,33
VM 44,40,44
Zva 188,196,12
s_mov_b32 s15, 223360
buffer_load_dwordx4 v18, s[24:27], s15 offen offset:2048 lds
s_waitcnt lgkmcnt(3)
Mva 160,200,16
VM 34,6,34
TR 208,19,18432
Mva 168,200,20
TR 210,19,26624
Mva 176,200,24
v_exp_f32 v32, v32
Mva 184,200,28
s_mov_b32 s15, 259200
buffer_load_dwordx4 v18, s[24:27], s15 offen offset:3072 lds
VM 45,41,45
s_waitcnt lgkmcnt(3)
Mva 164,204,16
VM 35,6,35
TR 212,20,18432
Mva 172,204,20
v_exp_f32 v33, v33
Mva 180,204,24
TR 214,20,26624
VM 46,42,46
Mva 188,204,28
VM 40,6,40
TR 192,19,19456
s_waitcnt lgkmcnt(3)
Mva 160,208,32
v_exp_f32 v34, v34
Mva 168,208,36
VF 32,32,10,10
TR 194,19,27648
Mva 176,208,40
VM 47,43,47
VM 41,6,41
Mva 184,208,44
v_exp_f32 v35, v35
s_waitcnt lgkmcnt(2)
Mva 164,212,32
TR 196,20,19456
VF 33,33,10,10
Mva 172,212,36
TR 198,20,27648
VM 42,6,42
Mva 180,212,40
v_exp_f32 v40, v40
Mva 188,212,44
VF 34,34,10,10
ds_read_b128 a[100:103], v4 offset:50176
s_waitcnt lgkmcnt(3)
Mva 160,192,48
ds_read_b128 a[104:107], v4 offset:51200
ds_read_b128 a[108:111], v4 offset:52224
ds_read_b128 a[112:115], v4 offset:53248
ds_read_b128 a[116:119], v4 offset:54272
ds_read_b128 a[120:123], v4 offset:55296
ds_read_b128 a[124:127], v4 offset:56320
Mva 168,192,52
TR 200,19,32768
TR 202,19,40960
Mva 176,192,56
v_rcp_f32 v32, v32
VM 43,6,43
Mva 184,192,60
v_exp_f32 v41, v41
s_waitcnt lgkmcnt(9)
Mva 164,196,48
TR 204,20,32768
VF 35,35,10,10
Mva 172,196,52
s_sub_u32 s52, s56, 42991616
s_subb_u32 s53, s57, 0
s_mov_b32 s54, 37748736
Mva 180,196,56
TR 206,20,40960
v_rcp_f32 v33, v33
Mva 188,196,60
v_exp_f32 v42, v42
s_waitcnt vmcnt(4)
s_barrier
s_waitcnt lgkmcnt(2)
Mva 160,200,64
s_mov_b32 m0, s69
TR 208,19,33792
Mva 168,200,68
VF 40,40,10,10
TR 210,19,41984
Mva 176,200,72
s_mov_b32 s15, 4352
buffer_load_dwordx4 v18, s[24:27], s15 offen offset:0 lds
v_rcp_f32 v34, v34
Mva 184,200,76
VM 36,36,32
TR 212,20,33792
s_waitcnt lgkmcnt(3)
Mva 164,204,64
v_exp_f32 v43, v43
Mva 172,204,68
s_mov_b32 s15, 40192
buffer_load_dwordx4 v18, s[24:27], s15 offen offset:1024 lds
VF 41,41,10,10
Mva 180,204,72
TR 214,20,41984
Mva 188,204,76
v_rcp_f32 v35, v35
VM 37,37,33
s_waitcnt lgkmcnt(2)
Mva 160,208,80
s_mov_b32 s15, 76032
buffer_load_dwordx4 v18, s[24:27], s15 offen offset:2048 lds
VF 42,42,10,10
Mva 168,208,84
TR 192,19,34816
TR 194,19,43008
Mva 176,208,88
v_rcp_f32 v40, v40
Mva 184,208,92
s_mov_b32 s15, 111872
buffer_load_dwordx4 v18, s[24:27], s15 offen offset:3072 lds
VM 38,38,34
VF 43,43,10,10
s_waitcnt lgkmcnt(2)
Mva 164,212,80
TR 196,20,34816
TR 198,20,43008
Mva 172,212,84
v_rcp_f32 v41, v41
Mva 180,212,88
VM 39,39,35
TR 200,19,35840
Mva 188,212,92
v_rcp_f32 v42, v42
s_waitcnt lgkmcnt(3)
Mva 160,192,96
VM 44,44,40
TR 202,19,44032
Mva 168,192,100
v_rcp_f32 v43, v43
Mva 176,192,104
VM 45,45,41
VM 46,46,42
Mva 184,192,108
VM 47,47,43
TR 204,20,35840
s_waitcnt lgkmcnt(3)
Mva 164,196,96
VC 32,36,37
VC 33,38,39
Mva 172,196,100
TR 206,20,44032
VC 34,44,45
Mva 180,196,104
VC 35,46,47
TR 208,19,49152
Mva 188,196,108
ds_write_b128 v5, v[32:35] offset:0
s_waitcnt lgkmcnt(4)
Mva 160,200,112
TR 210,19,57344
VM 52,48,52
Mva 168,200,116
VM 53,49,53
VM 54,50,54
Mva 176,200,120
VM 48,7,48
TR 212,20,49152
Mva 184,200,124
VM 55,51,55
TR 214,20,57344
s_waitcnt lgkmcnt(5)
Mva 164,204,112
VM 49,7,49
VM 60,56,60
Mva 172,204,116
VM 50,7,50
TR 192,19,50176
Mva 180,204,120
v_exp_f32 v48, v48
Mva 188,204,124
TR 194,19,58368
VM 61,57,61
s_waitcnt vmcnt(4)
s_barrier
s_waitcnt lgkmcnt(4)
Zva 96,208,0
s_mov_b32 m0, s70
VM 51,7,51
Zva 104,208,4
s_mov_b32 s15, 151808
buffer_load_dwordx4 v18, s[24:27], s15 offen offset:0 lds
Zva 112,208,8
v_exp_f32 v49, v49
Zva 120,208,12
TR 196,20,50176
VM 62,58,62
s_waitcnt lgkmcnt(3)
Zva 100,212,0
)"
R"(s_mov_b32 s15, 187648
buffer_load_dwordx4 v18, s[24:27], s15 offen offset:1024 lds
Zva 108,212,4
TR 198,20,58368
VM 56,7,56
Zva 116,212,8
v_exp_f32 v50, v50
Zva 124,212,12
s_mov_b32 s15, 223488
buffer_load_dwordx4 v18, s[24:27], s15 offen offset:2048 lds
s_waitcnt lgkmcnt(2)
Mva 96,192,16
VF 48,48,11,11
TR 200,19,51200
Mva 104,192,20
VM 63,59,63
TR 202,19,59392
Mva 112,192,24
VM 57,7,57
TR 204,20,51200
Mva 120,192,28
s_mov_b32 s15, 259328
buffer_load_dwordx4 v18, s[24:27], s15 offen offset:3072 lds
v_exp_f32 v51, v51
s_waitcnt lgkmcnt(3)
Mva 100,196,16
VF 49,49,11,11
VM 58,7,58
Mva 108,196,20
TR 206,20,59392
Mva 116,196,24
v_exp_f32 v56, v56
Mva 124,196,28
VF 50,50,11,11
TR 208,19,52224
s_waitcnt lgkmcnt(3)
Mva 96,200,32
v_rcp_f32 v48, v48
Mva 104,200,36
VM 59,7,59
TR 210,19,60416
Mva 112,200,40
v_exp_f32 v57, v57
Mva 120,200,44
VF 51,51,11,11
TR 212,20,52224
s_waitcnt lgkmcnt(3)
Mva 100,204,32
v_rcp_f32 v49, v49
Mva 108,204,36
TR 214,20,60416
Mva 116,204,40
v_exp_f32 v58, v58
Mva 124,204,44
VF 56,56,11,11
TR 192,19,0
s_waitcnt lgkmcnt(3)
Mva 96,208,48
v_rcp_f32 v50, v50
Mva 104,208,52
TR 194,19,8192
VM 52,52,48
Mva 112,208,56
v_exp_f32 v59, v59
Mva 120,208,60
VF 57,57,11,11
TR 196,20,0
s_waitcnt lgkmcnt(3)
Mva 100,212,48
TR 198,20,8192
Mva 108,212,52
v_rcp_f32 v51, v51
Mva 116,212,56
VM 53,53,49
VF 58,58,11,11
Mva 124,212,60
TR 200,19,1024
s_waitcnt vmcnt(4)
s_barrier
s_waitcnt lgkmcnt(3)
Mva 96,192,64
s_mov_b32 m0, s71
TR 202,19,9216
Mva 104,192,68
v_rcp_f32 v56, v56
Mva 112,192,72
s_mov_b32 s15, 4608
buffer_load_dwordx4 v18, s[24:27], s15 offen offset:0 lds
VM 54,54,50
VF 59,59,11,11
Mva 120,192,76
v_rcp_f32 v57, v57
s_waitcnt lgkmcnt(2)
Mva 100,196,64
s_mov_b32 s15, 40448
buffer_load_dwordx4 v18, s[24:27], s15 offen offset:1024 lds
Mva 108,196,68
TR 204,20,1024
VM 55,55,51
Mva 116,196,72
TR 206,20,9216
v_rcp_f32 v58, v58
Mva 124,196,76
VM 60,60,56
TR 208,19,2048
s_waitcnt lgkmcnt(3)
Mva 96,200,80
s_mov_b32 s15, 76288
buffer_load_dwordx4 v18, s[24:27], s15 offen offset:2048 lds
Mva 104,200,84
v_rcp_f32 v59, v59
VM 61,61,57
Mva 112,200,88
TR 210,19,10240
VM 62,62,58
Mva 120,200,92
s_mov_b32 s15, 112128
buffer_load_dwordx4 v18, s[24:27], s15 offen offset:3072 lds
VM 63,63,59
s_waitcnt lgkmcnt(2)
Mva 100,204,80
VC 48,52,53
TR 212,20,2048
Mva 108,204,84
VC 49,54,55
VC 50,60,61
Mva 116,204,88
TR 214,20,10240
VC 51,62,63
Mva 124,204,92
ds_write_b128 v5, v[48:51] offset:1024
s_waitcnt lgkmcnt(3)
Mva 96,208,96
TR 192,19,3072
VM 68,64,68
Mva 104,208,100
TR 194,19,11264
VM 69,65,69
Mva 112,208,104
VM 70,66,70
VM 64,8,64
Mva 120,208,108
VM 71,67,71
TR 196,20,3072
s_waitcnt lgkmcnt(4)
Mva 100,212,96
VM 65,8,65
TR 198,20,11264
Mva 108,212,100
VM 76,72,76
VM 66,8,66
Mva 116,212,104
v_exp_f32 v64, v64
Mva 124,212,108
TR 200,19,16384
VM 77,73,77
s_waitcnt lgkmcnt(3)
Mva 96,192,112
TR 202,19,24576
VM 67,8,67
Mva 104,192,116
v_exp_f32 v65, v65
Mva 112,192,120
VM 78,74,78
TR 204,20,16384
Mva 120,192,124
VM 72,8,72
TR 206,20,24576
s_waitcnt lgkmcnt(4)
Mva 100,196,112
v_exp_f32 v66, v66
Mva 108,196,116
VF 64,64,12,12
VM 79,75,79
Mva 116,196,120
TR 208,19,17408
VM 73,8,73
Mva 124,196,124
TR 210,19,25600
s_waitcnt vmcnt(4)
s_barrier
s_waitcnt lgkmcnt(4)
Zva 128,200,0
s_mov_b32 m0, s68
TR 212,20,17408
Zva 136,200,4
s_mov_b32 s15, 152064
buffer_load_dwordx4 v18, s[24:27], s15 offen offset:0 lds
Zva 144,200,8
v_exp_f32 v67, v67
Zva 152,200,12
VF 65,65,12,12
VM 74,8,74
s_waitcnt lgkmcnt(3)
Zva 132,204,0
s_mov_b32 s15, 187904
buffer_load_dwordx4 v18, s[24:27], s15 offen offset:1024 lds
Zva 140,204,4
v_exp_f32 v72, v72
Zva 148,204,8
TR 214,20,25600
VF 66,66,12,12
Zva 156,204,12
s_mov_b32 s15, 223744
buffer_load_dwordx4 v18, s[24:27], s15 offen offset:2048 lds
s_waitcnt lgkmcnt(2)
Mva 128,208,16
v_rcp_f32 v64, v64
VM 75,8,75
Mva 136,208,20
TR 192,19,18432
TR 194,19,26624
Mva 144,208,24
v_exp_f32 v73, v73
Mva 152,208,28
s_mov_b32 s15, 259584
buffer_load_dwordx4 v18, s[24:27], s15 offen offset:3072 lds
VF 67,67,12,12
s_waitcnt lgkmcnt(2)
Mva 132,212,16
v_rcp_f32 v65, v65
Mva 140,212,20
TR 196,20,18432
TR 198,20,26624
Mva 148,212,24
v_exp_f32 v74, v74
Mva 156,212,28
VF 72,72,12,12
TR 200,19,19456
s_waitcnt lgkmcnt(3)
Mva 128,192,32
v_rcp_f32 v66, v66
Mva 136,192,36
VM 68,68,64
TR 202,19,27648
Mva 144,192,40
v_exp_f32 v75, v75
Mva 152,192,44
VF 73,73,12,12
TR 204,20,19456
s_waitcnt lgkmcnt(3)
Mva 132,196,32
v_rcp_f32 v67, v67
Mva 140,196,36
TR 206,20,27648
VM 69,69,65
Mva 148,196,40
VF 74,74,12,12
TR 208,19,32768
Mva 156,196,44
v_rcp_f32 v72, v72
s_waitcnt lgkmcnt(3)
Mva 128,200,48
VM 70,70,66
VF 75,75,12,12
Mva 136,200,52
TR 210,19,40960
Mva 144,200,56
v_rcp_f32 v73, v73
Mva 152,200,60
VM 71,71,67
TR 212,20,32768
s_waitcnt lgkmcnt(3)
Mva 132,204,48
v_rcp_f32 v74, v74
Mva 140,204,52
TR 214,20,40960
VM 76,76,72
Mva 148,204,56
v_rcp_f32 v75, v75
Mva 156,204,60
VM 77,77,73
TR 192,19,33792
s_waitcnt vmcnt(4)
s_barrier
s_waitcnt lgkmcnt(3)
Mva 128,208,64
s_mov_b32 m0, s69
VM 78,78,74
Mva 136,208,68
s_mov_b32 s15, 299008
buffer_load_dwordx4 v18, s[24:27], s15 offen offset:0 lds
Mva 144,208,72
TR 194,19,41984
VM 79,79,75
Mva 152,208,76
VC 64,68,69
VC 65,70,71
s_waitcnt lgkmcnt(2)
Mva 132,212,64
VC 66,76,77
TR 196,20,33792
Mva 140,212,68
s_mov_b32 s15, 334848
buffer_load_dwordx4 v18, s[24:27], s15 offen offset:1024 lds
VC 67,78,79
Mva 148,212,72
TR 198,20,41984
Mva 156,212,76
ds_write_b128 v5, v[64:67] offset:2048
s_waitcnt lgkmcnt(3)
Mva 128,192,80
s_mov_b32 s15, 297088
buffer_load_dwordx4 v18, s[24:27], s15 offen offset:2048 lds
VM 84,80,84
Mva 136,192,84
TR 200,19,34816
VM 85,81,85
Mva 144,192,88
TR 202,19,43008
VM 86,82,86
Mva 152,192,92
s_mov_b32 s15, 332928
buffer_load_dwordx4 v18, s[24:27], s15 offen offset:3072 lds
VM 80,9,80
VM 87,83,87
s_waitcnt lgkmcnt(3)
Mva 132,196,80
VM 81,9,81
TR 204,20,34816
Mva 140,196,84
VM 92,88,92
TR 206,20,43008
Mva 148,196,88
VM 82,9,82
TR 208,19,35840
Mva 156,196,92
v_exp_f32 v80, v80
s_waitcnt lgkmcnt(3)
Mva 128,200,96
VM 93,89,93
VM 83,9,83
Mva 136,200,100
TR 210,19,44032
Mva 144,200,104
v_exp_f32 v81, v81
Mva 152,200,108
VM 94,90,94
VM 88,9,88
s_waitcnt lgkmcnt(2)
Mva 132,204,96
TR 212,20,35840
TR 214,20,44032
Mva 140,204,100
v_exp_f32 v82, v82
Mva 148,204,104
VF 80,80,13,13
VM 95,91,95
Mva 156,204,108
VM 89,9,89
TR 192,19,49152
s_waitcnt lgkmcnt(3)
Mva 128,208,112
v_exp_f32 v83, v83
Mva 136,208,116
TR 194,19,57344
VF 81,81,13,13
Mva 144,208,120
VM 90,9,90
TR 196,20,49152
Mva 152,208,124
v_exp_f32 v88, v88
s_waitcnt lgkmcnt(3)
Mva 132,212,112
VF 82,82,13,13
TR 198,20,57344
Mva 140,212,116
v_rcp_f32 v80, v80
Mva 148,212,120
VM 91,9,91
TR 200,19,50176
Mva 156,212,124
v_exp_f32 v89, v89
s_waitcnt vmcnt(4)
s_barrier
s_waitcnt lgkmcnt(3)
Zva 224,192,0
s_mov_b32 m0, s70
TR 202,19,58368
Zva 232,192,4
s_mov_b32 s15, 299264
buffer_load_dwordx4 v18, s[24:27], s15 offen offset:0 lds
Zva 240,192,8
VF 83,83,13,13
TR 204,20,50176
Zva 248,192,12
v_rcp_f32 v81, v81
s_waitcnt lgkmcnt(3)
Zva 228,196,0
s_mov_b32 s15, 335104
buffer_load_dwordx4 v18, s[24:27], s15 offen offset:1024 lds
Zva 236,196,4
v_exp_f32 v90, v90
Zva 244,196,8
TR 206,20,58368
VF 88,88,13,13
Zva 252,196,12
v_rcp_f32 v82, v82
s_waitcnt lgkmcnt(2)
Mva 224,200,16
s_mov_b32 s15, 297472
buffer_load_dwordx4 v18, s[24:27], s15 offen offset:2048 lds
VM 84,84,80
Mva 232,200,20
TR 208,19,51200
TR 210,19,59392
Mva 240,200,24
v_exp_f32 v91, v91
VF 89,89,13,13
Mva 248,200,28
s_mov_b32 s15, 333312
buffer_load_dwordx4 v18, s[24:27], s15 offen offset:3072 lds
s_waitcnt lgkmcnt(2)
Mva 228,204,16
v_rcp_f32 v83, v83
Mva 236,204,20
TR 212,20,51200
VM 85,85,81
VF 90,90,13,13
Mva 244,204,24
TR 214,20,59392
Mva 252,204,28
v_rcp_f32 v88, v88
s_waitcnt lgkmcnt(2)
Mva 224,208,32
VM 86,86,82
TR 192,19,52224
Mva 232,208,36
VF 91,91,13,13
TR 194,19,60416
Mva 240,208,40
v_rcp_f32 v89, v89
Mva 248,208,44
VM 87,87,83
TR 196,20,52224
s_waitcnt lgkmcnt(3)
Mva 228,212,32
v_rcp_f32 v90, v90
Mva 236,212,36
VM 92,92,88
TR 198,20,60416
Mva 244,212,40
v_rcp_f32 v91, v91
Mva 252,212,44
VM 93,93,89
VM 94,94,90
s_waitcnt lgkmcnt(2)
Mva 224,192,48
TR 200,19,0
VM 95,95,91
Mva 232,192,52
TR 202,19,8192
VC 80,84,85
Mva 240,192,56
VC 81,86,87
VC 82,92,93
Mva 248,192,60
VC 83,94,95
TR 204,20,0
s_waitcnt lgkmcnt(3)
Mva 228,196,48
TR 206,20,8192
Mva 236,196,52
ds_write_b128 v5, v[80:83] offset:3072
Mva 244,196,56
TR 208,19,1024
Mva 252,196,60
TR 210,19,9216
s_waitcnt lgkmcnt(2)
s_waitcnt vmcnt(4)
s_barrier
Mva 224,200,64
s_mov_b32 m0, s71
v_add_u32 v4, 0x10000, v4
Mva 232,200,68
s_mov_b32 s15, 4480
buffer_load_dwordx4 v18, s[24:27], s15 offen offset:0 lds
Mva 240,200,72
TR 212,20,1024
Mva 248,200,76
TR 214,20,9216
Mva 228,204,64
s_mov_b32 s15, 40320
buffer_load_dwordx4 v18, s[24:27], s15 offen offset:1024 lds
Mva 236,204,68
TR 192,19,2048
Mva 244,204,72
TR 194,19,10240
Mva 252,204,76
s_mov_b32 s15, 76160
buffer_load_dwordx4 v18, s[24:27], s15 offen offset:2048 lds
s_waitcnt lgkmcnt(4)
Mva 224,208,80
TR 196,20,2048
Mva 232,208,84
TR 198,20,10240
Mva 240,208,88
s_mov_b32 s15, 112000
buffer_load_dwordx4 v18, s[24:27], s15 offen offset:3072 lds
Mva 248,208,92
TR 200,19,3072
s_waitcnt lgkmcnt(5)
Mva 228,212,80
TR 202,19,11264
Mva 236,212,84
ds_read_b128 a[128:131], v4 offset:0
Mva 244,212,88
TR 204,20,3072
Mva 252,212,92
TR 206,20,11264
s_waitcnt lgkmcnt(7)
Mva 224,192,96
ds_read_b128 a[132:135], v4 offset:1024
Mva 232,192,100
ds_read_b128 a[136:139], v4 offset:2048
Mva 240,192,104
ds_read_b128 a[140:143], v4 offset:3072
Mva 248,192,108
ds_read_b128 a[144:147], v4 offset:4096
s_waitcnt lgkmcnt(9)
Mva 228,196,96
ds_read_b128 a[148:151], v4 offset:5120
Mva 236,196,100
ds_read_b128 a[152:155], v4 offset:6144
Mva 244,196,104
ds_read_b128 a[156:159], v4 offset:7168
Mva 252,196,108
TR 208,19,16384
s_waitcnt lgkmcnt(11)
Mva 224,200,112
TR 210,19,24576
Mva 232,200,116
TR 212,20,16384
Mva 240,200,120
TR 214,20,24576
Mva 248,200,124
TR 192,19,17408
s_waitcnt lgkmcnt(12)
Mva 228,204,112
TR 194,19,25600
Mva 236,204,116
Mva 244,204,120
TR 196,20,17408
Mva 252,204,124
TR 198,20,25600
s_waitcnt vmcnt(4)
s_barrier
s_waitcnt lgkmcnt(6)
Mva 160,208,128
s_mov_b32 m0, s68
Mva 168,208,132
s_mov_b32 s15, 151936
buffer_load_dwordx4 v18, s[24:27], s15 offen offset:0 lds
Mva 176,208,136
TR 200,19,18432
Mva 184,208,140
TR 202,19,26624
s_waitcnt lgkmcnt(6)
Mva 164,212,128
s_mov_b32 s15, 187776
buffer_load_dwordx4 v18, s[24:27], s15 offen offset:1024 lds
Mva 172,212,132
Mva 180,212,136
TR 204,20,18432
Mva 188,212,140
s_mov_b32 s15, 223616
buffer_load_dwordx4 v18, s[24:27], s15 offen offset:2048 lds
s_waitcnt lgkmcnt(5)
Mva 160,192,144
TR 206,20,26624
Mva 168,192,148
Mva 176,192,152
s_mov_b32 s15, 259456
buffer_load_dwordx4 v18, s[24:27], s15 offen offset:3072 lds
Mva 184,192,156
TR 208,19,19456
s_waitcnt lgkmcnt(5)
Mva 164,196,144
TR 210,19,27648
Mva 172,196,148
Mva 180,196,152
TR 212,20,19456
Mva 188,196,156
TR 214,20,27648
s_waitcnt lgkmcnt(6)
Mva 96,200,128
Mva 104,200,132
Mva 112,200,136
VC 160,160,161
VC 161,162,163
Mva 120,200,140
VC 162,164,165
VC 163,166,167
s_waitcnt lgkmcnt(4)
Mva 100,204,128
s_mov_b32 s67, 0
buffer_store_dwordx4 v[160:163], v21, s[52:55], s67 offen sc1
Mva 108,204,132
TR 192,19,32768
VC 168,168,169
)"
R"(Mva 116,204,136
TR 194,19,40960
VC 169,170,171
Mva 124,204,140
VC 170,172,173
VC 171,174,175
s_waitcnt lgkmcnt(4)
Mva 96,208,144
TR 196,20,32768
Mva 104,208,148
TR 198,20,40960
Mva 112,208,152
s_mov_b32 s67, 9216
buffer_store_dwordx4 v[168:171], v21, s[52:55], s67 offen sc1
Mva 120,208,156
VC 176,176,177
VC 177,178,179
s_waitcnt lgkmcnt(4)
Mva 100,212,144
TR 200,19,33792
VC 178,180,181
Mva 108,212,148
TR 202,19,41984
VC 179,182,183
Mva 116,212,152
s_mov_b32 s67, 18432
buffer_store_dwordx4 v[176:179], v21, s[52:55], s67 offen sc1
Mva 124,212,156
TR 204,20,33792
VC 184,184,185
s_waitcnt vmcnt(7)
s_barrier
s_waitcnt lgkmcnt(5)
Mva 128,192,128
s_mov_b32 m0, s69
TR 206,20,41984
Mva 136,192,132
s_mov_b32 s15, 299392
buffer_load_dwordx4 v18, s[24:27], s15 offen offset:0 lds
Mva 144,192,136
VC 185,186,187
VC 186,188,189
Mva 152,192,140
VC 187,190,191
TR 208,19,34816
s_waitcnt lgkmcnt(5)
Mva 132,196,128
s_mov_b32 s15, 335232
buffer_load_dwordx4 v18, s[24:27], s15 offen offset:1024 lds
Mva 140,196,132
TR 210,19,43008
Mva 148,196,136
s_mov_b32 s67, 27648
buffer_store_dwordx4 v[184:187], v21, s[52:55], s67 offen sc1
Mva 156,196,140
s_mov_b32 s15, 2688
buffer_load_dwordx4 v18, s[24:27], s15 offen offset:2048 lds
s_waitcnt lgkmcnt(4)
Mva 128,200,144
TR 212,20,34816
Mva 136,200,148
TR 214,20,43008
Mva 144,200,152
s_mov_b32 s15, 38528
buffer_load_dwordx4 v18, s[24:27], s15 offen offset:3072 lds
Mva 152,200,156
VC 96,96,97
VC 97,98,99
s_waitcnt lgkmcnt(4)
Mva 132,204,144
VC 98,100,101
s_load_dwordx4 s[72:75], s[8:9], 96
Mva 140,204,148
VC 99,102,103
v_and_b32 v180, 7, v0
Mva 148,204,152
TR 192,19,35840
v_lshrrev_b32 v181, 5, v0
Mva 156,204,156
s_mov_b32 s67, 1024
buffer_store_dwordx4 v[96:99], v21, s[52:55], s67 offen sc1
s_waitcnt lgkmcnt(0)
Mva 224,208,128
TR 194,19,44032
VC 104,104,105
Mva 232,208,132
VC 105,106,107
v_bfe_u32 v182, v0, 3, 2
Mva 240,208,136
VC 106,108,109
TR 196,20,35840
Mva 248,208,140
VC 107,110,111
v_and_b32 v183, 4, v0
Mva 228,212,128
TR 198,20,44032
v_and_b32 v184, 3, v0
Mva 236,212,132
s_mov_b32 s67, 10240
buffer_store_dwordx4 v[104:107], v21, s[52:55], s67 offen sc1
Mva 244,212,136
VC 112,112,113
TR 200,19,49152
Mva 252,212,140
VC 113,114,115
v_lshl_add_u32 v183, v183, 2, v184
s_waitcnt lgkmcnt(3)
Mva 224,192,144
TR 202,19,57344
VC 114,116,117
Mva 232,192,148
s_lshl_b32 s76, s64, 3
VC 115,118,119
Mva 240,192,152
v_lshl_add_u32 v188, v182, 4, v180
TR 204,20,49152
Mva 248,192,156
s_mov_b32 s67, 19456
buffer_store_dwordx4 v[112:115], v21, s[52:55], s67 offen sc1
s_waitcnt lgkmcnt(3)
Mva 228,196,144
TR 206,20,57344
VC 120,120,121
Mva 236,196,148
v_add_lshl_u32 v188, v188, s76, 4
VC 121,122,123
Mva 244,196,152
VC 122,124,125
v_mul_u32_u24 v185, 147456, v181
Mva 252,196,156
TR 208,19,50176
VC 123,126,127
s_waitcnt vmcnt(11)
s_barrier
s_waitcnt lgkmcnt(3)
Zva 32,200,0
s_mov_b32 m0, s70
TR 210,19,58368
Zva 40,200,4
s_mov_b32 s15, 78464
buffer_load_dwordx4 v18, s[24:27], s15 offen offset:0 lds
Zva 48,200,8
v_add_u32 v188, v185, v188
s_lshr_b32 s90, s13, 1
s_and_b32 s91, s13, 1
Zva 56,200,12
s_mov_b32 s67, 28672
buffer_store_dwordx4 v[120:123], v21, s[52:55], s67 offen sc1
s_waitcnt lgkmcnt(2)
Zva 36,204,0
VC 128,128,129
s_lshr_b32 s77, s13, 3
s_lshl_b32 s93, s14, 6
Zva 44,204,4
s_mov_b32 s15, 114304
buffer_load_dwordx4 v18, s[24:27], s15 offen offset:1024 lds
TR 212,20,50176
VC 129,130,131
Zva 52,204,8
TR 214,20,58368
VC 130,132,133
s_add_u32 s93, s93, s77
Zva 60,204,12
VC 131,134,135
s_add_u32 s93, s93, s91
s_add_u32 s93, s93, s91
s_waitcnt lgkmcnt(2)
Mva 32,208,16
s_mov_b32 s15, 150144
buffer_load_dwordx4 v18, s[24:27], s15 offen offset:2048 lds
Mva 40,208,20
TR 192,19,51200
s_lshl_b32 s93, s93, 2
s_and_b32 s92, s90, 3
Mva 48,208,24
TR 194,19,59392
s_mov_b32 s67, 2048
buffer_store_dwordx4 v[128:131], v21, s[52:55], s67 offen sc1
Mva 56,208,28
s_mov_b32 s15, 185984
buffer_load_dwordx4 v18, s[24:27], s15 offen offset:3072 lds
VC 136,136,137
s_add_u32 s93, s93, s92
s_waitcnt lgkmcnt(2)
Mva 36,212,16
VC 137,138,139
s_mul_i32 s93, s93, 9
s_add_u32 s93, s93, s65
Mva 44,212,20
TR 196,20,51200
VC 138,140,141
Mva 52,212,24
TR 198,20,59392
VC 139,142,143
s_lshl_b32 s93, s93, 10
Mva 60,212,28
s_mov_b32 s67, 11264
buffer_store_dwordx4 v[136:139], v21, s[52:55], s67 offen sc1
s_waitcnt lgkmcnt(2)
Mva 32,192,32
v_add_u32 v188, s93, v188
TR 200,19,52224
Mva 40,192,36
VC 144,144,145
s_lshl_b32 s93, s77, 4
s_and_b32 s90, s90, 3
Mva 48,192,40
TR 202,19,60416
VC 145,146,147
Mva 56,192,44
VC 146,148,149
s_add_u32 s93, s93, s90
s_add_u32 s93, s93, s76
s_waitcnt lgkmcnt(2)
Mva 36,196,32
VC 147,150,151
TR 204,20,52224
Mva 44,196,36
s_lshl_b32 s93, s93, 2
TR 206,20,60416
Mva 52,196,40
s_mov_b32 s67, 20480
buffer_store_dwordx4 v[144:147], v21, s[52:55], s67 offen sc1
Mva 60,196,44
VC 152,152,153
v_add_u32 v183, s93, v183
s_waitcnt lgkmcnt(2)
Mva 32,200,48
VC 153,154,155
TR 208,19,0
Mva 40,200,52
VC 154,156,157
v_and_b32 v185, 16, v0
Mva 48,200,56
TR 210,19,8192
VC 155,158,159
Mva 56,200,60
v_lshl_add_u32 v185, v181, 3, v185
TR 212,20,0
s_waitcnt lgkmcnt(3)
Mva 36,204,48
s_mov_b32 s67, 29696
buffer_store_dwordx4 v[152:155], v21, s[52:55], s67 offen sc1
Mva 44,204,52
v_and_b32 v182, 1, v182
TR 214,20,8192
Mva 52,204,56
v_lshl_add_u32 v185, v182, 2, v185
s_lshl_b32 s93, s65, 1
s_add_u32 s93, s93, s91
Mva 60,204,60
TR 192,19,1024
s_lshl_b32 s93, s93, 5
s_waitcnt vmcnt(12)
s_barrier
s_waitcnt lgkmcnt(3)
Mva 32,208,64
s_mov_b32 m0, s71
TR 194,19,9216
Mva 40,208,68
s_mov_b32 s15, 225920
buffer_load_dwordx4 v18, s[24:27], s15 offen offset:0 lds
Mva 48,208,72
v_add_lshl_u32 v185, v185, s93, 1
s_lshl_b32 s93, s14, 7
Mva 56,208,76
TR 196,20,1024
v_add_u32 v184, s93, v183
s_waitcnt lgkmcnt(3)
Mva 36,212,64
s_mov_b32 s15, 261760
buffer_load_dwordx4 v18, s[24:27], s15 offen offset:1024 lds
Mva 44,212,68
TR 198,20,9216
v_mul_u32_u24 v184, 1152, v184
Mva 52,212,72
TR 200,19,2048
v_add_u32 v184, v184, v185
Mva 60,212,76
s_mov_b32 s15, 297600
buffer_load_dwordx4 v18, s[24:27], s15 offen offset:2048 lds
s_waitcnt lgkmcnt(3)
Mva 32,192,80
TR 202,19,10240
v_lshlrev_b32 v189, 1, v184
Mva 40,192,84
TR 204,20,2048
v_lshlrev_b32 v183, 3, v183
Mva 48,192,88
s_mov_b32 s15, 333440
buffer_load_dwordx4 v18, s[24:27], s15 offen offset:3072 lds
Mva 56,192,92
TR 206,20,10240
s_lshl_b32 s91, s91, 1
s_add_u32 s91, s91, s77
s_waitcnt lgkmcnt(4)
Mva 36,196,80
TR 208,19,3072
s_lshl_b32 s93, s14, 2
s_add_u32 s91, s91, s93
Mva 44,196,84
TR 210,19,11264
s_lshl_b32 s91, s91, 9
s_add_u32 s91, s91, 163840
Mva 52,196,88
TR 212,20,3072
v_lshl_add_u32 v190, v0, 3, s91
Mva 60,196,92
TR 214,20,11264
s_add_u32 s93, s93, s11
s_lshl_b32 s93, s93, 6
s_waitcnt lgkmcnt(6)
Mva 32,200,96
s_add_u32 s93, s93, s13
s_lshl_b32 s93, s93, 3
s_add_u32 s93, s93, 163840
Mva 40,200,100
v_bfrev_b32 v191, 1
v_mov_b32 v186, s93
Mva 48,200,104
TR 192,19,16384
v_cmp_eq_u32 vcc, 0, v0
Mva 56,200,108
TR 194,19,24576
v_cndmask_b32 v191, v191, v186, vcc
s_waitcnt lgkmcnt(6)
Mva 36,204,96
v_mov_b32 v186, s100
v_mov_b32 v187, s100
Mva 44,204,100
s_mov_b32 s92, 0x40000
s_mov_b32 s16, s74
s_mov_b32 s17, s75
s_mov_b32 s18, 4718592
Mva 52,204,104
TR 196,20,16384
s_mov_b32 s19, 0x20000
s_mov_b32 s20, s72
Mva 60,204,108
TR 198,20,24576
s_mov_b32 s21, s73
s_mov_b32 s22, 1152
s_waitcnt lgkmcnt(6)
Mva 32,208,112
s_mov_b32 s23, 0x20000
Mva 40,208,116
Mva 48,208,120
TR 200,19,17408
Mva 56,208,124
TR 202,19,25600
s_waitcnt lgkmcnt(6)
Mva 36,212,112
Mva 44,212,116
Mva 52,212,120
TR 204,20,17408
Mva 60,212,124
TR 206,20,25600
s_waitcnt vmcnt(7)
s_barrier
s_waitcnt lgkmcnt(6)
Mva 32,192,128
s_mov_b32 m0, s68
Mva 40,192,132
s_mov_b32 s15, 4864
buffer_load_dwordx4 v18, s[24:27], s15 offen offset:0 lds
Mva 48,192,136
TR 208,19,18432
Mva 56,192,140
TR 210,19,26624
s_waitcnt lgkmcnt(6)
Mva 36,196,128
s_mov_b32 s15, 40704
buffer_load_dwordx4 v18, s[24:27], s15 offen offset:1024 lds
Mva 44,196,132
Mva 52,196,136
TR 212,20,18432
Mva 60,196,140
s_mov_b32 s15, 76544
buffer_load_dwordx4 v18, s[24:27], s15 offen offset:2048 lds
s_waitcnt lgkmcnt(5)
Mva 32,200,144
TR 214,20,26624
Mva 40,200,148
Mva 48,200,152
s_mov_b32 s15, 112384
buffer_load_dwordx4 v18, s[24:27], s15 offen offset:3072 lds
Mva 56,200,156
TR 192,19,19456
s_waitcnt lgkmcnt(5)
Mva 36,204,144
TR 194,19,27648
Mva 44,204,148
Mva 52,204,152
TR 196,20,19456
Mva 60,204,156
TR 198,20,27648
s_waitcnt lgkmcnt(6)
Zva 64,208,0
Zva 72,208,4
Zva 80,208,8
VC 32,32,33
VC 33,34,35
Zva 88,208,12
VC 34,36,37
VC 35,38,39
s_waitcnt lgkmcnt(4)
Zva 68,212,0
s_mov_b32 s67, 3072
buffer_store_dwordx4 v[32:35], v21, s[52:55], s67 offen sc1
Zva 76,212,4
TR 200,19,32768
VC 40,40,41
Zva 84,212,8
TR 202,19,40960
VC 41,42,43
Zva 92,212,12
VC 42,44,45
VC 43,46,47
s_waitcnt lgkmcnt(4)
Mva 64,192,16
TR 204,20,32768
Mva 72,192,20
TR 206,20,40960
Mva 80,192,24
s_mov_b32 s67, 12288
buffer_store_dwordx4 v[40:43], v21, s[52:55], s67 offen sc1
Mva 88,192,28
VC 48,48,49
VC 49,50,51
s_waitcnt lgkmcnt(4)
Mva 68,196,16
TR 208,19,33792
VC 50,52,53
Mva 76,196,20
TR 210,19,41984
VC 51,54,55
Mva 84,196,24
s_mov_b32 s67, 21504
buffer_store_dwordx4 v[48:51], v21, s[52:55], s67 offen sc1
Mva 92,196,28
TR 212,20,33792
VC 56,56,57
s_waitcnt vmcnt(7)
s_barrier
s_waitcnt lgkmcnt(5)
Mva 64,200,32
s_mov_b32 m0, s69
TR 214,20,41984
Mva 72,200,36
s_mov_b32 s15, 152320
buffer_load_dwordx4 v18, s[24:27], s15 offen offset:0 lds
Mva 80,200,40
VC 57,58,59
VC 58,60,61
Mva 88,200,44
VC 59,62,63
TR 192,19,34816
s_waitcnt lgkmcnt(5)
Mva 68,204,32
s_mov_b32 s15, 188160
buffer_load_dwordx4 v18, s[24:27], s15 offen offset:1024 lds
Mva 76,204,36
TR 194,19,43008
Mva 84,204,40
s_mov_b32 s67, 30720
buffer_store_dwordx4 v[56:59], v21, s[52:55], s67 offen sc1
Mva 92,204,44
s_mov_b32 s15, 224000
buffer_load_dwordx4 v18, s[24:27], s15 offen offset:2048 lds
s_waitcnt lgkmcnt(4)
Mva 64,208,48
TR 196,20,34816
Mva 72,208,52
TR 198,20,43008
Mva 80,208,56
s_mov_b32 s15, 259840
buffer_load_dwordx4 v18, s[24:27], s15 offen offset:3072 lds
Mva 88,208,60
VC 224,224,225
VC 225,226,227
s_waitcnt lgkmcnt(4)
Mva 68,212,48
VC 226,228,229
VC 227,230,231
Mva 76,212,52
buffer_load_dwordx2 v[52:53], v183, s[60:63], 0 offen sc1
Mva 84,212,56
TR 200,19,35840
Mva 92,212,60
s_mov_b32 s67, 4096
buffer_store_dwordx4 v[224:227], v21, s[52:55], s67 offen sc1
s_waitcnt lgkmcnt(3)
Mva 64,192,64
buffer_load_dwordx2 v[32:33], v184, s[48:51], 0 offen offset:0
Mva 72,192,68
TR 202,19,44032
VC 232,232,233
Mva 80,192,72
VC 233,234,235
VC 234,236,237
Mva 88,192,76
buffer_load_dwordx2 v[42:43], v185, s[20:23], 0 offen offset:0
s_waitcnt lgkmcnt(2)
Mva 68,196,64
TR 204,20,35840
VC 235,238,239
Mva 76,196,68
TR 206,20,44032
Mva 84,196,72
s_mov_b32 s67, 13312
buffer_store_dwordx4 v[232:235], v21, s[52:55], s67 offen sc1
Mva 92,196,76
buffer_load_dwordx2 v[34:35], v184, s[48:51], 0 offen offset:256
s_waitcnt lgkmcnt(2)
Mva 64,200,80
VC 240,240,241
TR 208,19,49152
Mva 72,200,84
VC 241,242,243
TR 210,19,57344
Mva 80,200,88
VC 242,244,245
buffer_load_dwordx2 v[44:45], v185, s[20:23], 0 offen offset:256
Mva 88,200,92
VC 243,246,247
TR 212,20,49152
s_waitcnt lgkmcnt(3)
Mva 68,204,80
buffer_load_dwordx2 v[36:37], v184, s[48:51], 0 offen offset:512
Mva 76,204,84
s_mov_b32 s67, 22528
buffer_store_dwordx4 v[240:243], v21, s[52:55], s67 offen sc1
Mva 84,204,88
TR 214,20,57344
VC 248,248,249
Mva 92,204,92
VC 249,250,251
buffer_load_dwordx2 v[46:47], v185, s[20:23], 0 offen offset:512
s_waitcnt vmcnt(18)
s_barrier
s_waitcnt lgkmcnt(2)
Mva 64,208,96
s_mov_b32 m0, s70
VC 250,252,253
Mva 72,208,100
TR 192,19,50176
VC 251,254,255
Mva 80,208,104
s_mov_b32 s15, 299776
buffer_load_dwordx4 v18, s[24:27], s15 offen offset:0 lds
TR 194,19,58368
buffer_load_dwordx2 v[38:39], v184, s[48:51], 0 offen offset:768
Mva 88,208,108
s_mov_b32 s67, 31744
buffer_store_dwordx4 v[248:251], v21, s[52:55], s67 offen sc1
s_waitcnt lgkmcnt(2)
Mva 68,212,96
s_mov_b32 s15, 335616
buffer_load_dwordx4 v18, s[24:27], s15 offen offset:1024 lds
Mva 76,212,100
TR 196,20,50176
buffer_load_dwordx2 v[48:49], v185, s[20:23], 0 offen offset:768
Mva 84,212,104
TR 198,20,58368
Mva 92,212,108
s_mov_b32 s15, 2944
buffer_load_dwordx4 v18, s[24:27], s15 offen offset:2048 lds
s_waitcnt lgkmcnt(2)
Mva 64,192,112
buffer_load_dwordx2 v[40:41], v184, s[48:51], 0 offen offset:1024
Mva 72,192,116
TR 200,19,51200
TR 202,19,59392
Mva 80,192,120
s_mov_b32 s15, 38784
buffer_load_dwordx4 v18, s[24:27], s15 offen offset:3072 lds
Mva 88,192,124
buffer_load_dwordx2 v[50:51], v185, s[20:23], 0 offen offset:1024
s_waitcnt lgkmcnt(2)
Mva 68,196,112
TR 204,20,51200
TR 206,20,59392
Mva 76,196,116
TR 208,19,52224
Mva 84,196,120
TR 210,19,60416
Mva 92,196,124
TR 212,20,52224
s_waitcnt lgkmcnt(5)
Mva 64,200,128
TR 214,20,60416
Mva 72,200,132
Mva 80,200,136
TR 192,19,0
Mva 88,200,140
TR 194,19,8192
s_waitcnt lgkmcnt(6)
Mva 68,204,128
Mva 76,204,132
Mva 84,204,136
)"
R"(TR 196,20,0
Mva 92,204,140
TR 198,20,8192
s_waitcnt lgkmcnt(6)
Mva 64,208,144
Mva 72,208,148
Mva 80,208,152
TR 200,19,1024
Mva 88,208,156
TR 202,19,9216
s_waitcnt lgkmcnt(6)
Mva 68,212,144
Mva 76,212,148
Mva 84,212,152
TR 204,20,1024
Mva 92,212,156
TR 206,20,9216
s_waitcnt vmcnt(19)
s_barrier
s_waitcnt lgkmcnt(6)
Zva 96,192,0
s_mov_b32 m0, s71
Zva 104,192,4
s_mov_b32 s15, 78720
buffer_load_dwordx4 v18, s[24:27], s15 offen offset:0 lds
Zva 112,192,8
VC 64,64,65
VC 65,66,67
Zva 120,192,12
VC 66,68,69
VC 67,70,71
s_waitcnt lgkmcnt(4)
Zva 100,196,0
s_mov_b32 s15, 114560
buffer_load_dwordx4 v18, s[24:27], s15 offen offset:1024 lds
Zva 108,196,4
s_mov_b32 s67, 5120
buffer_store_dwordx4 v[64:67], v21, s[52:55], s67 offen sc1
Zva 116,196,8
TR 208,19,2048
VC 72,72,73
Zva 124,196,12
s_mov_b32 s15, 150400
buffer_load_dwordx4 v18, s[24:27], s15 offen offset:2048 lds
s_waitcnt lgkmcnt(3)
Mva 96,200,16
TR 210,19,10240
VC 73,74,75
Mva 104,200,20
VC 74,76,77
VC 75,78,79
Mva 112,200,24
s_mov_b32 s15, 186240
buffer_load_dwordx4 v18, s[24:27], s15 offen offset:3072 lds
Mva 120,200,28
TR 212,20,2048
s_waitcnt lgkmcnt(3)
Mva 100,204,16
buffer_store_dwordx2 v[186:187], v191, s[56:59], 0 offen offset:0 sc1
Mva 108,204,20
TR 214,20,10240
Mva 116,204,24
s_mov_b32 s67, 14336
buffer_store_dwordx4 v[72:75], v21, s[52:55], s67 offen sc1
Mva 124,204,28
VC 80,80,81
VC 81,82,83
s_waitcnt lgkmcnt(2)
Mva 96,208,32
TR 192,19,3072
VC 82,84,85
Mva 104,208,36
TR 194,19,11264
VC 83,86,87
Mva 112,208,40
s_mov_b32 s67, 23552
buffer_store_dwordx4 v[80:83], v21, s[52:55], s67 offen sc1
Mva 120,208,44
TR 196,20,3072
VC 88,88,89
s_waitcnt lgkmcnt(3)
Mva 100,212,32
TR 198,20,11264
VC 89,90,91
Mva 108,212,36
VC 90,92,93
VC 91,94,95
Mva 116,212,40
TR 200,19,16384
Mva 124,212,44
TR 202,19,24576
s_waitcnt lgkmcnt(4)
Mva 96,192,48
s_mov_b32 s67, 32768
buffer_store_dwordx4 v[88:91], v21, s[52:55], s67 offen sc1
Mva 104,192,52
TR 204,20,16384
Mva 112,192,56
TR 206,20,24576
Mva 120,192,60
TR 208,19,17408
s_waitcnt lgkmcnt(5)
Mva 100,196,48
TR 210,19,25600
Mva 108,196,52
Mva 116,196,56
TR 212,20,17408
Mva 124,196,60
TR 214,20,25600
s_waitcnt vmcnt(10)
s_barrier
s_waitcnt lgkmcnt(6)
Mva 96,200,64
s_mov_b32 m0, s68
Mva 104,200,68
s_mov_b32 s15, 226176
buffer_load_dwordx4 v18, s[24:27], s15 offen offset:0 lds
Mva 112,200,72
TR 192,19,18432
Mva 120,200,76
TR 194,19,26624
s_waitcnt lgkmcnt(6)
Mva 100,204,64
s_mov_b32 s15, 262016
buffer_load_dwordx4 v18, s[24:27], s15 offen offset:1024 lds
Mva 108,204,68
Mva 116,204,72
TR 196,20,18432
Mva 124,204,76
s_mov_b32 s15, 297856
buffer_load_dwordx4 v18, s[24:27], s15 offen offset:2048 lds
s_waitcnt lgkmcnt(5)
Mva 96,208,80
TR 198,20,26624
Mva 104,208,84
Mva 112,208,88
s_mov_b32 s15, 333696
buffer_load_dwordx4 v18, s[24:27], s15 offen offset:3072 lds
Mva 120,208,92
TR 200,19,19456
s_waitcnt lgkmcnt(5)
Mva 100,212,80
v_rcp_f32 v53, v53
Mva 108,212,84
VM 52,52,53
TR 202,19,27648
Mva 116,212,88
v_lshlrev_b32 v160, 16, v32
v_and_b32 v161, 0xffff0000, v32
Mva 124,212,92
TR 204,20,19456
v_lshlrev_b32 v56, 16, v42
s_waitcnt lgkmcnt(5)
Mva 96,192,96
TR 206,20,27648
v_and_b32 v42, 0xffff0000, v42
Mva 104,192,100
v_fma_f32 v32, v160, v53, -v52
v_fma_f32 v57, v161, v53, -v52
Mva 112,192,104
TR 208,19,32768
VF 160,32,56,160
Mva 120,192,108
TR 210,19,40960
VF 161,57,42,161
s_waitcnt lgkmcnt(6)
Mva 100,196,96
v_lshlrev_b32 v162, 16, v33
v_and_b32 v163, 0xffff0000, v33
Mva 108,196,100
v_lshlrev_b32 v56, 16, v43
v_and_b32 v43, 0xffff0000, v43
Mva 116,196,104
TR 212,20,32768
v_fma_f32 v33, v162, v53, -v52
Mva 124,196,108
TR 214,20,40960
v_fma_f32 v57, v163, v53, -v52
s_waitcnt lgkmcnt(6)
Mva 96,200,112
VF 162,33,56,162
VF 163,57,43,163
Mva 104,200,116
v_lshlrev_b32 v164, 16, v34
v_and_b32 v165, 0xffff0000, v34
Mva 112,200,120
TR 192,19,33792
v_lshlrev_b32 v56, 16, v44
Mva 120,200,124
TR 194,19,41984
v_and_b32 v44, 0xffff0000, v44
s_waitcnt lgkmcnt(6)
Mva 100,204,112
v_fma_f32 v34, v164, v53, -v52
v_fma_f32 v57, v165, v53, -v52
Mva 108,204,116
VF 164,34,56,164
VF 165,57,44,165
Mva 116,204,120
TR 196,20,33792
v_lshlrev_b32 v166, 16, v35
Mva 124,204,124
TR 198,20,41984
v_and_b32 v167, 0xffff0000, v35
s_waitcnt vmcnt(8)
s_barrier
s_waitcnt lgkmcnt(6)
Mva 96,208,128
s_mov_b32 m0, s69
v_lshlrev_b32 v56, 16, v45
Mva 104,208,132
s_mov_b32 s15, 5120
buffer_load_dwordx4 v18, s[24:27], s15 offen offset:0 lds
Mva 112,208,136
TR 200,19,34816
v_and_b32 v45, 0xffff0000, v45
Mva 120,208,140
TR 202,19,43008
v_fma_f32 v35, v166, v53, -v52
s_waitcnt lgkmcnt(6)
Mva 100,212,128
s_mov_b32 s15, 40960
buffer_load_dwordx4 v18, s[24:27], s15 offen offset:1024 lds
Mva 108,212,132
v_fma_f32 v57, v167, v53, -v52
VF 166,35,56,166
Mva 116,212,136
TR 204,20,34816
VF 167,57,45,167
Mva 124,212,140
s_mov_b32 s15, 76800
buffer_load_dwordx4 v18, s[24:27], s15 offen offset:2048 lds
s_waitcnt lgkmcnt(5)
Mva 96,192,144
TR 206,20,43008
v_lshlrev_b32 v168, 16, v36
Mva 104,192,148
v_and_b32 v169, 0xffff0000, v36
v_lshlrev_b32 v56, 16, v46
Mva 112,192,152
s_mov_b32 s15, 112640
buffer_load_dwordx4 v18, s[24:27], s15 offen offset:3072 lds
Mva 120,192,156
TR 208,19,35840
v_and_b32 v46, 0xffff0000, v46
s_waitcnt lgkmcnt(5)
Mva 100,196,144
TR 210,19,44032
v_fma_f32 v36, v168, v53, -v52
Mva 108,196,148
v_fma_f32 v57, v169, v53, -v52
VF 168,36,56,168
Mva 116,196,152
TR 212,20,35840
VF 169,57,46,169
Mva 124,196,156
TR 214,20,44032
v_lshlrev_b32 v170, 16, v37
s_waitcnt lgkmcnt(6)
Zva 128,200,0
v_and_b32 v171, 0xffff0000, v37
v_lshlrev_b32 v56, 16, v47
Zva 136,200,4
v_and_b32 v47, 0xffff0000, v47
v_fma_f32 v37, v170, v53, -v52
Zva 144,200,8
VC 96,96,97
VC 97,98,99
Zva 152,200,12
VC 98,100,101
VC 99,102,103
s_waitcnt lgkmcnt(4)
Zva 132,204,0
s_mov_b32 s67, 6144
buffer_store_dwordx4 v[96:99], v21, s[52:55], s67 offen sc1
Zva 140,204,4
TR 192,19,49152
VC 104,104,105
Zva 148,204,8
TR 194,19,57344
VC 105,106,107
Zva 156,204,12
VC 106,108,109
VC 107,110,111
s_waitcnt lgkmcnt(4)
Mva 128,208,16
TR 196,20,49152
v_fma_f32 v57, v171, v53, -v52
Mva 136,208,20
TR 198,20,57344
VF 170,37,56,170
Mva 144,208,24
s_mov_b32 s67, 15360
buffer_store_dwordx4 v[104:107], v21, s[52:55], s67 offen sc1
Mva 152,208,28
s_waitcnt vmcnt(10)
buffer_store_dwordx2 v[186:187], v191, s[56:59], 0 offen offset:128 sc1
s_waitcnt lgkmcnt(4)
Mva 132,212,16
VC 112,112,113
VC 113,114,115
Mva 140,212,20
TR 200,19,50176
VC 114,116,117
Mva 148,212,24
TR 202,19,58368
VC 115,118,119
Mva 156,212,28
s_mov_b32 s67, 24576
buffer_store_dwordx4 v[112:115], v21, s[52:55], s67 offen sc1
s_waitcnt vmcnt(8)
s_barrier
s_waitcnt lgkmcnt(4)
Mva 128,192,32
s_mov_b32 m0, s70
TR 204,20,50176
Mva 136,192,36
s_mov_b32 s15, 152576
buffer_load_dwordx4 v18, s[24:27], s15 offen offset:0 lds
Mva 144,192,40
VC 120,120,121
TR 206,20,58368
Mva 152,192,44
VC 121,122,123
VC 122,124,125
s_waitcnt lgkmcnt(4)
Mva 132,196,32
s_mov_b32 s15, 188416
buffer_load_dwordx4 v18, s[24:27], s15 offen offset:1024 lds
Mva 140,196,36
VC 123,126,127
TR 208,19,51200
Mva 148,196,40
TR 210,19,59392
VF 171,57,47,171
Mva 156,196,44
s_mov_b32 s15, 224256
buffer_load_dwordx4 v18, s[24:27], s15 offen offset:2048 lds
s_waitcnt lgkmcnt(4)
Mva 128,200,48
s_mov_b32 s67, 33792
buffer_store_dwordx4 v[120:123], v21, s[52:55], s67 offen sc1
Mva 136,200,52
TR 212,20,51200
v_lshlrev_b32 v172, 16, v38
Mva 144,200,56
s_mov_b32 s15, 260096
buffer_load_dwordx4 v18, s[24:27], s15 offen offset:3072 lds
Mva 152,200,60
TR 214,20,59392
v_and_b32 v173, 0xffff0000, v38
s_waitcnt lgkmcnt(4)
Mva 132,204,48
TR 192,19,52224
v_lshlrev_b32 v56, 16, v48
Mva 140,204,52
TR 194,19,60416
v_and_b32 v48, 0xffff0000, v48
Mva 148,204,56
TR 196,20,52224
v_fma_f32 v38, v172, v53, -v52
Mva 156,204,60
TR 198,20,60416
v_fma_f32 v57, v173, v53, -v52
s_waitcnt lgkmcnt(6)
Mva 128,208,64
VF 172,38,56,172
VF 173,57,48,173
Mva 136,208,68
v_lshlrev_b32 v174, 16, v39
v_and_b32 v175, 0xffff0000, v39
Mva 144,208,72
TR 200,19,0
v_lshlrev_b32 v56, 16, v49
Mva 152,208,76
TR 202,19,8192
v_and_b32 v49, 0xffff0000, v49
s_waitcnt lgkmcnt(6)
Mva 132,212,64
v_fma_f32 v39, v174, v53, -v52
v_fma_f32 v57, v175, v53, -v52
Mva 140,212,68
VF 174,39,56,174
VF 175,57,49,175
Mva 148,212,72
TR 204,20,0
v_lshlrev_b32 v176, 16, v40
Mva 156,212,76
TR 206,20,8192
v_and_b32 v177, 0xffff0000, v40
s_waitcnt lgkmcnt(6)
Mva 128,192,80
v_lshlrev_b32 v56, 16, v50
v_and_b32 v50, 0xffff0000, v50
Mva 136,192,84
v_fma_f32 v40, v176, v53, -v52
v_fma_f32 v57, v177, v53, -v52
Mva 144,192,88
TR 208,19,1024
VF 176,40,56,176
Mva 152,192,92
TR 210,19,9216
VF 177,57,50,177
s_waitcnt lgkmcnt(6)
Mva 132,196,80
v_lshlrev_b32 v178, 16, v41
v_and_b32 v179, 0xffff0000, v41
Mva 140,196,84
v_lshlrev_b32 v56, 16, v51
v_and_b32 v51, 0xffff0000, v51
Mva 148,196,88
TR 212,20,1024
v_fma_f32 v41, v178, v53, -v52
Mva 156,196,92
TR 214,20,9216
v_fma_f32 v57, v179, v53, -v52
s_waitcnt vmcnt(9)
s_barrier
s_waitcnt lgkmcnt(6)
Mva 128,200,96
s_mov_b32 m0, s71
VF 178,41,56,178
Mva 136,200,100
s_mov_b32 s15, 300032
buffer_load_dwordx4 v18, s[24:27], s15 offen offset:0 lds
Mva 144,200,104
TR 192,19,2048
VF 179,57,51,179
Mva 152,200,108
TR 194,19,10240
v_and_b32 v56, 1, v0
s_waitcnt lgkmcnt(6)
Mva 132,204,96
s_mov_b32 s15, 335872
buffer_load_dwordx4 v18, s[24:27], s15 offen offset:1024 lds
Mva 140,204,100
v_lshlrev_b32 v56, 4, v56
v_mov_b32 v57, 0x3f80
Mva 148,204,104
TR 196,20,2048
v_lshlrev_b32 v57, v56, v57
Mva 156,204,108
TR 198,20,10240
v_bfe_u32 v56, v0, 1, 3
s_waitcnt lgkmcnt(6)
Mva 128,208,112
v_lshrrev_b32 v32, 2, v0
v_and_b32 v32, 4, v32
Mva 136,208,116
v_xor_b32 v56, v56, v32
v_cmp_eq_u32 vcc, 0, v56
Mva 144,208,120
TR 200,19,3072
v_cndmask_b32 v32, 0, v57, vcc
Mva 152,208,124
TR 202,19,11264
v_cmp_eq_u32 vcc, 1, v56
s_waitcnt lgkmcnt(6)
Mva 132,212,112
v_cndmask_b32 v33, 0, v57, vcc
v_cmp_eq_u32 vcc, 2, v56
Mva 140,212,116
s_nop 0
v_cndmask_b32 v34, 0, v57, vcc
v_cmp_eq_u32 vcc, 3, v56
Mva 148,212,120
TR 204,20,3072
v_cndmask_b32 v35, 0, v57, vcc
Mva 156,212,124
TR 206,20,11264
s_waitcnt lgkmcnt(6)
Mva 128,192,128
Mva 136,192,132
Mva 144,192,136
TR 208,19,16384
Mva 152,192,140
TR 210,19,24576
s_waitcnt lgkmcnt(6)
Mva 132,196,128
Mva 140,196,132
Mva 148,196,136
TR 212,20,16384
Mva 156,196,140
TR 214,20,24576
s_waitcnt lgkmcnt(6)
Mva 128,200,144
Mva 136,200,148
Mva 144,200,152
TR 192,19,17408
Mva 152,200,156
TR 194,19,25600
s_waitcnt lgkmcnt(6)
Mva 132,204,144
Mva 140,204,148
Mva 148,204,152
TR 196,20,17408
Mva 156,204,156
TR 198,20,25600
s_waitcnt vmcnt(2)
s_barrier
s_waitcnt lgkmcnt(6)
Zva 224,208,0
Zva 232,208,4
Zva 240,208,8
VC 128,128,129
VC 129,130,131
Zva 248,208,12
VC 130,132,133
VC 131,134,135
s_waitcnt lgkmcnt(4)
Zva 228,212,0
s_mov_b32 s67, 7168
buffer_store_dwordx4 v[128:131], v21, s[52:55], s67 offen sc1
Zva 236,212,4
TR 200,19,18432
VC 136,136,137
Zva 244,212,8
TR 202,19,26624
VC 137,138,139
Zva 252,212,12
VC 138,140,141
VC 139,142,143
s_waitcnt lgkmcnt(4)
Mva 224,192,16
TR 204,20,18432
Mva 232,192,20
TR 206,20,26624
Mva 240,192,24
s_mov_b32 s67, 16384
buffer_store_dwordx4 v[136:139], v21, s[52:55], s67 offen sc1
Mva 248,192,28
VC 144,144,145
VC 145,146,147
s_waitcnt lgkmcnt(4)
Mva 228,196,16
TR 208,19,19456
VC 146,148,149
Mva 236,196,20
TR 210,19,27648
VC 147,150,151
Mva 244,196,24
s_mov_b32 s67, 25600
buffer_store_dwordx4 v[144:147], v21, s[52:55], s67 offen sc1
Mva 252,196,28
TR 212,20,19456
VC 152,152,153
s_waitcnt lgkmcnt(5)
Mva 224,200,32
TR 214,20,27648
VC 153,154,155
Mva 232,200,36
VC 154,156,157
VC 155,158,159
Mva 240,200,40
TR 192,19,32768
Mva 248,200,44
TR 194,19,40960
s_waitcnt lgkmcnt(6)
Mva 228,204,32
s_mov_b32 s67, 34816
buffer_store_dwordx4 v[152:155], v21, s[52:55], s67 offen sc1
Mva 236,204,36
Mva 244,204,40
TR 196,20,32768
Mva 252,204,44
TR 198,20,40960
s_waitcnt lgkmcnt(6)
Mva 224,208,48
Mva 232,208,52
Mva 240,208,56
TR 200,19,33792
Mva 248,208,60
TR 202,19,41984
s_waitcnt lgkmcnt(6)
Mva 228,212,48
Mva 236,212,52
Mva 244,212,56
TR 204,20,33792
Mva 252,212,60
TR 206,20,41984
s_waitcnt vmcnt(4)
s_barrier
s_waitcnt lgkmcnt(6)
Mva 224,192,64
Mva 232,192,68
Mva 240,192,72
TR 208,19,34816
Mva 248,192,76
TR 210,19,43008
s_waitcnt lgkmcnt(6)
Mva 228,196,64
Mva 236,196,68
Mva 244,196,72
TR 212,20,34816
Mva 252,196,76
TR 214,20,43008
s_waitcnt lgkmcnt(6)
Mva 224,200,80
buffer_load_dwordx2 v[54:55], v190, s[56:59], 0 offen sc1
Mva 232,200,84
)"
R"(Mva 240,200,88
TR 192,19,35840
Mva 248,200,92
TR 194,19,44032
s_waitcnt lgkmcnt(6)
Mva 228,204,80
Mva 236,204,84
Mva 244,204,88
TR 196,20,35840
Mva 252,204,92
TR 198,20,44032
s_waitcnt lgkmcnt(6)
Mva 224,208,96
Mva 232,208,100
Mva 240,208,104
TR 200,19,49152
Mva 248,208,108
TR 202,19,57344
s_waitcnt lgkmcnt(6)
Mva 228,212,96
Mva 236,212,100
Mva 244,212,104
TR 204,20,49152
Mva 252,212,108
TR 206,20,57344
s_waitcnt lgkmcnt(6)
Mva 224,192,112
Mva 232,192,116
Mva 240,192,120
TR 208,19,50176
Mva 248,192,124
TR 210,19,58368
s_waitcnt lgkmcnt(6)
Mva 228,196,112
Mva 236,196,116
Mva 244,196,120
TR 212,20,50176
Mva 252,196,124
TR 214,20,58368
s_waitcnt lgkmcnt(6)
Mva 224,200,128
Mva 232,200,132
Mva 240,200,136
Mva 248,200,140
s_waitcnt lgkmcnt(4)
Mva 228,204,128
Mva 236,204,132
Mva 244,204,136
Mva 252,204,140
s_waitcnt lgkmcnt(2)
Mva 224,208,144
Mva 232,208,148
Mva 240,208,152
Mva 248,208,156
s_waitcnt lgkmcnt(0)
Mva 228,212,144
Mva 236,212,148
Mva 244,212,152
Mva 252,212,156
VC 224,224,225
VC 225,226,227
s_nop 2
VC 226,228,229
VC 227,230,231
s_mov_b32 s67, 8192
buffer_store_dwordx4 v[224:227], v21, s[52:55], s67 offen sc1
VC 232,232,233
VC 233,234,235
VC 234,236,237
VC 235,238,239
s_mov_b32 s67, 17408
buffer_store_dwordx4 v[232:235], v21, s[52:55], s67 offen sc1
VC 240,240,241
VC 241,242,243
VC 242,244,245
VC 243,246,247
s_mov_b32 s67, 26624
buffer_store_dwordx4 v[240:243], v21, s[52:55], s67 offen sc1
VC 248,248,249
VC 249,250,251
VC 250,252,253
VC 251,254,255
s_mov_b32 s67, 35840
buffer_store_dwordx4 v[248:251], v21, s[52:55], s67 offen sc1
s_nop 0
s_waitcnt vmcnt(5)
buffer_store_dwordx2 v[186:187], v191, s[56:59], 0 offen offset:256 sc1
s_waitcnt vmcnt(1)
buffer_store_dwordx2 v[186:187], v191, s[56:59], 0 offen offset:384 sc1
LCB50:
v_cmp_eq_u32 vcc, s100, v54
v_cmp_eq_u32 s[76:77], s100, v55
s_nop 0
s_and_b64 s[76:77], s[76:77], vcc
s_andn2_b32 s76, 0xffff, s76
s_cbranch_scc1 LRB50
LGB50:
buffer_load_dwordx2 v[54:55], v190, s[56:59], 0 offen sc1
s_mov_b32 s67, 0
buffer_load_dwordx4 a[192:195], v188, s[52:55], s67 offen sc1
s_mov_b32 s67, 294912
buffer_load_dwordx4 a[196:199], v188, s[52:55], s67 offen sc1
s_mov_b32 s67, 589824
buffer_load_dwordx4 a[200:203], v188, s[52:55], s67 offen sc1
s_mov_b32 s67, 884736
buffer_load_dwordx4 a[204:207], v188, s[52:55], s67 offen sc1
s_mov_b32 s67, 1179648
buffer_load_dwordx4 a[208:211], v188, s[52:55], s67 offen sc1
s_mov_b32 s67, 1474560
buffer_load_dwordx4 a[212:215], v188, s[52:55], s67 offen sc1
s_mov_b32 s67, 1769472
buffer_load_dwordx4 a[216:219], v188, s[52:55], s67 offen sc1
s_mov_b32 s67, 2064384
buffer_load_dwordx4 a[220:223], v188, s[52:55], s67 offen sc1
s_mov_b32 s67, 2048
buffer_load_dwordx4 a[224:227], v188, s[52:55], s67 offen sc1
s_mov_b32 s67, 296960
buffer_load_dwordx4 a[228:231], v188, s[52:55], s67 offen sc1
s_mov_b32 s67, 591872
buffer_load_dwordx4 a[232:235], v188, s[52:55], s67 offen sc1
s_mov_b32 s67, 886784
buffer_load_dwordx4 a[236:239], v188, s[52:55], s67 offen sc1
s_mov_b32 s67, 1181696
buffer_load_dwordx4 a[240:243], v188, s[52:55], s67 offen sc1
s_mov_b32 s67, 1476608
buffer_load_dwordx4 a[244:247], v188, s[52:55], s67 offen sc1
s_mov_b32 s67, 1771520
buffer_load_dwordx4 a[248:251], v188, s[52:55], s67 offen sc1
s_mov_b32 s67, 2066432
buffer_load_dwordx4 a[252:255], v188, s[52:55], s67 offen sc1
LCB51:
s_waitcnt vmcnt(16)
v_cmp_eq_u32 vcc, s100, v54
v_cmp_eq_u32 s[76:77], s100, v55
s_nop 0
s_and_b64 s[76:77], s[76:77], vcc
s_andn2_b32 s76, 0xffff0000, s76
s_cbranch_scc1 LRB51
LGB51:
buffer_load_dwordx2 v[54:55], v190, s[56:59], 0 offen sc1
s_mov_b32 s67, 4096
buffer_load_dwordx4 a[0:3], v188, s[52:55], s67 offen sc1
s_mov_b32 s67, 299008
buffer_load_dwordx4 a[4:7], v188, s[52:55], s67 offen sc1
s_mov_b32 s67, 593920
buffer_load_dwordx4 a[8:11], v188, s[52:55], s67 offen sc1
s_mov_b32 s67, 888832
buffer_load_dwordx4 a[12:15], v188, s[52:55], s67 offen sc1
s_mov_b32 s67, 1183744
buffer_load_dwordx4 a[16:19], v188, s[52:55], s67 offen sc1
s_mov_b32 s67, 1478656
buffer_load_dwordx4 a[20:23], v188, s[52:55], s67 offen sc1
s_mov_b32 s67, 1773568
buffer_load_dwordx4 a[24:27], v188, s[52:55], s67 offen sc1
s_mov_b32 s67, 2068480
buffer_load_dwordx4 a[28:31], v188, s[52:55], s67 offen sc1
s_waitcnt vmcnt(24)
Mva 160,32,192
s_waitcnt vmcnt(16)
Mva 164,32,224
Mva 160,32,196
s_waitcnt vmcnt(15)
Mva 164,32,228
Mva 160,32,200
s_waitcnt vmcnt(14)
Mva 164,32,232
Mva 160,32,204
s_waitcnt vmcnt(13)
Mva 164,32,236
Mva 160,32,208
s_waitcnt vmcnt(12)
Mva 164,32,240
Mva 160,32,212
s_waitcnt vmcnt(11)
Mva 164,32,244
Mva 160,32,216
s_waitcnt vmcnt(10)
Mva 164,32,248
Mva 160,32,220
s_waitcnt vmcnt(9)
Mva 164,32,252
s_nop 5
buffer_store_dwordx4 v[160:163], v189, s[16:19], 0 offen offset:0 sc1
s_nop 0
buffer_store_dwordx4 v[164:167], v189, s[16:19], 0 offen offset:512 sc1
LCB52:
s_waitcnt vmcnt(10)
v_cmp_eq_u32 vcc, s100, v54
v_cmp_eq_u32 s[76:77], s100, v55
s_nop 0
s_and_b64 s[76:77], s[76:77], vcc
s_andn2_b32 s76, 0xffff, s77
s_cbranch_scc1 LRB52
LGB52:
buffer_load_dwordx2 v[54:55], v190, s[56:59], 0 offen sc1
s_mov_b32 s67, 6144
buffer_load_dwordx4 a[32:35], v188, s[52:55], s67 offen sc1
s_mov_b32 s67, 301056
buffer_load_dwordx4 a[36:39], v188, s[52:55], s67 offen sc1
s_mov_b32 s67, 595968
buffer_load_dwordx4 a[40:43], v188, s[52:55], s67 offen sc1
s_mov_b32 s67, 890880
buffer_load_dwordx4 a[44:47], v188, s[52:55], s67 offen sc1
s_mov_b32 s67, 1185792
buffer_load_dwordx4 a[48:51], v188, s[52:55], s67 offen sc1
s_mov_b32 s67, 1480704
buffer_load_dwordx4 a[52:55], v188, s[52:55], s67 offen sc1
s_mov_b32 s67, 1775616
buffer_load_dwordx4 a[56:59], v188, s[52:55], s67 offen sc1
s_mov_b32 s67, 2070528
buffer_load_dwordx4 a[60:63], v188, s[52:55], s67 offen sc1
s_waitcnt vmcnt(18)
Mva 168,32,0
s_waitcnt vmcnt(17)
Mva 168,32,4
s_waitcnt vmcnt(16)
Mva 168,32,8
s_waitcnt vmcnt(15)
Mva 168,32,12
s_waitcnt vmcnt(14)
Mva 168,32,16
s_waitcnt vmcnt(13)
Mva 168,32,20
s_waitcnt vmcnt(12)
Mva 168,32,24
s_waitcnt vmcnt(11)
Mva 168,32,28
s_nop 7
buffer_store_dwordx4 v[168:171], v189, s[16:19], 0 offen offset:1024 sc1
LCB53:
s_waitcnt vmcnt(9)
v_cmp_eq_u32 vcc, s100, v54
v_cmp_eq_u32 s[76:77], s100, v55
s_nop 0
s_and_b64 s[76:77], s[76:77], vcc
s_andn2_b32 s76, 0xffff0000, s77
s_cbranch_scc1 LRB53
LGB53:
s_cmp_lg_u32 s65, 0
s_cselect_b32 s54, 0, s54
s_mov_b32 s67, 8192
buffer_load_dwordx4 a[64:67], v188, s[52:55], s67 offen sc1
s_mov_b32 s67, 303104
buffer_load_dwordx4 a[68:71], v188, s[52:55], s67 offen sc1
s_mov_b32 s67, 598016
buffer_load_dwordx4 a[72:75], v188, s[52:55], s67 offen sc1
s_mov_b32 s67, 892928
buffer_load_dwordx4 a[76:79], v188, s[52:55], s67 offen sc1
s_mov_b32 s67, 1187840
buffer_load_dwordx4 a[80:83], v188, s[52:55], s67 offen sc1
s_mov_b32 s67, 1482752
buffer_load_dwordx4 a[84:87], v188, s[52:55], s67 offen sc1
s_mov_b32 s67, 1777664
buffer_load_dwordx4 a[88:91], v188, s[52:55], s67 offen sc1
s_mov_b32 s67, 2072576
buffer_load_dwordx4 a[92:95], v188, s[52:55], s67 offen sc1
s_waitcnt vmcnt(16)
Mva 172,32,32
s_waitcnt vmcnt(15)
Mva 172,32,36
s_waitcnt vmcnt(14)
Mva 172,32,40
s_waitcnt vmcnt(13)
Mva 172,32,44
s_waitcnt vmcnt(12)
Mva 172,32,48
s_waitcnt vmcnt(11)
Mva 172,32,52
s_waitcnt vmcnt(10)
Mva 172,32,56
s_waitcnt vmcnt(9)
Mva 172,32,60
s_nop 7
buffer_store_dwordx4 v[172:175], v189, s[16:19], 0 offen offset:1536 sc1
s_waitcnt vmcnt(8)
Mva 176,32,64
s_waitcnt vmcnt(7)
Mva 176,32,68
s_waitcnt vmcnt(6)
Mva 176,32,72
s_waitcnt vmcnt(5)
Mva 176,32,76
s_waitcnt vmcnt(4)
Mva 176,32,80
s_waitcnt vmcnt(3)
Mva 176,32,84
s_waitcnt vmcnt(2)
Mva 176,32,88
s_waitcnt vmcnt(1)
Mva 176,32,92
s_cmp_lg_u32 s65, 0
s_cselect_b32 s18, 0, s18
s_nop 5
buffer_store_dwordx4 v[176:179], v189, s[16:19], 0 offen offset:2048 sc1
s_lshl_b32 s90, s14, 7
s_add_u32 s90, s90, 196608
s_add_u32 s91, s13, s11
s_cmp_eq_u32 s91, 0
s_cselect_b32 s90, s90, 0x80000000
v_mov_b32 v1, s90
buffer_store_dword v186, v1, s[56:59], 0 offen sc1
s_endpgm
LRB50:
s_sleep 8
buffer_load_dwordx2 v[54:55], v190, s[56:59], 0 offen sc1
s_waitcnt vmcnt(0)
s_sub_u32 s92, s92, 1
s_cbranch_scc1 LGB50
s_branch LCB50
LRB51:
s_sleep 4
buffer_load_dwordx2 v[54:55], v190, s[56:59], 0 offen sc1
s_waitcnt vmcnt(0)
s_sub_u32 s92, s92, 1
s_cbranch_scc1 LGB51
s_branch LCB51
LRB52:
s_sleep 2
buffer_load_dwordx2 v[54:55], v190, s[56:59], 0 offen sc1
s_waitcnt vmcnt(0)
s_sub_u32 s92, s92, 1
s_cbranch_scc1 LGB52
s_branch LCB52
LRB53:
s_sleep 1
buffer_load_dwordx2 v[54:55], v190, s[56:59], 0 offen sc1
s_waitcnt vmcnt(0)
s_sub_u32 s92, s92, 1
s_cbranch_scc1 LGB53
s_branch LCB53
LENDB5:
PB6:
v_and_b32 v0, 63, v0
s_mov_b32 m0, 161792
s_and_b32 s72, s14, 3
s_lshl_b32 s72, s72, 2
s_add_u32 s72, s72, s11
s_mov_b32 s7, 0x20000
s_getpc_b64 s[4:5]
LPCB6:
s_mov_b32 s6, LENDB6-LPCB6
s_lshl_b32 s73, s72, 13
v_lshl_add_u32 v47, v0, 7, s73
buffer_load_dword v47, s[4:7], 0 offen lds
v_and_b32 v32, 15, v0
v_lshrrev_b32 v33, 4, v0
v_lshrrev_b32 v34, 2, v32
v_and_b32 v35, 3, v0
s_and_b32 s64, s11, 1
s_lshr_b32 s65, s11, 1
s_lshl_b32 s66, s64, 6
s_lshl_b32 s67, s65, 3
s_add_u32 s66, s66, s67
v_lshl_add_u32 v36, v33, 4, v34
v_add_u32 v36, s66, v36
s_lshl_b32 s67, s14, 7
v_add_u32 v37, s67, v36
v_mul_u32_u24 v38, 1152, v37
v_lshl_add_u32 v38, v35, 4, v38
v_add_u32 v39, 4608, v38
s_waitcnt lgkmcnt(0)
s_mov_b32 s48, s76
s_mov_b32 s49, s77
s_mov_b32 s50, 2359296
s_mov_b32 s51, 0x20000
buffer_load_dwordx4 a[0:3], v38, s[48:51], 0 offen offset:0
buffer_load_dwordx4 a[4:7], v39, s[48:51], 0 offen offset:0
buffer_load_dwordx4 a[16:19], v38, s[48:51], 0 offen offset:64
buffer_load_dwordx4 a[20:23], v39, s[48:51], 0 offen offset:64
buffer_load_dwordx4 a[32:35], v38, s[48:51], 0 offen offset:128
buffer_load_dwordx4 a[36:39], v39, s[48:51], 0 offen offset:128
buffer_load_dwordx4 a[48:51], v38, s[48:51], 0 offen offset:192
buffer_load_dwordx4 a[52:55], v39, s[48:51], 0 offen offset:192
buffer_load_dwordx4 a[64:67], v38, s[48:51], 0 offen offset:256
buffer_load_dwordx4 a[68:71], v39, s[48:51], 0 offen offset:256
buffer_load_dwordx4 a[80:83], v38, s[48:51], 0 offen offset:320
buffer_load_dwordx4 a[84:87], v39, s[48:51], 0 offen offset:320
s_mul_i32 s74, s13, 5
s_sub_u32 s74, s74, 9
s_lshl_b32 s75, s74, 7
s_add_u32 s16, s92, s75
s_addc_u32 s17, s93, 0
s_sub_u32 s16, s16, 0x1000
s_subb_u32 s17, s17, 0
s_sub_u32 s18, 5312512, s75
s_mov_b32 s19, 0x20000
s_add_u32 s20, s94, s75
s_addc_u32 s21, s95, 0
s_sub_u32 s20, s20, 0x1000
s_subb_u32 s21, s21, 0
s_sub_u32 s22, 5312512, s75
s_mov_b32 s23, 0x20000
s_mul_i32 s75, s74, 73728
s_add_u32 s24, s96, s75
s_addc_u32 s25, s97, 0
s_sub_u32 s24, s24, 0x1000
s_subb_u32 s25, s25, 0
s_sub_u32 s26, 5312512, s75
s_mov_b32 s27, 0x20000
v_lshrrev_b32 v40, 3, v0
v_and_b32 v41, 7, v0
v_and_b32 v42, 1, v0
v_lshlrev_b32 v42, 4, v42
v_lshrrev_b32 v43, 1, v41
s_lshl_b32 s67, s11, 3
v_add_u32 v44, s67, v40
s_lshl_b32 s68, s64, 1
v_and_b32 v45, 1, v33
v_or_b32 v46, s68, v45
v_xor_b32 v46, v46, v43
v_lshl_add_u32 v46, v46, 5, v42
v_mul_u32_u24 v1, 9216, v44
v_add_u32 v1, v1, v46
v_xor_b32 v46, v33, v43
v_lshl_add_u32 v46, v46, 5, v42
v_mul_u32_u24 v18, 1152, v44
v_add_u32 v18, v18, v46
v_add_u32 v44, s67, v44
v_xor_b32 v46, v33, v41
v_mul_u32_u24 v14, 1152, v44
v_lshl_add_u32 v14, v46, 4, v14
v_xor_b32 v15, 64, v14
v_add_u32 v15, 9216, v15
s_lshl_b32 s68, s11, 12
s_add_u32 s69, s68, 0x4000
s_add_u32 s70, s68, 0x8000
s_add_u32 s71, s68, 0xc000
v_lshrrev_b32 v46, 1, v34
v_lshl_or_b32 v46, v45, 1, v46
s_lshl_b32 s67, s65, 1
v_xor_b32 v46, s67, v46
v_lshlrev_b32 v46, 5, v46
v_lshl_add_u32 v46, v35, 3, v46
v_lshl_add_u32 v46, v34, 7, v46
v_lshl_add_u32 v2, v33, 12, v46
v_xor_b32 v3, 32, v2
v_lshl_add_u32 v19, v45, 9, v46
v_lshrrev_b32 v47, 1, v33
v_lshl_add_u32 v19, v47, 12, v19
v_xor_b32 v20, 32, v19
v_lshrrev_b32 v46, 1, v32
v_xor_b32 v46, v46, v33
v_lshlrev_b32 v46, 4, v46
v_and_b32 v47, 7, v32
v_lshl_add_u32 v46, v47, 7, v46
v_lshrrev_b32 v47, 3, v32
v_lshl_add_u32 v46, v47, 11, v46
s_lshl_b32 s67, s65, 13
v_add_u32 v16, s67, v46
v_xor_b32 v17, 64, v16
s_lshl_b32 s67, s64, 13
s_add_u32 s67, s67, 0x10000
v_lshl_add_u32 v4, v0, 4, s67
s_lshl_b32 s67, s65, 12
v_add_u32 v5, s67, v4
s_lshl_b32 s67, s64, 13
s_add_u32 s67, s67, 0x10000
v_and_b32 v46, 2, v33
v_lshl_or_b32 v46, v33, 2, v46
v_xor_b32 v46, v46, v0
v_lshl_add_u32 v157, v46, 4, s67
v_and_b32 v46, 2, v35
v_lshl_or_b32 v46, v35, 2, v46
v_lshrrev_b32 v47, 2, v0
v_xor_b32 v46, v46, v47
v_lshl_add_u32 v46, v35, 4, v46
s_lshl_b32 s72, s65, 11
s_add_u32 s67, s67, s72
v_lshl_add_u32 v156, v46, 4, s67
s_lshl_b32 s67, s14, 4
s_add_u32 s67, s67, s13
s_lshl_b32 s67, s67, 2
s_add_u32 s67, s67, s11
s_mul_i32 s67, s67, 36864
v_lshl_add_u32 v21, v0, 4, s67
s_add_u32 s52, s98, s67
s_addc_u32 s53, s99, 0x800000
s_mov_b32 s54, 64
s_mov_b32 s55, 0x20000
s_add_u32 s56, s98, 42991616
s_addc_u32 s57, s99, 0
s_mov_b32 s58, 262144
s_mov_b32 s59, 0x20000
s_lshl_b32 s67, s10, 10
s_add_u32 s67, s67, 41943040
s_add_u32 s60, s98, s67
s_addc_u32 s61, s99, 0
s_mov_b32 s62, 1024
s_mov_b32 s63, 0x20000
s_lshl_b32 s67, s14, 7
s_add_u32 s67, s67, 196608
s_load_dword s100, s[56:57], s67
s_lshl_b32 s67, s13, 7
s_add_u32 s67, s67, 200704
s_load_dword s101, s[56:57], s67
v_mov_b32 v96, 0x3f803f80
v_mov_b32 v97, 0x3f803f80
v_mov_b32 v98, 0x3f803f80
v_mov_b32 v99, 0x3f803f80
v_lshlrev_b32 v46, v42, v96
v_and_b32 v47, 0xffff, v96
v_cmp_eq_u32 vcc, 0, v42
s_nop 1
v_cndmask_b32 v46, v46, v47, vcc
v_cmp_eq_u32 vcc, v33, v34
s_nop 1
v_cndmask_b32 v46, 0, v46, vcc
v_cmp_gt_u32 vcc, 2, v35
v_mov_b32 v100, 0
v_mov_b32 v101, 0
v_cndmask_b32 v102, 0, v46, vcc
v_cndmask_b32 v103, v46, 0, vcc
v_mov_b32 v104, 0
v_mov_b32 v105, 0
v_cmp_eq_u32 s[72:73], 1, v35
v_cmp_eq_u32 s[74:75], 2, v35
v_cmp_eq_u32 s[76:77], 3, v35
s_mov_b32 m0, 161792
s_lshl_b32 s90, s14, 2
s_add_u32 s90, s90, s11
s_mov_b32 s7, 0x20000
s_add_u32 s91, s90, 108
s_mul_i32 s92, s91, 3641
s_lshr_b32 s92, s92, 15
s_mul_i32 s93, s92, 9
s_sub_u32 s93, s91, s93
s_bitcmp1_b32 s92, 0
s_cselect_b32 s4, s20, s16
s_cselect_b32 s5, s21, s17
s_cselect_b32 s32, s44, s40
s_cselect_b32 s33, s45, s41
s_lshr_b32 s92, s92, 1
s_lshl_b32 s92, s92, 7
s_mul_i32 s34, s93, 589824
s_add_u32 s92, s92, s34
s_or_b32 s5, s5, 603979776
s_or_b32 s33, s33, 75497472
s_mul_i32 s34, s93, 73728
s_sub_u32 s93, s91, 108
s_lshl_b32 s93, s93, 13
s_or_b32 s35, s25, 0x800000
s_cmp_ge_u32 s91, 108
s_cselect_b32 s4, s24, s4
s_cselect_b32 s5, s35, s5
s_cselect_b32 s92, s93, s92
s_add_u32 s92, s92, 0x1000
s_cmp_ge_u32 s91, 162
s_cselect_b32 s6, 0, 64
s_cselect_b32 s92, 0, s92
buffer_load_dwordx4 a[96:99], v38, s[48:51], 0 offen offset:384
buffer_load_dwordx4 a[100:103], v39, s[48:51], 0 offen offset:384
s_waitcnt vmcnt(13)
v_mfma_f32_4x4x4_16b_bf16 v[108:111], v[96:97], a[0:1], 0
v_mfma_f32_4x4x4_16b_bf16 v[116:119], a[0:1], a[0:1], 0
v_mfma_f32_4x4x4_16b_bf16 v[108:111], v[96:97], a[2:3], v[108:111]
v_mfma_f32_4x4x4_16b_bf16 v[116:119], a[2:3], a[2:3], v[116:119]
s_waitcnt vmcnt(12)
v_mfma_f32_4x4x4_16b_bf16 v[112:115], v[96:97], a[4:5], 0
v_mfma_f32_4x4x4_16b_bf16 v[120:123], a[4:5], a[4:5], 0
v_mfma_f32_4x4x4_16b_bf16 v[112:115], v[96:97], a[6:7], v[112:115]
v_mfma_f32_4x4x4_16b_bf16 v[120:123], a[6:7], a[6:7], v[120:123]
buffer_load_dwordx4 a[112:115], v38, s[48:51], 0 offen offset:448
buffer_load_dwordx4 a[116:119], v39, s[48:51], 0 offen offset:448
s_waitcnt vmcnt(13)
v_mfma_f32_4x4x4_16b_bf16 v[108:111], v[96:97], a[16:17], v[108:111]
v_mfma_f32_4x4x4_16b_bf16 v[116:119], a[16:17], a[16:17], v[116:119]
v_mfma_f32_4x4x4_16b_bf16 v[108:111], v[96:97], a[18:19], v[108:111]
v_mfma_f32_4x4x4_16b_bf16 v[116:119], a[18:19], a[18:19], v[116:119]
s_waitcnt vmcnt(12)
v_mfma_f32_4x4x4_16b_bf16 v[112:115], v[96:97], a[20:21], v[112:115]
v_mfma_f32_4x4x4_16b_bf16 v[120:123], a[20:21], a[20:21], v[120:123]
v_mfma_f32_4x4x4_16b_bf16 v[112:115], v[96:97], a[22:23], v[112:115]
v_mfma_f32_4x4x4_16b_bf16 v[120:123], a[22:23], a[22:23], v[120:123]
buffer_load_dwordx4 a[128:131], v38, s[48:51], 0 offen offset:512
buffer_load_dwordx4 a[132:135], v39, s[48:51], 0 offen offset:512
s_waitcnt vmcnt(13)
v_mfma_f32_4x4x4_16b_bf16 v[108:111], v[96:97], a[32:33], v[108:111]
v_mfma_f32_4x4x4_16b_bf16 v[116:119], a[32:33], a[32:33], v[116:119]
v_mfma_f32_4x4x4_16b_bf16 v[108:111], v[96:97], a[34:35], v[108:111]
v_mfma_f32_4x4x4_16b_bf16 v[116:119], a[34:35], a[34:35], v[116:119]
s_waitcnt vmcnt(12)
v_mfma_f32_4x4x4_16b_bf16 v[112:115], v[96:97], a[36:37], v[112:115]
v_mfma_f32_4x4x4_16b_bf16 v[120:123], a[36:37], a[36:37], v[120:123]
v_mfma_f32_4x4x4_16b_bf16 v[112:115], v[96:97], a[38:39], v[112:115]
v_mfma_f32_4x4x4_16b_bf16 v[120:123], a[38:39], a[38:39], v[120:123]
buffer_load_dwordx4 a[144:147], v38, s[48:51], 0 offen offset:576
buffer_load_dwordx4 a[148:151], v39, s[48:51], 0 offen offset:576
s_waitcnt vmcnt(13)
v_mfma_f32_4x4x4_16b_bf16 v[108:111], v[96:97], a[48:49], v[108:111]
v_mfma_f32_4x4x4_16b_bf16 v[116:119], a[48:49], a[48:49], v[116:119]
v_mfma_f32_4x4x4_16b_bf16 v[108:111], v[96:97], a[50:51], v[108:111]
v_mfma_f32_4x4x4_16b_bf16 v[116:119], a[50:51], a[50:51], v[116:119]
s_waitcnt vmcnt(12)
v_mfma_f32_4x4x4_16b_bf16 v[112:115], v[96:97], a[52:53], v[112:115]
v_mfma_f32_4x4x4_16b_bf16 v[120:123], a[52:53], a[52:53], v[120:123]
v_mfma_f32_4x4x4_16b_bf16 v[112:115], v[96:97], a[54:55], v[112:115]
v_mfma_f32_4x4x4_16b_bf16 v[120:123], a[54:55], a[54:55], v[120:123]
buffer_load_dwordx4 a[160:163], v38, s[48:51], 0 offen offset:640
buffer_load_dwordx4 a[164:167], v39, s[48:51], 0 offen offset:640
s_waitcnt vmcnt(13)
)"
R"(v_mfma_f32_4x4x4_16b_bf16 v[108:111], v[96:97], a[64:65], v[108:111]
v_mfma_f32_4x4x4_16b_bf16 v[116:119], a[64:65], a[64:65], v[116:119]
v_mfma_f32_4x4x4_16b_bf16 v[108:111], v[96:97], a[66:67], v[108:111]
v_mfma_f32_4x4x4_16b_bf16 v[116:119], a[66:67], a[66:67], v[116:119]
s_waitcnt vmcnt(12)
v_mfma_f32_4x4x4_16b_bf16 v[112:115], v[96:97], a[68:69], v[112:115]
v_mfma_f32_4x4x4_16b_bf16 v[120:123], a[68:69], a[68:69], v[120:123]
v_mfma_f32_4x4x4_16b_bf16 v[112:115], v[96:97], a[70:71], v[112:115]
v_mfma_f32_4x4x4_16b_bf16 v[120:123], a[70:71], a[70:71], v[120:123]
buffer_load_dwordx4 a[176:179], v38, s[48:51], 0 offen offset:704
buffer_load_dwordx4 a[180:183], v39, s[48:51], 0 offen offset:704
s_waitcnt vmcnt(13)
v_mfma_f32_4x4x4_16b_bf16 v[108:111], v[96:97], a[80:81], v[108:111]
v_mfma_f32_4x4x4_16b_bf16 v[116:119], a[80:81], a[80:81], v[116:119]
v_mfma_f32_4x4x4_16b_bf16 v[108:111], v[96:97], a[82:83], v[108:111]
v_mfma_f32_4x4x4_16b_bf16 v[116:119], a[82:83], a[82:83], v[116:119]
s_waitcnt vmcnt(12)
v_mfma_f32_4x4x4_16b_bf16 v[112:115], v[96:97], a[84:85], v[112:115]
v_mfma_f32_4x4x4_16b_bf16 v[120:123], a[84:85], a[84:85], v[120:123]
v_mfma_f32_4x4x4_16b_bf16 v[112:115], v[96:97], a[86:87], v[112:115]
v_mfma_f32_4x4x4_16b_bf16 v[120:123], a[86:87], a[86:87], v[120:123]
buffer_load_dwordx4 a[192:195], v38, s[48:51], 0 offen offset:768
buffer_load_dwordx4 a[196:199], v39, s[48:51], 0 offen offset:768
s_waitcnt vmcnt(13)
v_mfma_f32_4x4x4_16b_bf16 v[108:111], v[96:97], a[96:97], v[108:111]
v_mfma_f32_4x4x4_16b_bf16 v[116:119], a[96:97], a[96:97], v[116:119]
v_mfma_f32_4x4x4_16b_bf16 v[108:111], v[96:97], a[98:99], v[108:111]
v_mfma_f32_4x4x4_16b_bf16 v[116:119], a[98:99], a[98:99], v[116:119]
s_waitcnt vmcnt(12)
v_mfma_f32_4x4x4_16b_bf16 v[112:115], v[96:97], a[100:101], v[112:115]
v_mfma_f32_4x4x4_16b_bf16 v[120:123], a[100:101], a[100:101], v[120:123]
v_mfma_f32_4x4x4_16b_bf16 v[112:115], v[96:97], a[102:103], v[112:115]
v_mfma_f32_4x4x4_16b_bf16 v[120:123], a[102:103], a[102:103], v[120:123]
buffer_load_dwordx4 a[208:211], v38, s[48:51], 0 offen offset:832
buffer_load_dwordx4 a[212:215], v39, s[48:51], 0 offen offset:832
s_waitcnt vmcnt(13)
v_mfma_f32_4x4x4_16b_bf16 v[108:111], v[96:97], a[112:113], v[108:111]
v_mfma_f32_4x4x4_16b_bf16 v[116:119], a[112:113], a[112:113], v[116:119]
v_mfma_f32_4x4x4_16b_bf16 v[108:111], v[96:97], a[114:115], v[108:111]
v_mfma_f32_4x4x4_16b_bf16 v[116:119], a[114:115], a[114:115], v[116:119]
s_waitcnt vmcnt(12)
v_mfma_f32_4x4x4_16b_bf16 v[112:115], v[96:97], a[116:117], v[112:115]
v_mfma_f32_4x4x4_16b_bf16 v[120:123], a[116:117], a[116:117], v[120:123]
v_mfma_f32_4x4x4_16b_bf16 v[112:115], v[96:97], a[118:119], v[112:115]
v_mfma_f32_4x4x4_16b_bf16 v[120:123], a[118:119], a[118:119], v[120:123]
buffer_load_dwordx4 a[224:227], v38, s[48:51], 0 offen offset:896
buffer_load_dwordx4 a[228:231], v39, s[48:51], 0 offen offset:896
s_waitcnt vmcnt(13)
v_mfma_f32_4x4x4_16b_bf16 v[108:111], v[96:97], a[128:129], v[108:111]
v_mfma_f32_4x4x4_16b_bf16 v[116:119], a[128:129], a[128:129], v[116:119]
v_mfma_f32_4x4x4_16b_bf16 v[108:111], v[96:97], a[130:131], v[108:111]
v_mfma_f32_4x4x4_16b_bf16 v[116:119], a[130:131], a[130:131], v[116:119]
s_waitcnt vmcnt(12)
v_mfma_f32_4x4x4_16b_bf16 v[112:115], v[96:97], a[132:133], v[112:115]
v_mfma_f32_4x4x4_16b_bf16 v[120:123], a[132:133], a[132:133], v[120:123]
v_mfma_f32_4x4x4_16b_bf16 v[112:115], v[96:97], a[134:135], v[112:115]
v_mfma_f32_4x4x4_16b_bf16 v[120:123], a[134:135], a[134:135], v[120:123]
buffer_load_dwordx4 a[240:243], v38, s[48:51], 0 offen offset:960
buffer_load_dwordx4 a[244:247], v39, s[48:51], 0 offen offset:960
s_waitcnt vmcnt(13)
v_mfma_f32_4x4x4_16b_bf16 v[108:111], v[96:97], a[144:145], v[108:111]
v_mfma_f32_4x4x4_16b_bf16 v[116:119], a[144:145], a[144:145], v[116:119]
v_mfma_f32_4x4x4_16b_bf16 v[108:111], v[96:97], a[146:147], v[108:111]
v_mfma_f32_4x4x4_16b_bf16 v[116:119], a[146:147], a[146:147], v[116:119]
s_waitcnt vmcnt(12)
v_mfma_f32_4x4x4_16b_bf16 v[112:115], v[96:97], a[148:149], v[112:115]
v_mfma_f32_4x4x4_16b_bf16 v[120:123], a[148:149], a[148:149], v[120:123]
v_mfma_f32_4x4x4_16b_bf16 v[112:115], v[96:97], a[150:151], v[112:115]
v_mfma_f32_4x4x4_16b_bf16 v[120:123], a[150:151], a[150:151], v[120:123]
buffer_load_dwordx4 v[224:227], v38, s[48:51], 0 offen offset:1024
buffer_load_dwordx4 v[228:231], v39, s[48:51], 0 offen offset:1024
s_waitcnt vmcnt(13)
v_mfma_f32_4x4x4_16b_bf16 v[108:111], v[96:97], a[160:161], v[108:111]
v_mfma_f32_4x4x4_16b_bf16 v[116:119], a[160:161], a[160:161], v[116:119]
v_mfma_f32_4x4x4_16b_bf16 v[108:111], v[96:97], a[162:163], v[108:111]
v_mfma_f32_4x4x4_16b_bf16 v[116:119], a[162:163], a[162:163], v[116:119]
s_waitcnt vmcnt(12)
v_mfma_f32_4x4x4_16b_bf16 v[112:115], v[96:97], a[164:165], v[112:115]
v_mfma_f32_4x4x4_16b_bf16 v[120:123], a[164:165], a[164:165], v[120:123]
v_mfma_f32_4x4x4_16b_bf16 v[112:115], v[96:97], a[166:167], v[112:115]
v_mfma_f32_4x4x4_16b_bf16 v[120:123], a[166:167], a[166:167], v[120:123]
buffer_load_dwordx4 v[240:243], v38, s[48:51], 0 offen offset:1088
buffer_load_dwordx4 v[244:247], v39, s[48:51], 0 offen offset:1088
s_waitcnt vmcnt(13)
v_mfma_f32_4x4x4_16b_bf16 v[108:111], v[96:97], a[176:177], v[108:111]
v_mfma_f32_4x4x4_16b_bf16 v[116:119], a[176:177], a[176:177], v[116:119]
v_mfma_f32_4x4x4_16b_bf16 v[108:111], v[96:97], a[178:179], v[108:111]
v_mfma_f32_4x4x4_16b_bf16 v[116:119], a[178:179], a[178:179], v[116:119]
s_waitcnt vmcnt(12)
v_mfma_f32_4x4x4_16b_bf16 v[112:115], v[96:97], a[180:181], v[112:115]
v_mfma_f32_4x4x4_16b_bf16 v[120:123], a[180:181], a[180:181], v[120:123]
v_mfma_f32_4x4x4_16b_bf16 v[112:115], v[96:97], a[182:183], v[112:115]
v_mfma_f32_4x4x4_16b_bf16 v[120:123], a[182:183], a[182:183], v[120:123]
s_mov_b32 m0, s68
s_mov_b32 s15, 4096
buffer_load_dwordx4 v1, s[16:19], s15 offen offset:0 lds
s_mov_b32 s15, 3072
buffer_load_dwordx4 v1, s[20:23], s15 offen offset:1024 lds
s_mov_b32 s15, 296960
buffer_load_dwordx4 v1, s[16:19], s15 offen offset:2048 lds
s_mov_b32 s15, 295936
buffer_load_dwordx4 v1, s[20:23], s15 offen offset:3072 lds
s_mov_b32 m0, s69
s_mov_b32 s15, 593920
buffer_load_dwordx4 v1, s[16:19], s15 offen offset:0 lds
s_mov_b32 s15, 592896
buffer_load_dwordx4 v1, s[20:23], s15 offen offset:1024 lds
s_mov_b32 s15, 886784
buffer_load_dwordx4 v1, s[16:19], s15 offen offset:2048 lds
s_mov_b32 s15, 885760
buffer_load_dwordx4 v1, s[20:23], s15 offen offset:3072 lds
s_mov_b32 m0, s70
s_mov_b32 s15, 1183744
buffer_load_dwordx4 v1, s[16:19], s15 offen offset:0 lds
s_mov_b32 s15, 1182720
buffer_load_dwordx4 v1, s[20:23], s15 offen offset:1024 lds
s_mov_b32 s15, 1476608
buffer_load_dwordx4 v1, s[16:19], s15 offen offset:2048 lds
s_mov_b32 s15, 1475584
buffer_load_dwordx4 v1, s[20:23], s15 offen offset:3072 lds
s_mov_b32 m0, 161792
s_waitcnt vmcnt(23)
v_mfma_f32_4x4x4_16b_bf16 v[108:111], v[96:97], a[192:193], v[108:111]
v_mfma_f32_4x4x4_16b_bf16 v[116:119], a[192:193], a[192:193], v[116:119]
v_mfma_f32_4x4x4_16b_bf16 v[108:111], v[96:97], a[194:195], v[108:111]
v_mfma_f32_4x4x4_16b_bf16 v[116:119], a[194:195], a[194:195], v[116:119]
s_waitcnt vmcnt(22)
v_mfma_f32_4x4x4_16b_bf16 v[112:115], v[96:97], a[196:197], v[112:115]
v_mfma_f32_4x4x4_16b_bf16 v[120:123], a[196:197], a[196:197], v[120:123]
v_mfma_f32_4x4x4_16b_bf16 v[112:115], v[96:97], a[198:199], v[112:115]
v_mfma_f32_4x4x4_16b_bf16 v[120:123], a[198:199], a[198:199], v[120:123]
s_waitcnt vmcnt(21)
v_mfma_f32_4x4x4_16b_bf16 v[108:111], v[96:97], a[208:209], v[108:111]
v_mfma_f32_4x4x4_16b_bf16 v[116:119], a[208:209], a[208:209], v[116:119]
v_mfma_f32_4x4x4_16b_bf16 v[108:111], v[96:97], a[210:211], v[108:111]
v_mfma_f32_4x4x4_16b_bf16 v[116:119], a[210:211], a[210:211], v[116:119]
s_waitcnt vmcnt(20)
v_mfma_f32_4x4x4_16b_bf16 v[112:115], v[96:97], a[212:213], v[112:115]
v_mfma_f32_4x4x4_16b_bf16 v[120:123], a[212:213], a[212:213], v[120:123]
v_mfma_f32_4x4x4_16b_bf16 v[112:115], v[96:97], a[214:215], v[112:115]
v_mfma_f32_4x4x4_16b_bf16 v[120:123], a[214:215], a[214:215], v[120:123]
s_waitcnt vmcnt(19)
v_mfma_f32_4x4x4_16b_bf16 v[108:111], v[96:97], a[224:225], v[108:111]
v_mfma_f32_4x4x4_16b_bf16 v[116:119], a[224:225], a[224:225], v[116:119]
v_mfma_f32_4x4x4_16b_bf16 v[108:111], v[96:97], a[226:227], v[108:111]
v_mfma_f32_4x4x4_16b_bf16 v[116:119], a[226:227], a[226:227], v[116:119]
s_waitcnt vmcnt(18)
v_mfma_f32_4x4x4_16b_bf16 v[112:115], v[96:97], a[228:229], v[112:115]
v_mfma_f32_4x4x4_16b_bf16 v[120:123], a[228:229], a[228:229], v[120:123]
v_mfma_f32_4x4x4_16b_bf16 v[112:115], v[96:97], a[230:231], v[112:115]
v_mfma_f32_4x4x4_16b_bf16 v[120:123], a[230:231], a[230:231], v[120:123]
s_waitcnt vmcnt(17)
v_mfma_f32_4x4x4_16b_bf16 v[108:111], v[96:97], a[240:241], v[108:111]
v_mfma_f32_4x4x4_16b_bf16 v[116:119], a[240:241], a[240:241], v[116:119]
v_mfma_f32_4x4x4_16b_bf16 v[108:111], v[96:97], a[242:243], v[108:111]
v_mfma_f32_4x4x4_16b_bf16 v[116:119], a[242:243], a[242:243], v[116:119]
s_waitcnt vmcnt(16)
v_mfma_f32_4x4x4_16b_bf16 v[112:115], v[96:97], a[244:245], v[112:115]
v_mfma_f32_4x4x4_16b_bf16 v[120:123], a[244:245], a[244:245], v[120:123]
v_mfma_f32_4x4x4_16b_bf16 v[112:115], v[96:97], a[246:247], v[112:115]
v_mfma_f32_4x4x4_16b_bf16 v[120:123], a[246:247], a[246:247], v[120:123]
s_waitcnt vmcnt(15)
v_mfma_f32_4x4x4_16b_bf16 v[108:111], v[96:97], v[224:225], v[108:111]
v_mfma_f32_4x4x4_16b_bf16 v[116:119], v[224:225], v[224:225], v[116:119]
v_mfma_f32_4x4x4_16b_bf16 v[108:111], v[96:97], v[226:227], v[108:111]
v_mfma_f32_4x4x4_16b_bf16 v[116:119], v[226:227], v[226:227], v[116:119]
s_waitcnt vmcnt(14)
v_mfma_f32_4x4x4_16b_bf16 v[112:115], v[96:97], v[228:229], v[112:115]
v_mfma_f32_4x4x4_16b_bf16 v[120:123], v[228:229], v[228:229], v[120:123]
v_mfma_f32_4x4x4_16b_bf16 v[112:115], v[96:97], v[230:231], v[112:115]
v_mfma_f32_4x4x4_16b_bf16 v[120:123], v[230:231], v[230:231], v[120:123]
s_waitcnt vmcnt(13)
v_mfma_f32_4x4x4_16b_bf16 v[108:111], v[96:97], v[240:241], v[108:111]
v_mfma_f32_4x4x4_16b_bf16 v[116:119], v[240:241], v[240:241], v[116:119]
v_mfma_f32_4x4x4_16b_bf16 v[108:111], v[96:97], v[242:243], v[108:111]
v_mfma_f32_4x4x4_16b_bf16 v[116:119], v[242:243], v[242:243], v[116:119]
s_waitcnt vmcnt(12)
v_mfma_f32_4x4x4_16b_bf16 v[112:115], v[96:97], v[244:245], v[112:115]
v_mfma_f32_4x4x4_16b_bf16 v[120:123], v[244:245], v[244:245], v[120:123]
v_mfma_f32_4x4x4_16b_bf16 v[112:115], v[96:97], v[246:247], v[112:115]
v_mfma_f32_4x4x4_16b_bf16 v[120:123], v[246:247], v[246:247], v[120:123]
buffer_load_dword v0, s[4:7], s92 idxen  lds
s_nop 1
v_cndmask_b32 v116, v116, v117, s[72:73]
s_nop 3
v_cndmask_b32 v120, v120, v121, s[72:73]
v_cndmask_b32 v116, v116, v118, s[74:75]
v_cndmask_b32 v120, v120, v122, s[74:75]
v_cndmask_b32 v116, v116, v119, s[76:77]
v_cndmask_b32 v120, v120, v123, s[76:77]
v_add_f32_dpp v108, v108, v108 quad_perm:[1,0,3,2] row_mask:0xf bank_mask:0xf
v_add_f32_dpp v112, v112, v112 quad_perm:[1,0,3,2] row_mask:0xf bank_mask:0xf
v_add_f32_dpp v116, v116, v116 quad_perm:[1,0,3,2] row_mask:0xf bank_mask:0xf
v_add_f32_dpp v120, v120, v120 quad_perm:[1,0,3,2] row_mask:0xf bank_mask:0xf
v_add_f32_dpp v108, v108, v108 quad_perm:[2,3,0,1] row_mask:0xf bank_mask:0xf
v_add_f32_dpp v112, v112, v112 quad_perm:[2,3,0,1] row_mask:0xf bank_mask:0xf
v_add_f32_dpp v116, v116, v116 quad_perm:[2,3,0,1] row_mask:0xf bank_mask:0xf
v_add_f32_dpp v120, v120, v120 quad_perm:[2,3,0,1] row_mask:0xf bank_mask:0xf
v_mul_f32 v109, 0x3ae38e39, v108
v_mul_f32 v113, 0x3ae38e39, v112
VC 108,109,109
VC 112,113,113
v_and_b32 v50, 0xffff0000, v108
v_and_b32 v52, 0xffff0000, v112
VM 109,109,109
VM 113,113,113
v_lshlrev_b32 v54, 2, v36
v_lshlrev_b32 v55, 2, v36
v_add_u32 v54, 147456, v54
v_add_u32 v55, 147472, v55
v_lshl_add_u32 v56, v36, 3, 0
v_lshl_add_u32 v57, v36, 3, 32
v_mov_b32 v117, 0x3ae38e39
v_mov_b32 v121, 0x3ae38e39
v_fma_f32 v116, v116, v117, -v109
v_fma_f32 v120, v120, v121, -v113
v_max_f32 v116, 0, v116
v_max_f32 v120, 0, v120
VC 116,116,116
VC 120,120,120
v_and_b32 v116, 0xffff0000, v116
v_and_b32 v120, 0xffff0000, v120
v_add_f32 v116, 0x3727c5ac, v116
v_add_f32 v120, 0x3727c5ac, v120
VC 116,116,116
VC 120,120,120
v_and_b32 v116, 0xffff0000, v116
v_and_b32 v120, 0xffff0000, v120
v_sqrt_f32 v116, v116
v_sqrt_f32 v120, v120
v_xor_b32 v108, 0x80000000, v50
v_xor_b32 v112, 0x80000000, v52
VC 116,116,116
VC 120,120,120
v_and_b32 v51, 0xffff0000, v116
v_and_b32 v53, 0xffff0000, v120
v_rcp_f32 v116, v51
v_rcp_f32 v120, v53
ds_write_b32 v54, v51
ds_write_b32 v55, v53
v_mov_b32 v109, v108
v_mov_b32 v113, v112
v_mov_b32 v110, v108
v_mov_b32 v114, v112
v_mov_b32 v111, v108
v_mov_b32 v115, v112
ds_write_b32 v54, v116 offset:512
ds_write_b32 v55, v120 offset:512
buffer_store_dwordx2 v[50:51], v56, s[60:63], 0 offen sc1
buffer_store_dwordx2 v[52:53], v57, s[60:63], 0 offen sc1
s_waitcnt lgkmcnt(0)
s_add_u32 s100, s100, 1
s_add_u32 s101, s101, 1
v_mfma_f32_16x16x32_bf16 v[124:127], v[102:105], a[0:3], v[108:111]
v_mfma_f32_16x16x32_bf16 v[128:131], v[100:103], a[0:3], v[108:111]
v_mfma_f32_16x16x32_bf16 v[132:135], v[102:105], a[4:7], v[112:115]
v_mfma_f32_16x16x32_bf16 v[136:139], v[100:103], a[4:7], v[112:115]
s_nop 4
VC 140,124,125
VC 141,126,127
VC 142,128,129
VC 143,130,131
ds_write_b128 v156, v[140:143] offset:0
v_mfma_f32_16x16x32_bf16 v[124:127], v[102:105], a[16:19], v[108:111]
v_mfma_f32_16x16x32_bf16 v[128:131], v[100:103], a[16:19], v[108:111]
VC 144,132,133
VC 145,134,135
VC 146,136,137
VC 147,138,139
ds_write_b128 v156, v[144:147] offset:1024
v_mfma_f32_16x16x32_bf16 v[132:135], v[102:105], a[20:23], v[112:115]
v_mfma_f32_16x16x32_bf16 v[136:139], v[100:103], a[20:23], v[112:115]
VC 148,124,125
VC 149,126,127
VC 150,128,129
VC 151,130,131
ds_write_b128 v156, v[148:151] offset:4096
v_mfma_f32_16x16x32_bf16 v[124:127], v[102:105], a[32:35], v[108:111]
v_mfma_f32_16x16x32_bf16 v[128:131], v[100:103], a[32:35], v[108:111]
VC 152,132,133
VC 153,134,135
VC 154,136,137
VC 155,138,139
ds_write_b128 v156, v[152:155] offset:5120
v_mfma_f32_16x16x32_bf16 v[132:135], v[102:105], a[36:39], v[112:115]
v_mfma_f32_16x16x32_bf16 v[136:139], v[100:103], a[36:39], v[112:115]
VC 140,124,125
VC 141,126,127
VC 142,128,129
VC 143,130,131
ds_write_b128 v156, v[140:143] offset:16384
v_mfma_f32_16x16x32_bf16 v[124:127], v[102:105], a[48:51], v[108:111]
v_mfma_f32_16x16x32_bf16 v[128:131], v[100:103], a[48:51], v[108:111]
VC 144,132,133
VC 145,134,135
VC 146,136,137
VC 147,138,139
ds_write_b128 v156, v[144:147] offset:17408
v_mfma_f32_16x16x32_bf16 v[132:135], v[102:105], a[52:55], v[112:115]
v_mfma_f32_16x16x32_bf16 v[136:139], v[100:103], a[52:55], v[112:115]
VC 148,124,125
VC 149,126,127
VC 150,128,129
VC 151,130,131
ds_write_b128 v156, v[148:151] offset:20480
s_nop 1
VC 152,132,133
VC 153,134,135
VC 154,136,137
VC 155,138,139
ds_write_b128 v156, v[152:155] offset:21504
s_waitcnt lgkmcnt(0)
s_waitcnt vmcnt(11)
s_barrier
ds_read_b128 a[0:3], v157 offset:0
ds_read_b128 a[4:7], v157 offset:1024
ds_read_b128 a[8:11], v157 offset:2048
ds_read_b128 a[12:15], v157 offset:3072
ds_read_b128 a[16:19], v157 offset:4096
ds_read_b128 a[20:23], v157 offset:5120
ds_read_b128 a[24:27], v157 offset:6144
ds_read_b128 a[28:31], v157 offset:7168
v_lshl_add_u32 v54, v34, 4, v35
s_lshl_b32 s67, s64, 6
v_add_u32 v54, s67, v54
v_lshlrev_b32 v54, 2, v54
v_add_u32 v54, 147456, v54
ds_read_b32 v10, v54 offset:0
ds_read_b32 v6, v54 offset:512
ds_read_b32 v11, v54 offset:16
ds_read_b32 v7, v54 offset:528
ds_read_b32 v12, v54 offset:32
ds_read_b32 v8, v54 offset:544
ds_read_b32 v13, v54 offset:48
ds_read_b32 v9, v54 offset:560
TR 192,2,0
TR 194,2,512
TR 196,2,1024
TR 198,2,1536
s_waitcnt lgkmcnt(11)
VM 10,10,10
s_waitcnt lgkmcnt(10)
v_mul_f32 v6, 0xbfb8aa3b, v6
s_waitcnt lgkmcnt(9)
VM 11,11,11
s_waitcnt lgkmcnt(8)
v_mul_f32 v7, 0xbfb8aa3b, v7
s_waitcnt lgkmcnt(7)
VM 12,12,12
s_waitcnt lgkmcnt(6)
v_mul_f32 v8, 0xbfb8aa3b, v8
s_waitcnt lgkmcnt(5)
VM 13,13,13
s_waitcnt lgkmcnt(4)
v_mul_f32 v9, 0xbfb8aa3b, v9
s_waitcnt vmcnt(7)
s_barrier
s_waitcnt lgkmcnt(2)
Zva 32,192,0
s_mov_b32 m0, s71
ds_read_b128 a[32:35], v157 offset:16384
Zva 48,192,4
s_mov_b32 s15, 1773568
buffer_load_dwordx4 v1, s[16:19], s15 offen offset:0 lds
Zva 64,192,8
TR 200,3,0
Zva 80,192,12
TR 202,3,512
v_mfma_f32_16x16x32_bf16 v[124:127], v[102:105], a[64:67], v[108:111]
s_mov_b32 s15, 1772544
buffer_load_dwordx4 v1, s[20:23], s15 offen offset:1024 lds
v_mfma_f32_16x16x32_bf16 v[128:131], v[100:103], a[64:67], v[108:111]
ds_read_b128 a[36:39], v157 offset:17408
s_waitcnt lgkmcnt(4)
Zva 36,196,0
TR 204,3,1024
Zva 52,196,4
s_mov_b32 s15, 2066432
buffer_load_dwordx4 v1, s[16:19], s15 offen offset:2048 lds
Zva 68,196,8
TR 206,3,1536
VC 140,124,125
Zva 84,196,12
ds_read_b128 a[40:43], v157 offset:18432
VC 141,126,127
v_mfma_f32_16x16x32_bf16 v[132:135], v[102:105], a[68:71], v[112:115]
s_mov_b32 s15, 2065408
buffer_load_dwordx4 v1, s[20:23], s15 offen offset:3072 lds
v_mfma_f32_16x16x32_bf16 v[136:139], v[100:103], a[68:71], v[112:115]
VC 142,128,129
VC 143,130,131
s_waitcnt lgkmcnt(4)
Zva 40,200,0
TR 208,2,2048
Zva 56,200,4
TR 210,2,2560
Zva 72,200,8
ds_write_b128 v156, v[140:143] offset:32768
Zva 88,200,12
ds_read_b128 a[44:47], v157 offset:19456
VC 144,132,133
s_waitcnt lgkmcnt(5)
Zva 44,204,0
VC 145,134,135
VC 146,136,137
Zva 60,204,4
VC 147,138,139
TR 212,2,3072
Zva 76,204,8
ds_read_b128 a[48:51], v157 offset:20480
Zva 92,204,12
TR 214,2,3584
v_mfma_f32_16x16x32_bf16 v[124:127], v[102:105], a[80:83], v[108:111]
ds_write_b128 v156, v[144:147] offset:33792
v_mfma_f32_16x16x32_bf16 v[128:131], v[100:103], a[80:83], v[108:111]
TR 192,3,2048
s_waitcnt lgkmcnt(7)
Mva 32,208,16
ds_read_b128 a[52:55], v157 offset:21504
Mva 48,208,20
TR 194,3,2560
Mva 64,208,24
TR 196,3,3072
VC 148,124,125
Mva 80,208,28
ds_read_b128 a[56:59], v157 offset:22528
VC 149,126,127
v_mfma_f32_16x16x32_bf16 v[132:135], v[102:105], a[84:87], v[112:115]
TR 198,3,3584
VC 150,128,129
v_mfma_f32_16x16x32_bf16 v[136:139], v[100:103], a[84:87], v[112:115]
VC 151,130,131
TR 200,2,16384
s_waitcnt lgkmcnt(8)
Mva 36,212,16
ds_write_b128 v156, v[148:151] offset:36864
Mva 52,212,20
TR 202,2,16896
Mva 68,212,24
ds_read_b128 a[60:63], v157 offset:23552
VC 152,132,133
Mva 84,212,28
VC 153,134,135
VC 154,136,137
s_waitcnt lgkmcnt(7)
Mva 40,192,16
VC 155,138,139
TR 204,2,17408
Mva 56,192,20
ds_write_b128 v156, v[152:155] offset:37888
Mva 72,192,24
TR 206,2,17920
Mva 88,192,28
s_waitcnt lgkmcnt(7)
Mva 44,196,16
Mva 60,196,20
Mva 76,196,24
TR 208,3,16384
Mva 92,196,28
TR 210,3,16896
s_waitcnt lgkmcnt(3)
s_waitcnt vmcnt(7)
s_barrier
Mva 32,200,32
s_mov_b32 m0, s68
ds_read_b128 a[64:67], v157 offset:32768
Mva 48,200,36
s_mov_b32 s15, 2363392
buffer_load_dwordx4 v1, s[16:19], s15 offen offset:0 lds
Mva 64,200,40
ds_read_b128 a[68:71], v157 offset:33792
Mva 80,200,44
TR 212,3,17408
v_mfma_f32_16x16x32_bf16 v[124:127], v[102:105], a[96:99], v[108:111]
s_mov_b32 s15, 2362368
buffer_load_dwordx4 v1, s[20:23], s15 offen offset:1024 lds
v_mfma_f32_16x16x32_bf16 v[128:131], v[100:103], a[96:99], v[108:111]
TR 214,3,17920
s_waitcnt lgkmcnt(6)
Mva 36,204,32
ds_read_b128 a[72:75], v157 offset:34816
Mva 52,204,36
s_mov_b32 s15, 2656256
buffer_load_dwordx4 v1, s[16:19], s15 offen offset:2048 lds
Mva 68,204,40
VC 140,124,125
VC 141,126,127
Mva 84,204,44
VC 142,128,129
VC 143,130,131
v_mfma_f32_16x16x32_bf16 v[132:135], v[102:105], a[100:103], v[112:115]
s_mov_b32 s15, 2655232
buffer_load_dwordx4 v1, s[20:23], s15 offen offset:3072 lds
v_mfma_f32_16x16x32_bf16 v[136:139], v[100:103], a[100:103], v[112:115]
TR 192,2,18432
s_waitcnt lgkmcnt(6)
Mva 40,208,32
TR 194,2,18944
Mva 56,208,36
ds_write_b128 v156, v[140:143] offset:0
Mva 72,208,40
ds_read_b128 a[76:79], v157 offset:35840
VC 144,132,133
Mva 88,208,44
VC 145,134,135
VC 146,136,137
s_waitcnt lgkmcnt(5)
Mva 44,212,32
VC 147,138,139
TR 196,2,19456
Mva 60,212,36
ds_read_b128 a[80:83], v157 offset:36864
Mva 76,212,40
TR 198,2,19968
Mva 92,212,44
ds_write_b128 v156, v[144:147] offset:1024
v_mfma_f32_16x16x32_bf16 v[124:127], v[102:105], a[112:115], v[108:111]
TR 200,3,18432
v_mfma_f32_16x16x32_bf16 v[128:131], v[100:103], a[112:115], v[108:111]
ds_read_b128 a[84:87], v157 offset:37888
s_waitcnt lgkmcnt(8)
Mva 32,192,48
TR 202,3,18944
Mva 48,192,52
TR 204,3,19456
Mva 64,192,56
ds_read_b128 a[88:91], v157 offset:38912
VC 148,124,125
Mva 80,192,60
TR 206,3,19968
VC 149,126,127
v_mfma_f32_16x16x32_bf16 v[132:135], v[102:105], a[116:119], v[112:115]
VC 150,128,129
VC 151,130,131
v_mfma_f32_16x16x32_bf16 v[136:139], v[100:103], a[116:119], v[112:115]
TR 208,2,32768
s_waitcnt lgkmcnt(8)
Mva 36,196,48
ds_write_b128 v156, v[148:151] offset:4096
Mva 52,196,52
TR 210,2,33280
Mva 68,196,56
ds_read_b128 a[92:95], v157 offset:39936
VC 152,132,133
Mva 84,196,60
VC 153,134,135
VC 154,136,137
s_waitcnt lgkmcnt(7)
Mva 40,200,48
VC 155,138,139
TR 212,2,33792
Mva 56,200,52
ds_write_b128 v156, v[152:155] offset:5120
Mva 72,200,56
TR 214,2,34304
Mva 88,200,60
s_waitcnt lgkmcnt(7)
Mva 44,204,48
Mva 60,204,52
Mva 76,204,56
TR 192,3,32768
Mva 92,204,60
TR 194,3,33280
s_waitcnt lgkmcnt(3)
s_waitcnt vmcnt(4)
s_barrier
Mva 32,208,64
s_mov_b32 m0, s69
ds_read_b128 a[96:99], v157 offset:0
Mva 48,208,68
s_mov_b32 s15, 2953216
buffer_load_dwordx4 v1, s[16:19], s15 offen offset:0 lds
Mva 64,208,72
ds_read_b128 a[100:103], v157 offset:1024
Mva 80,208,76
TR 196,3,33792
v_mfma_f32_16x16x32_bf16 v[124:127], v[102:105], a[128:131], v[108:111]
s_mov_b32 s15, 2952192
buffer_load_dwordx4 v1, s[20:23], s15 offen offset:1024 lds
v_mfma_f32_16x16x32_bf16 v[128:131], v[100:103], a[128:131], v[108:111]
TR 198,3,34304
s_waitcnt lgkmcnt(6)
Mva 36,212,64
ds_read_b128 a[104:107], v157 offset:2048
Mva 52,212,68
s_mov_b32 s15, 3246080
buffer_load_dwordx4 v1, s[16:19], s15 offen offset:2048 lds
Mva 68,212,72
VC 140,124,125
VC 141,126,127
Mva 84,212,76
VC 142,128,129
VC 143,130,131
v_mfma_f32_16x16x32_bf16 v[132:135], v[102:105], a[132:135], v[112:115]
s_mov_b32 s15, 3245056
buffer_load_dwordx4 v1, s[20:23], s15 offen offset:3072 lds
v_mfma_f32_16x16x32_bf16 v[136:139], v[100:103], a[132:135], v[112:115]
TR 200,2,34816
s_waitcnt lgkmcnt(6)
Mva 40,192,64
)"
R"(TR 202,2,35328
Mva 56,192,68
ds_write_b128 v156, v[140:143] offset:16384
Mva 72,192,72
ds_read_b128 a[108:111], v157 offset:3072
VC 144,132,133
Mva 88,192,76
VC 145,134,135
VC 146,136,137
s_waitcnt lgkmcnt(5)
Mva 44,196,64
VC 147,138,139
TR 204,2,35840
Mva 60,196,68
ds_read_b128 a[112:115], v157 offset:4096
Mva 76,196,72
TR 206,2,36352
Mva 92,196,76
ds_write_b128 v156, v[144:147] offset:17408
v_mfma_f32_16x16x32_bf16 v[124:127], v[102:105], a[144:147], v[108:111]
TR 208,3,34816
v_mfma_f32_16x16x32_bf16 v[128:131], v[100:103], a[144:147], v[108:111]
ds_read_b128 a[116:119], v157 offset:5120
s_waitcnt lgkmcnt(8)
Mva 32,200,80
TR 210,3,35328
Mva 48,200,84
TR 212,3,35840
Mva 64,200,88
ds_read_b128 a[120:123], v157 offset:6144
VC 148,124,125
Mva 80,200,92
TR 214,3,36352
VC 149,126,127
v_mfma_f32_16x16x32_bf16 v[132:135], v[102:105], a[148:151], v[112:115]
VC 150,128,129
VC 151,130,131
v_mfma_f32_16x16x32_bf16 v[136:139], v[100:103], a[148:151], v[112:115]
TR 192,2,49152
s_waitcnt lgkmcnt(8)
Mva 36,204,80
ds_write_b128 v156, v[148:151] offset:20480
Mva 52,204,84
TR 194,2,49664
Mva 68,204,88
ds_read_b128 a[124:127], v157 offset:7168
VC 152,132,133
Mva 84,204,92
VC 153,134,135
VC 154,136,137
s_waitcnt lgkmcnt(7)
Mva 40,208,80
VC 155,138,139
TR 196,2,50176
Mva 56,208,84
ds_write_b128 v156, v[152:155] offset:21504
Mva 72,208,88
TR 198,2,50688
Mva 88,208,92
s_waitcnt lgkmcnt(7)
Mva 44,212,80
Mva 60,212,84
Mva 76,212,88
TR 200,3,49152
Mva 92,212,92
TR 202,3,49664
s_waitcnt lgkmcnt(3)
s_waitcnt vmcnt(4)
s_barrier
Mva 32,192,96
s_mov_b32 m0, s70
ds_read_b128 a[128:131], v157 offset:16384
Mva 48,192,100
s_mov_b32 s15, 3543040
buffer_load_dwordx4 v1, s[16:19], s15 offen offset:0 lds
Mva 64,192,104
ds_read_b128 a[132:135], v157 offset:17408
Mva 80,192,108
TR 204,3,50176
v_mfma_f32_16x16x32_bf16 v[124:127], v[102:105], a[160:163], v[108:111]
s_mov_b32 s15, 3542016
buffer_load_dwordx4 v1, s[20:23], s15 offen offset:1024 lds
v_mfma_f32_16x16x32_bf16 v[128:131], v[100:103], a[160:163], v[108:111]
TR 206,3,50688
s_waitcnt lgkmcnt(6)
Mva 36,196,96
ds_read_b128 a[136:139], v157 offset:18432
Mva 52,196,100
s_mov_b32 s15, 3835904
buffer_load_dwordx4 v1, s[16:19], s15 offen offset:2048 lds
Mva 68,196,104
VC 140,124,125
VC 141,126,127
Mva 84,196,108
VC 142,128,129
VC 143,130,131
v_mfma_f32_16x16x32_bf16 v[132:135], v[102:105], a[164:167], v[112:115]
s_mov_b32 s15, 3834880
buffer_load_dwordx4 v1, s[20:23], s15 offen offset:3072 lds
v_mfma_f32_16x16x32_bf16 v[136:139], v[100:103], a[164:167], v[112:115]
TR 208,2,51200
s_waitcnt lgkmcnt(6)
Mva 40,200,96
TR 210,2,51712
Mva 56,200,100
ds_write_b128 v156, v[140:143] offset:32768
Mva 72,200,104
ds_read_b128 a[140:143], v157 offset:19456
VC 144,132,133
Mva 88,200,108
VC 145,134,135
VC 146,136,137
s_waitcnt lgkmcnt(5)
Mva 44,204,96
VC 147,138,139
TR 212,2,52224
Mva 60,204,100
ds_read_b128 a[144:147], v157 offset:20480
Mva 76,204,104
TR 214,2,52736
Mva 92,204,108
ds_write_b128 v156, v[144:147] offset:33792
v_mfma_f32_16x16x32_bf16 v[124:127], v[102:105], a[176:179], v[108:111]
TR 192,3,51200
v_mfma_f32_16x16x32_bf16 v[128:131], v[100:103], a[176:179], v[108:111]
ds_read_b128 a[148:151], v157 offset:21504
s_waitcnt lgkmcnt(8)
Mva 32,208,112
TR 194,3,51712
Mva 48,208,116
TR 196,3,52224
Mva 64,208,120
ds_read_b128 a[152:155], v157 offset:22528
VC 148,124,125
Mva 80,208,124
TR 198,3,52736
VC 149,126,127
v_mfma_f32_16x16x32_bf16 v[132:135], v[102:105], a[180:183], v[112:115]
VC 150,128,129
VC 151,130,131
v_mfma_f32_16x16x32_bf16 v[136:139], v[100:103], a[180:183], v[112:115]
TR 200,2,0
s_waitcnt lgkmcnt(8)
Mva 36,212,112
ds_write_b128 v156, v[148:151] offset:36864
Mva 52,212,116
TR 202,2,512
Mva 68,212,120
ds_read_b128 a[156:159], v157 offset:23552
VC 152,132,133
Mva 84,212,124
VC 153,134,135
VC 154,136,137
s_waitcnt lgkmcnt(7)
Mva 40,192,112
VC 155,138,139
TR 204,2,1024
Mva 56,192,116
ds_write_b128 v156, v[152:155] offset:37888
Mva 72,192,120
TR 206,2,1536
Mva 88,192,124
s_waitcnt lgkmcnt(7)
Mva 44,196,112
Mva 60,196,116
Mva 76,196,120
TR 208,3,0
Mva 92,196,124
TR 210,3,512
s_waitcnt lgkmcnt(3)
s_waitcnt vmcnt(4)
s_barrier
Mva 32,200,128
s_mov_b32 m0, s71
ds_read_b128 a[160:163], v157 offset:32768
Mva 48,200,132
s_mov_b32 s15, 4132864
buffer_load_dwordx4 v1, s[16:19], s15 offen offset:0 lds
Mva 64,200,136
ds_read_b128 a[164:167], v157 offset:33792
Mva 80,200,140
TR 212,3,1024
v_mfma_f32_16x16x32_bf16 v[124:127], v[102:105], a[192:195], v[108:111]
s_mov_b32 s15, 4131840
buffer_load_dwordx4 v1, s[20:23], s15 offen offset:1024 lds
v_mfma_f32_16x16x32_bf16 v[128:131], v[100:103], a[192:195], v[108:111]
TR 214,3,1536
s_waitcnt lgkmcnt(6)
Mva 36,204,128
ds_read_b128 a[168:171], v157 offset:34816
Mva 52,204,132
s_mov_b32 s15, 4425728
buffer_load_dwordx4 v1, s[16:19], s15 offen offset:2048 lds
Mva 68,204,136
VC 140,124,125
VC 141,126,127
Mva 84,204,140
VC 142,128,129
VC 143,130,131
v_mfma_f32_16x16x32_bf16 v[132:135], v[102:105], a[196:199], v[112:115]
s_mov_b32 s15, 4424704
buffer_load_dwordx4 v1, s[20:23], s15 offen offset:3072 lds
v_mfma_f32_16x16x32_bf16 v[136:139], v[100:103], a[196:199], v[112:115]
TR 192,2,2048
s_waitcnt lgkmcnt(6)
Mva 40,208,128
TR 194,2,2560
Mva 56,208,132
ds_write_b128 v156, v[140:143] offset:0
Mva 72,208,136
ds_read_b128 a[172:175], v157 offset:35840
VC 144,132,133
Mva 88,208,140
VC 145,134,135
VC 146,136,137
s_waitcnt lgkmcnt(5)
Mva 44,212,128
VC 147,138,139
TR 196,2,3072
Mva 60,212,132
ds_read_b128 a[176:179], v157 offset:36864
Mva 76,212,136
TR 198,2,3584
Mva 92,212,140
ds_write_b128 v156, v[144:147] offset:1024
v_mfma_f32_16x16x32_bf16 v[124:127], v[102:105], a[208:211], v[108:111]
TR 200,3,2048
v_mfma_f32_16x16x32_bf16 v[128:131], v[100:103], a[208:211], v[108:111]
ds_read_b128 a[180:183], v157 offset:37888
s_waitcnt lgkmcnt(8)
Mva 32,192,144
TR 202,3,2560
Mva 48,192,148
TR 204,3,3072
Mva 64,192,152
ds_read_b128 a[184:187], v157 offset:38912
VC 148,124,125
Mva 80,192,156
TR 206,3,3584
VC 149,126,127
v_mfma_f32_16x16x32_bf16 v[132:135], v[102:105], a[212:215], v[112:115]
VC 150,128,129
VC 151,130,131
v_mfma_f32_16x16x32_bf16 v[136:139], v[100:103], a[212:215], v[112:115]
TR 208,2,16384
s_waitcnt lgkmcnt(8)
Mva 36,196,144
ds_write_b128 v156, v[148:151] offset:4096
Mva 52,196,148
TR 210,2,16896
Mva 68,196,152
ds_read_b128 a[188:191], v157 offset:39936
VC 152,132,133
Mva 84,196,156
VC 153,134,135
VC 154,136,137
s_waitcnt lgkmcnt(7)
Mva 40,200,144
VC 155,138,139
TR 212,2,17408
Mva 56,200,148
ds_write_b128 v156, v[152:155] offset:5120
Mva 72,200,152
TR 214,2,17920
Mva 88,200,156
s_waitcnt lgkmcnt(7)
Mva 44,204,144
Mva 60,204,148
Mva 76,204,152
TR 192,3,16384
Mva 92,204,156
TR 194,3,16896
s_waitcnt lgkmcnt(3)
s_waitcnt vmcnt(4)
s_barrier
Mva 32,208,160
s_mov_b32 m0, s68
ds_read_b128 a[192:195], v157 offset:0
Mva 48,208,164
s_mov_b32 s15, 4722688
buffer_load_dwordx4 v1, s[16:19], s15 offen offset:0 lds
Mva 64,208,168
ds_read_b128 a[196:199], v157 offset:1024
Mva 80,208,172
TR 196,3,17408
v_mfma_f32_16x16x32_bf16 v[124:127], v[102:105], a[224:227], v[108:111]
s_mov_b32 s15, 4721664
buffer_load_dwordx4 v1, s[20:23], s15 offen offset:1024 lds
v_mfma_f32_16x16x32_bf16 v[128:131], v[100:103], a[224:227], v[108:111]
TR 198,3,17920
s_waitcnt lgkmcnt(6)
Mva 36,212,160
ds_read_b128 a[200:203], v157 offset:2048
Mva 52,212,164
s_mov_b32 s15, 5015552
buffer_load_dwordx4 v1, s[16:19], s15 offen offset:2048 lds
Mva 68,212,168
VC 140,124,125
VC 141,126,127
Mva 84,212,172
VC 142,128,129
VC 143,130,131
v_mfma_f32_16x16x32_bf16 v[132:135], v[102:105], a[228:231], v[112:115]
s_mov_b32 s15, 5014528
buffer_load_dwordx4 v1, s[20:23], s15 offen offset:3072 lds
v_mfma_f32_16x16x32_bf16 v[136:139], v[100:103], a[228:231], v[112:115]
TR 200,2,18432
s_waitcnt lgkmcnt(6)
Mva 40,192,160
TR 202,2,18944
Mva 56,192,164
ds_write_b128 v156, v[140:143] offset:16384
Mva 72,192,168
ds_read_b128 a[204:207], v157 offset:3072
VC 144,132,133
Mva 88,192,172
VC 145,134,135
VC 146,136,137
s_waitcnt lgkmcnt(5)
Mva 44,196,160
VC 147,138,139
TR 204,2,19456
Mva 60,196,164
ds_read_b128 a[208:211], v157 offset:4096
Mva 76,196,168
TR 206,2,19968
Mva 92,196,172
ds_write_b128 v156, v[144:147] offset:17408
v_mfma_f32_16x16x32_bf16 v[124:127], v[102:105], a[240:243], v[108:111]
TR 208,3,18432
v_mfma_f32_16x16x32_bf16 v[128:131], v[100:103], a[240:243], v[108:111]
ds_read_b128 a[212:215], v157 offset:5120
s_waitcnt lgkmcnt(8)
Mva 32,200,176
TR 210,3,18944
Mva 48,200,180
TR 212,3,19456
Mva 64,200,184
ds_read_b128 a[216:219], v157 offset:6144
VC 148,124,125
Mva 80,200,188
TR 214,3,19968
VC 149,126,127
v_mfma_f32_16x16x32_bf16 v[132:135], v[102:105], a[244:247], v[112:115]
VC 150,128,129
VC 151,130,131
v_mfma_f32_16x16x32_bf16 v[136:139], v[100:103], a[244:247], v[112:115]
TR 192,2,32768
s_waitcnt lgkmcnt(8)
Mva 36,204,176
ds_write_b128 v156, v[148:151] offset:20480
Mva 52,204,180
TR 194,2,33280
Mva 68,204,184
ds_read_b128 a[220:223], v157 offset:7168
VC 152,132,133
Mva 84,204,188
VC 153,134,135
VC 154,136,137
s_waitcnt lgkmcnt(7)
Mva 40,208,176
VC 155,138,139
TR 196,2,33792
Mva 56,208,180
ds_write_b128 v156, v[152:155] offset:21504
Mva 72,208,184
TR 198,2,34304
Mva 88,208,188
s_waitcnt lgkmcnt(7)
Mva 44,212,176
Mva 60,212,180
Mva 76,212,184
TR 200,3,32768
Mva 92,212,188
TR 202,3,33280
s_waitcnt lgkmcnt(3)
s_waitcnt vmcnt(4)
s_barrier
Mva 32,192,192
s_mov_b32 m0, s69
ds_read_b128 a[224:227], v157 offset:16384
Mva 48,192,196
s_mov_b32 s15, 4224
buffer_load_dwordx4 v1, s[16:19], s15 offen offset:0 lds
Mva 64,192,200
ds_read_b128 a[228:231], v157 offset:17408
Mva 80,192,204
TR 204,3,33792
v_mfma_f32_16x16x32_bf16 v[124:127], v[102:105], v[224:227], v[108:111]
s_mov_b32 s15, 3200
buffer_load_dwordx4 v1, s[20:23], s15 offen offset:1024 lds
v_mfma_f32_16x16x32_bf16 v[128:131], v[100:103], v[224:227], v[108:111]
TR 206,3,34304
s_waitcnt lgkmcnt(6)
Mva 36,196,192
ds_read_b128 a[232:235], v157 offset:18432
Mva 52,196,196
s_mov_b32 s15, 297088
buffer_load_dwordx4 v1, s[16:19], s15 offen offset:2048 lds
Mva 68,196,200
VC 140,124,125
VC 141,126,127
Mva 84,196,204
VC 142,128,129
VC 143,130,131
v_mfma_f32_16x16x32_bf16 v[132:135], v[102:105], v[228:231], v[112:115]
s_mov_b32 s15, 296064
buffer_load_dwordx4 v1, s[20:23], s15 offen offset:3072 lds
v_mfma_f32_16x16x32_bf16 v[136:139], v[100:103], v[228:231], v[112:115]
TR 208,2,34816
s_waitcnt lgkmcnt(6)
Mva 40,200,192
TR 210,2,35328
Mva 56,200,196
ds_write_b128 v156, v[140:143] offset:32768
Mva 72,200,200
ds_read_b128 a[236:239], v157 offset:19456
VC 144,132,133
Mva 88,200,204
VC 145,134,135
VC 146,136,137
s_waitcnt lgkmcnt(5)
Mva 44,204,192
VC 147,138,139
TR 212,2,35840
Mva 60,204,196
ds_read_b128 a[240:243], v157 offset:20480
Mva 76,204,200
TR 214,2,36352
Mva 92,204,204
ds_write_b128 v156, v[144:147] offset:33792
v_mfma_f32_16x16x32_bf16 v[124:127], v[102:105], v[240:243], v[108:111]
TR 192,3,34816
v_mfma_f32_16x16x32_bf16 v[128:131], v[100:103], v[240:243], v[108:111]
ds_read_b128 a[244:247], v157 offset:21504
s_waitcnt lgkmcnt(8)
Mva 32,208,208
TR 194,3,35328
Mva 48,208,212
TR 196,3,35840
Mva 64,208,216
ds_read_b128 a[248:251], v157 offset:22528
VC 148,124,125
Mva 80,208,220
TR 198,3,36352
VC 149,126,127
v_mfma_f32_16x16x32_bf16 v[132:135], v[102:105], v[244:247], v[112:115]
VC 150,128,129
VC 151,130,131
v_mfma_f32_16x16x32_bf16 v[136:139], v[100:103], v[244:247], v[112:115]
TR 200,2,49152
s_waitcnt lgkmcnt(8)
Mva 36,212,208
ds_write_b128 v156, v[148:151] offset:36864
Mva 52,212,212
TR 202,2,49664
Mva 68,212,216
ds_read_b128 a[252:255], v157 offset:23552
VC 152,132,133
Mva 84,212,220
VC 153,134,135
VC 154,136,137
s_waitcnt lgkmcnt(7)
Mva 40,192,208
VC 155,138,139
TR 204,2,50176
Mva 56,192,212
TR 206,2,50688
Mva 72,192,216
ds_write_b128 v156, v[152:155] offset:37888
Mva 88,192,220
TR 208,3,49152
s_waitcnt lgkmcnt(8)
Mva 44,196,208
TR 210,3,49664
Mva 60,196,212
Mva 76,196,216
TR 212,3,50176
Mva 92,196,220
TR 214,3,50688
s_waitcnt lgkmcnt(4)
s_waitcnt vmcnt(4)
s_barrier
Mva 32,200,224
s_mov_b32 m0, s70
ds_read_b128 v[224:227], v157 offset:32768
Mva 48,200,228
s_mov_b32 s15, 594048
buffer_load_dwordx4 v1, s[16:19], s15 offen offset:0 lds
Mva 64,200,232
ds_read_b128 v[228:231], v157 offset:33792
Mva 80,200,236
TR 192,2,51200
Mva 36,204,224
s_mov_b32 s15, 593024
buffer_load_dwordx4 v1, s[20:23], s15 offen offset:1024 lds
Mva 52,204,228
ds_read_b128 v[232:235], v157 offset:34816
Mva 68,204,232
TR 194,2,51712
Mva 84,204,236
s_mov_b32 s15, 886912
buffer_load_dwordx4 v1, s[16:19], s15 offen offset:2048 lds
s_waitcnt lgkmcnt(7)
Mva 40,208,224
TR 196,2,52224
Mva 56,208,228
ds_read_b128 v[236:239], v157 offset:35840
Mva 72,208,232
s_mov_b32 s15, 885888
buffer_load_dwordx4 v1, s[20:23], s15 offen offset:3072 lds
Mva 88,208,236
TR 198,2,52736
s_waitcnt lgkmcnt(8)
Mva 44,212,224
ds_read_b128 v[240:243], v157 offset:36864
Mva 60,212,228
TR 200,3,51200
Mva 76,212,232
TR 202,3,51712
Mva 92,212,236
ds_read_b128 v[244:247], v157 offset:37888
s_waitcnt lgkmcnt(7)
Mva 32,192,240
TR 204,3,52224
Mva 48,192,244
TR 206,3,52736
Mva 64,192,248
ds_read_b128 v[248:251], v157 offset:38912
Mva 80,192,252
TR 208,2,0
s_waitcnt lgkmcnt(8)
Mva 36,196,240
TR 210,2,512
Mva 52,196,244
ds_read_b128 v[252:255], v157 offset:39936
Mva 68,196,248
TR 212,2,1024
Mva 84,196,252
TR 214,2,1536
s_waitcnt lgkmcnt(9)
Mva 40,200,240
Mva 56,200,244
Mva 72,200,248
TR 192,3,0
Mva 88,200,252
TR 194,3,512
s_waitcnt lgkmcnt(8)
Mva 44,204,240
Mva 60,204,244
Mva 76,204,248
TR 196,3,1024
Mva 92,204,252
TR 198,3,1536
s_waitcnt vmcnt(4)
s_barrier
s_waitcnt lgkmcnt(7)
Mvv 32,208,224
s_mov_b32 m0, s71
Mvv 48,208,228
s_mov_b32 s15, 1183872
buffer_load_dwordx4 v1, s[16:19], s15 offen offset:0 lds
Mvv 64,208,232
TR 200,2,2048
Mvv 80,208,236
TR 202,2,2560
s_waitcnt lgkmcnt(6)
Mvv 36,212,224
s_mov_b32 s15, 1182848
buffer_load_dwordx4 v1, s[20:23], s15 offen offset:1024 lds
Mvv 52,212,228
Mvv 68,212,232
TR 204,2,3072
Mvv 84,212,236
s_mov_b32 s15, 1476736
buffer_load_dwordx4 v1, s[16:19], s15 offen offset:2048 lds
s_waitcnt lgkmcnt(5)
Mvv 40,192,224
TR 206,2,3584
Mvv 56,192,228
Mvv 72,192,232
s_mov_b32 s15, 1475712
buffer_load_dwordx4 v1, s[20:23], s15 offen offset:3072 lds
Mvv 88,192,236
TR 208,3,2048
s_waitcnt lgkmcnt(5)
Mvv 44,196,224
TR 210,3,2560
Mvv 60,196,228
Mvv 76,196,232
TR 212,3,3072
Mvv 92,196,236
TR 214,3,3584
s_waitcnt lgkmcnt(6)
Mvv 32,200,240
Mvv 48,200,244
Mvv 64,200,248
TR 192,2,16384
Mvv 80,200,252
TR 194,2,16896
)"
R"(s_waitcnt lgkmcnt(6)
Mvv 36,204,240
Mvv 52,204,244
Mvv 68,204,248
TR 196,2,17408
Mvv 84,204,252
TR 198,2,17920
s_waitcnt lgkmcnt(6)
Mvv 40,208,240
Mvv 56,208,244
Mvv 72,208,248
TR 200,3,16384
Mvv 88,208,252
TR 202,3,16896
s_waitcnt lgkmcnt(6)
Mvv 44,212,240
Mvv 60,212,244
Mvv 76,212,248
TR 204,3,17408
Mvv 92,212,252
TR 206,3,17920
s_waitcnt vmcnt(4)
s_barrier
s_waitcnt lgkmcnt(6)
Zva 96,192,0
s_mov_b32 m0, s68
Zva 112,192,4
s_mov_b32 s15, 1773696
buffer_load_dwordx4 v1, s[16:19], s15 offen offset:0 lds
Zva 128,192,8
VM 36,32,36
VM 37,33,37
Zva 144,192,12
VM 38,34,38
VM 32,6,32
s_waitcnt lgkmcnt(4)
Zva 100,196,0
s_mov_b32 s15, 1772672
buffer_load_dwordx4 v1, s[20:23], s15 offen offset:1024 lds
Zva 116,196,4
VM 39,35,39
VM 33,6,33
Zva 132,196,8
TR 208,2,18432
VM 44,40,44
Zva 148,196,12
s_mov_b32 s15, 2066560
buffer_load_dwordx4 v1, s[16:19], s15 offen offset:2048 lds
s_waitcnt lgkmcnt(3)
Zva 104,200,0
TR 210,2,18944
VM 34,6,34
Zva 120,200,4
v_exp_f32 v32, v32
Zva 136,200,8
s_mov_b32 s15, 2065536
buffer_load_dwordx4 v1, s[20:23], s15 offen offset:3072 lds
Zva 152,200,12
TR 212,2,19456
VM 45,41,45
s_waitcnt lgkmcnt(3)
Zva 108,204,0
TR 214,2,19968
VM 35,6,35
Zva 124,204,4
v_exp_f32 v33, v33
Zva 140,204,8
TR 192,3,18432
VM 46,42,46
Zva 156,204,12
TR 194,3,18944
VM 40,6,40
s_waitcnt lgkmcnt(4)
Mva 96,208,16
v_exp_f32 v34, v34
Mva 112,208,20
TR 196,3,19456
VF 32,32,10,10
Mva 128,208,24
TR 198,3,19968
VM 47,43,47
Mva 144,208,28
VM 41,6,41
TR 200,2,32768
s_waitcnt lgkmcnt(5)
Mva 100,212,16
v_exp_f32 v35, v35
Mva 116,212,20
TR 202,2,33280
VF 33,33,10,10
Mva 132,212,24
VM 42,6,42
TR 204,2,33792
Mva 148,212,28
v_exp_f32 v40, v40
s_waitcnt lgkmcnt(5)
Mva 104,192,16
TR 206,2,34304
VF 34,34,10,10
Mva 120,192,20
v_rcp_f32 v32, v32
Mva 136,192,24
TR 208,3,32768
VM 43,6,43
Mva 152,192,28
TR 210,3,33280
s_waitcnt lgkmcnt(6)
Mva 108,196,16
v_exp_f32 v41, v41
Mva 124,196,20
VF 35,35,10,10
Mva 140,196,24
TR 212,3,33792
Mva 156,196,28
TR 214,3,34304
s_waitcnt vmcnt(4)
s_barrier
s_waitcnt lgkmcnt(6)
Mva 96,200,32
s_mov_b32 m0, s69
Mva 112,200,36
s_mov_b32 s15, 2363520
buffer_load_dwordx4 v1, s[16:19], s15 offen offset:0 lds
Mva 128,200,40
v_rcp_f32 v33, v33
Mva 144,200,44
v_exp_f32 v42, v42
s_waitcnt lgkmcnt(4)
Mva 100,204,32
s_mov_b32 s15, 2362496
buffer_load_dwordx4 v1, s[20:23], s15 offen offset:1024 lds
Mva 116,204,36
TR 192,2,34816
VF 40,40,10,10
Mva 132,204,40
TR 194,2,35328
Mva 148,204,44
s_mov_b32 s15, 2656384
buffer_load_dwordx4 v1, s[16:19], s15 offen offset:2048 lds
s_waitcnt lgkmcnt(4)
Mva 104,208,32
v_rcp_f32 v34, v34
Mva 120,208,36
VM 36,36,32
TR 196,2,35840
Mva 136,208,40
s_mov_b32 s15, 2655360
buffer_load_dwordx4 v1, s[20:23], s15 offen offset:3072 lds
Mva 152,208,44
TR 198,2,36352
s_waitcnt lgkmcnt(4)
Mva 108,212,32
v_exp_f32 v43, v43
Mva 124,212,36
VF 41,41,10,10
TR 200,3,34816
Mva 140,212,40
v_rcp_f32 v35, v35
Mva 156,212,44
TR 202,3,35328
VM 37,37,33
s_waitcnt lgkmcnt(4)
Mva 96,192,48
VF 42,42,10,10
TR 204,3,35840
Mva 112,192,52
v_rcp_f32 v40, v40
Mva 128,192,56
TR 206,3,36352
VM 38,38,34
Mva 144,192,60
VF 43,43,10,10
TR 208,2,49152
s_waitcnt lgkmcnt(5)
Mva 100,196,48
v_rcp_f32 v41, v41
Mva 116,196,52
TR 210,2,49664
VM 39,39,35
Mva 132,196,56
v_rcp_f32 v42, v42
Mva 148,196,60
TR 212,2,50176
VM 44,44,40
s_waitcnt lgkmcnt(5)
Mva 104,200,48
TR 214,2,50688
Mva 120,200,52
v_rcp_f32 v43, v43
Mva 136,200,56
VM 45,45,41
TR 192,3,49152
Mva 152,200,60
VM 46,46,42
TR 194,3,49664
s_waitcnt lgkmcnt(6)
Mva 108,204,48
VM 47,47,43
VC 32,36,37
Mva 124,204,52
VC 33,38,39
VC 34,44,45
Mva 140,204,56
TR 196,3,50176
VC 35,46,47
Mva 156,204,60
TR 198,3,50688
s_waitcnt vmcnt(4)
s_barrier
s_waitcnt lgkmcnt(6)
Mva 96,208,64
s_mov_b32 m0, s70
Mva 112,208,68
s_mov_b32 s15, 2953344
buffer_load_dwordx4 v1, s[16:19], s15 offen offset:0 lds
Mva 128,208,72
TR 200,2,51200
Mva 144,208,76
ds_write_b128 v5, v[32:35] offset:0
s_waitcnt lgkmcnt(6)
Mva 100,212,64
s_mov_b32 s15, 2952320
buffer_load_dwordx4 v1, s[20:23], s15 offen offset:1024 lds
Mva 116,212,68
TR 202,2,51712
VM 52,48,52
Mva 132,212,72
VM 53,49,53
VM 54,50,54
Mva 148,212,76
s_mov_b32 s15, 3246208
buffer_load_dwordx4 v1, s[16:19], s15 offen offset:2048 lds
s_waitcnt lgkmcnt(5)
Mva 104,192,64
TR 204,2,52224
VM 48,7,48
Mva 120,192,68
TR 206,2,52736
VM 55,51,55
Mva 136,192,72
s_mov_b32 s15, 3245184
buffer_load_dwordx4 v1, s[20:23], s15 offen offset:3072 lds
Mva 152,192,76
VM 49,7,49
VM 60,56,60
s_waitcnt lgkmcnt(5)
Mva 108,196,64
TR 208,3,51200
VM 50,7,50
Mva 124,196,68
TR 210,3,51712
Mva 140,196,72
v_exp_f32 v48, v48
Mva 156,196,76
VM 61,57,61
TR 212,3,52224
s_waitcnt lgkmcnt(5)
Mva 96,200,80
VM 51,7,51
TR 214,3,52736
Mva 112,200,84
v_exp_f32 v49, v49
Mva 128,200,88
VM 62,58,62
TR 192,2,0
Mva 144,200,92
VM 56,7,56
TR 194,2,512
s_waitcnt lgkmcnt(6)
Mva 100,204,80
v_exp_f32 v50, v50
Mva 116,204,84
VF 48,48,11,11
VM 63,59,63
Mva 132,204,88
TR 196,2,1024
VM 57,7,57
Mva 148,204,92
TR 198,2,1536
s_waitcnt lgkmcnt(6)
Mva 104,208,80
v_exp_f32 v51, v51
Mva 120,208,84
VF 49,49,11,11
VM 58,7,58
Mva 136,208,88
TR 200,3,0
Mva 152,208,92
TR 202,3,512
s_waitcnt lgkmcnt(6)
Mva 108,212,80
v_exp_f32 v56, v56
Mva 124,212,84
VF 50,50,11,11
Mva 140,212,88
TR 204,3,1024
Mva 156,212,92
TR 206,3,1536
s_waitcnt vmcnt(4)
s_barrier
s_waitcnt lgkmcnt(6)
Mva 96,192,96
s_mov_b32 m0, s71
Mva 112,192,100
s_mov_b32 s15, 3543168
buffer_load_dwordx4 v1, s[16:19], s15 offen offset:0 lds
Mva 128,192,104
v_rcp_f32 v48, v48
Mva 144,192,108
VM 59,7,59
TR 208,2,2048
s_waitcnt lgkmcnt(5)
Mva 100,196,96
s_mov_b32 s15, 3542144
buffer_load_dwordx4 v1, s[20:23], s15 offen offset:1024 lds
Mva 116,196,100
TR 210,2,2560
Mva 132,196,104
v_exp_f32 v57, v57
Mva 148,196,108
s_mov_b32 s15, 3836032
buffer_load_dwordx4 v1, s[16:19], s15 offen offset:2048 lds
s_waitcnt lgkmcnt(4)
Mva 104,200,96
VF 51,51,11,11
TR 212,2,3072
Mva 120,200,100
v_rcp_f32 v49, v49
Mva 136,200,104
s_mov_b32 s15, 3835008
buffer_load_dwordx4 v1, s[20:23], s15 offen offset:3072 lds
Mva 152,200,108
TR 214,2,3584
s_waitcnt lgkmcnt(4)
Mva 108,204,96
v_exp_f32 v58, v58
Mva 124,204,100
VF 56,56,11,11
TR 192,3,2048
Mva 140,204,104
TR 194,3,2560
Mva 156,204,108
v_rcp_f32 v50, v50
s_waitcnt lgkmcnt(4)
Mva 96,208,112
VM 52,52,48
TR 196,3,3072
Mva 112,208,116
v_exp_f32 v59, v59
Mva 128,208,120
TR 198,3,3584
VF 57,57,11,11
Mva 144,208,124
v_rcp_f32 v51, v51
s_waitcnt lgkmcnt(4)
Mva 100,212,112
TR 200,2,16384
VM 53,53,49
Mva 116,212,116
TR 202,2,16896
VF 58,58,11,11
Mva 132,212,120
v_rcp_f32 v56, v56
Mva 148,212,124
TR 204,2,17408
VM 54,54,50
s_waitcnt lgkmcnt(5)
Mva 104,192,112
TR 206,2,17920
VF 59,59,11,11
Mva 120,192,116
v_rcp_f32 v57, v57
Mva 136,192,120
TR 208,3,16384
VM 55,55,51
Mva 152,192,124
TR 210,3,16896
s_waitcnt lgkmcnt(6)
Mva 108,196,112
v_rcp_f32 v58, v58
Mva 124,196,116
VM 60,60,56
Mva 140,196,120
TR 212,3,17408
Mva 156,196,124
TR 214,3,17920
s_waitcnt vmcnt(4)
s_barrier
s_waitcnt lgkmcnt(6)
Mva 96,200,128
s_mov_b32 m0, s68
Mva 112,200,132
s_mov_b32 s15, 4132992
buffer_load_dwordx4 v1, s[16:19], s15 offen offset:0 lds
Mva 128,200,136
v_rcp_f32 v59, v59
Mva 144,200,140
VM 61,61,57
VM 62,62,58
s_waitcnt lgkmcnt(4)
Mva 100,204,128
s_mov_b32 s15, 4131968
buffer_load_dwordx4 v1, s[20:23], s15 offen offset:1024 lds
Mva 116,204,132
TR 192,2,18432
VM 63,63,59
Mva 132,204,136
TR 194,2,18944
VC 48,52,53
Mva 148,204,140
s_mov_b32 s15, 4425856
buffer_load_dwordx4 v1, s[16:19], s15 offen offset:2048 lds
s_waitcnt lgkmcnt(4)
Mva 104,208,128
VC 49,54,55
VC 50,60,61
Mva 120,208,132
TR 196,2,19456
VC 51,62,63
Mva 136,208,136
s_mov_b32 s15, 4424832
buffer_load_dwordx4 v1, s[20:23], s15 offen offset:3072 lds
Mva 152,208,140
TR 198,2,19968
s_waitcnt lgkmcnt(4)
Mva 108,212,128
ds_write_b128 v5, v[48:51] offset:1024
Mva 124,212,132
TR 200,3,18432
VM 68,64,68
Mva 140,212,136
TR 202,3,18944
VM 69,65,69
Mva 156,212,140
VM 70,66,70
VM 64,8,64
s_waitcnt lgkmcnt(5)
Mva 96,192,144
TR 204,3,19456
VM 71,67,71
Mva 112,192,148
TR 206,3,19968
VM 65,8,65
Mva 128,192,152
VM 76,72,76
VM 66,8,66
Mva 144,192,156
TR 208,2,32768
s_waitcnt lgkmcnt(6)
Mva 100,196,144
TR 210,2,33280
Mva 116,196,148
v_exp_f32 v64, v64
Mva 132,196,152
VM 77,73,77
VM 67,8,67
Mva 148,196,156
TR 212,2,33792
s_waitcnt lgkmcnt(5)
Mva 104,200,144
TR 214,2,34304
Mva 120,200,148
v_exp_f32 v65, v65
Mva 136,200,152
VM 78,74,78
VM 72,8,72
Mva 152,200,156
TR 192,3,32768
s_waitcnt lgkmcnt(5)
Mva 108,204,144
TR 194,3,33280
Mva 124,204,148
v_exp_f32 v66, v66
Mva 140,204,152
VF 64,64,12,12
VM 79,75,79
Mva 156,204,156
TR 196,3,33792
VM 73,8,73
s_waitcnt vmcnt(4)
s_barrier
s_waitcnt lgkmcnt(5)
Mva 96,208,160
s_mov_b32 m0, s69
TR 198,3,34304
Mva 112,208,164
s_mov_b32 s15, 4722816
buffer_load_dwordx4 v1, s[16:19], s15 offen offset:0 lds
Mva 128,208,168
v_exp_f32 v67, v67
Mva 144,208,172
VF 65,65,12,12
TR 200,2,34816
s_waitcnt lgkmcnt(5)
Mva 100,212,160
s_mov_b32 s15, 4721792
buffer_load_dwordx4 v1, s[20:23], s15 offen offset:1024 lds
Mva 116,212,164
VM 74,8,74
TR 202,2,35328
Mva 132,212,168
v_exp_f32 v72, v72
Mva 148,212,172
s_mov_b32 s15, 5015680
buffer_load_dwordx4 v1, s[16:19], s15 offen offset:2048 lds
s_waitcnt lgkmcnt(4)
Mva 104,192,160
VF 66,66,12,12
TR 204,2,35840
Mva 120,192,164
TR 206,2,36352
Mva 136,192,168
s_mov_b32 s15, 5014656
buffer_load_dwordx4 v1, s[20:23], s15 offen offset:3072 lds
Mva 152,192,172
v_rcp_f32 v64, v64
s_waitcnt lgkmcnt(4)
Mva 108,196,160
VM 75,8,75
TR 208,3,34816
Mva 124,196,164
v_exp_f32 v73, v73
Mva 140,196,168
TR 210,3,35328
VF 67,67,12,12
Mva 156,196,172
v_rcp_f32 v65, v65
s_waitcnt lgkmcnt(4)
Mva 96,200,176
TR 212,3,35840
Mva 112,200,180
TR 214,3,36352
Mva 128,200,184
v_exp_f32 v74, v74
Mva 144,200,188
VF 72,72,12,12
TR 192,2,49152
s_waitcnt lgkmcnt(5)
Mva 100,204,176
v_rcp_f32 v66, v66
Mva 116,204,180
TR 194,2,49664
VM 68,68,64
Mva 132,204,184
v_exp_f32 v75, v75
Mva 148,204,188
TR 196,2,50176
VF 73,73,12,12
s_waitcnt lgkmcnt(5)
Mva 104,208,176
TR 198,2,50688
Mva 120,208,180
v_rcp_f32 v67, v67
Mva 136,208,184
VM 69,69,65
TR 200,3,49152
Mva 152,208,188
VF 74,74,12,12
TR 202,3,49664
s_waitcnt lgkmcnt(6)
Mva 108,212,176
v_rcp_f32 v72, v72
Mva 124,212,180
VM 70,70,66
VF 75,75,12,12
Mva 140,212,184
TR 204,3,50176
Mva 156,212,188
TR 206,3,50688
s_waitcnt vmcnt(4)
s_barrier
s_waitcnt lgkmcnt(6)
Mva 96,192,192
s_mov_b32 m0, s70
Mva 112,192,196
s_mov_b32 s15, 4352
buffer_load_dwordx4 v1, s[16:19], s15 offen offset:0 lds
Mva 128,192,200
v_rcp_f32 v73, v73
Mva 144,192,204
VM 71,71,67
TR 208,2,51200
s_waitcnt lgkmcnt(5)
Mva 100,196,192
s_mov_b32 s15, 3328
buffer_load_dwordx4 v1, s[20:23], s15 offen offset:1024 lds
Mva 116,196,196
TR 210,2,51712
Mva 132,196,200
v_rcp_f32 v74, v74
Mva 148,196,204
s_mov_b32 s15, 297216
buffer_load_dwordx4 v1, s[16:19], s15 offen offset:2048 lds
s_waitcnt lgkmcnt(4)
Mva 104,200,192
VM 76,76,72
TR 212,2,52224
Mva 120,200,196
v_rcp_f32 v75, v75
Mva 136,200,200
)"
R"(s_mov_b32 s15, 296192
buffer_load_dwordx4 v1, s[20:23], s15 offen offset:3072 lds
Mva 152,200,204
TR 214,2,52736
VM 77,77,73
s_waitcnt lgkmcnt(4)
Mva 108,204,192
VM 78,78,74
VM 79,79,75
Mva 124,204,196
TR 192,3,51200
VC 64,68,69
Mva 140,204,200
TR 194,3,51712
VC 65,70,71
Mva 156,204,204
VC 66,76,77
VC 67,78,79
s_waitcnt lgkmcnt(4)
Mva 96,208,208
TR 196,3,52224
Mva 112,208,212
TR 198,3,52736
Mva 128,208,216
ds_write_b128 v5, v[64:67] offset:2048
Mva 144,208,220
VM 84,80,84
TR 200,2,0
s_waitcnt lgkmcnt(6)
Mva 100,212,208
VM 85,81,85
TR 202,2,512
Mva 116,212,212
VM 86,82,86
VM 80,9,80
Mva 132,212,216
VM 87,83,87
TR 204,2,1024
Mva 148,212,220
VM 81,9,81
TR 206,2,1536
s_waitcnt lgkmcnt(7)
Mva 104,192,208
VM 92,88,92
VM 82,9,82
Mva 120,192,212
v_exp_f32 v80, v80
Mva 136,192,216
TR 208,3,0
VM 93,89,93
Mva 152,192,220
TR 210,3,512
VM 83,9,83
s_waitcnt lgkmcnt(7)
Mva 108,196,208
v_exp_f32 v81, v81
Mva 124,196,212
VM 94,90,94
VM 88,9,88
Mva 140,196,216
TR 212,3,1024
Mva 156,196,220
TR 214,3,1536
s_waitcnt vmcnt(4)
s_barrier
s_waitcnt lgkmcnt(6)
Mva 96,200,224
s_mov_b32 m0, s71
Mva 112,200,228
s_mov_b32 s15, 594176
buffer_load_dwordx4 v1, s[16:19], s15 offen offset:0 lds
Mva 128,200,232
TR 192,2,2048
Mva 144,200,236
v_exp_f32 v82, v82
s_waitcnt lgkmcnt(5)
Mva 100,204,224
s_mov_b32 s15, 593152
buffer_load_dwordx4 v1, s[20:23], s15 offen offset:1024 lds
Mva 116,204,228
TR 194,2,2560
VF 80,80,13,13
Mva 132,204,232
VM 95,91,95
VM 89,9,89
Mva 148,204,236
s_mov_b32 s15, 887040
buffer_load_dwordx4 v1, s[16:19], s15 offen offset:2048 lds
s_waitcnt lgkmcnt(4)
Mva 104,208,224
TR 196,2,3072
Mva 120,208,228
TR 198,2,3584
Mva 136,208,232
s_mov_b32 s15, 886016
buffer_load_dwordx4 v1, s[20:23], s15 offen offset:3072 lds
Mva 152,208,236
v_exp_f32 v83, v83
s_waitcnt lgkmcnt(4)
Mva 108,212,224
VF 81,81,13,13
VM 90,9,90
Mva 124,212,228
TR 200,3,2048
Mva 140,212,232
TR 202,3,2560
Mva 156,212,236
v_exp_f32 v88, v88
s_waitcnt lgkmcnt(4)
Mva 96,192,240
VF 82,82,13,13
TR 204,3,3072
Mva 112,192,244
v_rcp_f32 v80, v80
Mva 128,192,248
TR 206,3,3584
VM 91,9,91
Mva 144,192,252
v_exp_f32 v89, v89
s_waitcnt lgkmcnt(4)
Mva 100,196,240
TR 208,2,16384
VF 83,83,13,13
Mva 116,196,244
TR 210,2,16896
Mva 132,196,248
v_rcp_f32 v81, v81
Mva 148,196,252
TR 212,2,17408
s_waitcnt lgkmcnt(5)
Mva 104,200,240
v_exp_f32 v90, v90
Mva 120,200,244
TR 214,2,17920
VF 88,88,13,13
Mva 136,200,248
v_rcp_f32 v82, v82
Mva 152,200,252
TR 192,3,16384
VM 84,84,80
s_waitcnt lgkmcnt(5)
Mva 108,204,240
TR 194,3,16896
Mva 124,204,244
v_exp_f32 v91, v91
Mva 140,204,248
VF 89,89,13,13
TR 196,3,17408
Mva 156,204,252
TR 198,3,17920
s_waitcnt vmcnt(4)
s_barrier
s_waitcnt lgkmcnt(6)
Mvv 96,208,224
s_mov_b32 m0, s68
Mvv 112,208,228
s_mov_b32 s15, 1184000
buffer_load_dwordx4 v1, s[16:19], s15 offen offset:0 lds
Mvv 128,208,232
v_rcp_f32 v83, v83
Mvv 144,208,236
VM 85,85,81
VF 90,90,13,13
s_waitcnt lgkmcnt(4)
Mvv 100,212,224
s_mov_b32 s15, 1182976
buffer_load_dwordx4 v1, s[20:23], s15 offen offset:1024 lds
Mvv 116,212,228
TR 200,2,18432
Mvv 132,212,232
TR 202,2,18944
Mvv 148,212,236
s_mov_b32 s15, 1476864
buffer_load_dwordx4 v1, s[16:19], s15 offen offset:2048 lds
s_waitcnt lgkmcnt(4)
Mvv 104,192,224
v_rcp_f32 v88, v88
Mvv 120,192,228
VM 86,86,82
VF 91,91,13,13
Mvv 136,192,232
s_mov_b32 s15, 1475840
buffer_load_dwordx4 v1, s[20:23], s15 offen offset:3072 lds
Mvv 152,192,236
TR 204,2,19456
s_waitcnt lgkmcnt(3)
Mvv 108,196,224
TR 206,2,19968
Mvv 124,196,228
v_rcp_f32 v89, v89
Mvv 140,196,232
VM 87,87,83
TR 208,3,18432
Mvv 156,196,236
v_rcp_f32 v90, v90
s_waitcnt lgkmcnt(3)
Mvv 96,200,240
TR 210,3,18944
VM 92,92,88
Mvv 112,200,244
v_rcp_f32 v91, v91
Mvv 128,200,248
TR 212,3,19456
VM 93,93,89
Mvv 144,200,252
TR 214,3,19968
VM 94,94,90
s_waitcnt lgkmcnt(4)
Mvv 100,204,240
VM 95,95,91
VC 80,84,85
Mvv 116,204,244
TR 192,2,32768
VC 81,86,87
Mvv 132,204,248
TR 194,2,33280
VC 82,92,93
Mvv 148,204,252
VC 83,94,95
TR 196,2,33792
s_waitcnt lgkmcnt(5)
Mvv 104,208,240
TR 198,2,34304
Mvv 120,208,244
ds_write_b128 v5, v[80:83] offset:3072
Mvv 136,208,248
TR 200,3,32768
Mvv 152,208,252
TR 202,3,33280
s_waitcnt lgkmcnt(7)
Mvv 108,212,240
Mvv 124,212,244
Mvv 140,212,248
TR 204,3,33792
Mvv 156,212,252
TR 206,3,34304
s_waitcnt lgkmcnt(4)
s_waitcnt vmcnt(4)
s_barrier
Zva 32,192,0
s_mov_b32 m0, s69
Zva 48,192,4
s_mov_b32 s15, 1773824
buffer_load_dwordx4 v1, s[16:19], s15 offen offset:0 lds
Zva 64,192,8
VM 100,96,100
VM 101,97,101
Zva 80,192,12
VM 102,98,102
VM 96,6,96
Zva 36,196,0
s_mov_b32 s15, 1772800
buffer_load_dwordx4 v1, s[20:23], s15 offen offset:1024 lds
Zva 52,196,4
VM 103,99,103
VM 97,6,97
Zva 68,196,8
TR 208,2,34816
VM 108,104,108
Zva 84,196,12
s_mov_b32 s15, 2066688
buffer_load_dwordx4 v1, s[16:19], s15 offen offset:2048 lds
s_waitcnt lgkmcnt(3)
Zva 40,200,0
TR 210,2,35328
VM 98,6,98
Zva 56,200,4
v_exp_f32 v96, v96
Zva 72,200,8
s_mov_b32 s15, 2065664
buffer_load_dwordx4 v1, s[20:23], s15 offen offset:3072 lds
Zva 88,200,12
TR 212,2,35840
VM 109,105,109
s_waitcnt lgkmcnt(3)
Zva 44,204,0
TR 214,2,36352
VM 99,6,99
Zva 60,204,4
v_exp_f32 v97, v97
Zva 76,204,8
TR 192,3,34816
VM 110,106,110
Zva 92,204,12
TR 194,3,35328
VM 104,6,104
s_waitcnt lgkmcnt(4)
Mva 32,208,16
v_exp_f32 v98, v98
Mva 48,208,20
TR 196,3,35840
VF 96,96,10,10
Mva 64,208,24
TR 198,3,36352
VM 111,107,111
Mva 80,208,28
VM 105,6,105
TR 200,2,49152
s_waitcnt lgkmcnt(5)
Mva 36,212,16
v_exp_f32 v99, v99
Mva 52,212,20
TR 202,2,49664
VF 97,97,10,10
Mva 68,212,24
VM 106,6,106
TR 204,2,50176
Mva 84,212,28
v_exp_f32 v104, v104
s_waitcnt lgkmcnt(5)
Mva 40,192,16
TR 206,2,50688
VF 98,98,10,10
Mva 56,192,20
v_rcp_f32 v96, v96
Mva 72,192,24
TR 208,3,49152
VM 107,6,107
Mva 88,192,28
TR 210,3,49664
s_waitcnt lgkmcnt(6)
Mva 44,196,16
v_exp_f32 v105, v105
Mva 60,196,20
VF 99,99,10,10
Mva 76,196,24
TR 212,3,50176
Mva 92,196,28
TR 214,3,50688
s_waitcnt vmcnt(4)
s_barrier
s_waitcnt lgkmcnt(6)
Mva 32,200,32
s_mov_b32 m0, s70
Mva 48,200,36
s_mov_b32 s15, 2363648
buffer_load_dwordx4 v1, s[16:19], s15 offen offset:0 lds
Mva 64,200,40
v_rcp_f32 v97, v97
Mva 80,200,44
v_exp_f32 v106, v106
s_waitcnt lgkmcnt(4)
Mva 36,204,32
s_mov_b32 s15, 2362624
buffer_load_dwordx4 v1, s[20:23], s15 offen offset:1024 lds
Mva 52,204,36
TR 192,2,51200
VF 104,104,10,10
Mva 68,204,40
TR 194,2,51712
Mva 84,204,44
s_mov_b32 s15, 2656512
buffer_load_dwordx4 v1, s[16:19], s15 offen offset:2048 lds
s_waitcnt lgkmcnt(4)
Mva 40,208,32
v_rcp_f32 v98, v98
Mva 56,208,36
VM 100,100,96
TR 196,2,52224
Mva 72,208,40
s_mov_b32 s15, 2655488
buffer_load_dwordx4 v1, s[20:23], s15 offen offset:3072 lds
Mva 88,208,44
TR 198,2,52736
s_waitcnt lgkmcnt(4)
Mva 44,212,32
v_exp_f32 v107, v107
Mva 60,212,36
VF 105,105,10,10
TR 200,3,51200
Mva 76,212,40
v_rcp_f32 v99, v99
Mva 92,212,44
TR 202,3,51712
VM 101,101,97
s_waitcnt lgkmcnt(4)
Mva 32,192,48
VF 106,106,10,10
TR 204,3,52224
Mva 48,192,52
v_rcp_f32 v104, v104
Mva 64,192,56
TR 206,3,52736
VM 102,102,98
Mva 80,192,60
VF 107,107,10,10
TR 208,2,0
s_waitcnt lgkmcnt(5)
Mva 36,196,48
v_rcp_f32 v105, v105
Mva 52,196,52
TR 210,2,512
VM 103,103,99
Mva 68,196,56
v_rcp_f32 v106, v106
Mva 84,196,60
TR 212,2,1024
VM 108,108,104
s_waitcnt lgkmcnt(5)
Mva 40,200,48
TR 214,2,1536
Mva 56,200,52
v_rcp_f32 v107, v107
Mva 72,200,56
VM 109,109,105
TR 192,3,0
Mva 88,200,60
VM 110,110,106
TR 194,3,512
s_waitcnt lgkmcnt(6)
Mva 44,204,48
VM 111,111,107
VC 96,100,101
Mva 60,204,52
VC 97,102,103
VC 98,108,109
Mva 76,204,56
TR 196,3,1024
VC 99,110,111
Mva 92,204,60
TR 198,3,1536
s_waitcnt vmcnt(4)
s_barrier
s_waitcnt lgkmcnt(6)
Mva 32,208,64
s_mov_b32 m0, s71
Mva 48,208,68
s_mov_b32 s15, 2953472
buffer_load_dwordx4 v1, s[16:19], s15 offen offset:0 lds
Mva 64,208,72
TR 200,2,2048
Mva 80,208,76
ds_write_b128 v5, v[96:99] offset:16384
s_waitcnt lgkmcnt(6)
Mva 36,212,64
s_mov_b32 s15, 2952448
buffer_load_dwordx4 v1, s[20:23], s15 offen offset:1024 lds
Mva 52,212,68
TR 202,2,2560
VM 116,112,116
Mva 68,212,72
VM 117,113,117
VM 118,114,118
Mva 84,212,76
s_mov_b32 s15, 3246336
buffer_load_dwordx4 v1, s[16:19], s15 offen offset:2048 lds
s_waitcnt lgkmcnt(5)
Mva 40,192,64
TR 204,2,3072
VM 112,7,112
Mva 56,192,68
TR 206,2,3584
VM 119,115,119
Mva 72,192,72
s_mov_b32 s15, 3245312
buffer_load_dwordx4 v1, s[20:23], s15 offen offset:3072 lds
Mva 88,192,76
VM 113,7,113
VM 124,120,124
s_waitcnt lgkmcnt(5)
Mva 44,196,64
TR 208,3,2048
VM 114,7,114
Mva 60,196,68
TR 210,3,2560
Mva 76,196,72
v_exp_f32 v112, v112
Mva 92,196,76
VM 125,121,125
TR 212,3,3072
s_waitcnt lgkmcnt(5)
Mva 32,200,80
VM 115,7,115
TR 214,3,3584
Mva 48,200,84
v_exp_f32 v113, v113
Mva 64,200,88
VM 126,122,126
TR 192,2,16384
Mva 80,200,92
VM 120,7,120
TR 194,2,16896
s_waitcnt lgkmcnt(6)
Mva 36,204,80
v_exp_f32 v114, v114
Mva 52,204,84
VF 112,112,11,11
VM 127,123,127
Mva 68,204,88
TR 196,2,17408
VM 121,7,121
Mva 84,204,92
TR 198,2,17920
s_waitcnt lgkmcnt(6)
Mva 40,208,80
v_exp_f32 v115, v115
Mva 56,208,84
VF 113,113,11,11
VM 122,7,122
Mva 72,208,88
TR 200,3,16384
Mva 88,208,92
TR 202,3,16896
s_waitcnt lgkmcnt(6)
Mva 44,212,80
v_exp_f32 v120, v120
Mva 60,212,84
VF 114,114,11,11
Mva 76,212,88
TR 204,3,17408
Mva 92,212,92
TR 206,3,17920
s_waitcnt vmcnt(4)
s_barrier
s_waitcnt lgkmcnt(6)
Mva 32,192,96
s_mov_b32 m0, s68
Mva 48,192,100
s_mov_b32 s15, 3543296
buffer_load_dwordx4 v1, s[16:19], s15 offen offset:0 lds
Mva 64,192,104
v_rcp_f32 v112, v112
Mva 80,192,108
VM 123,7,123
TR 208,2,18432
s_waitcnt lgkmcnt(5)
Mva 36,196,96
s_mov_b32 s15, 3542272
buffer_load_dwordx4 v1, s[20:23], s15 offen offset:1024 lds
Mva 52,196,100
TR 210,2,18944
Mva 68,196,104
v_exp_f32 v121, v121
Mva 84,196,108
s_mov_b32 s15, 3836160
buffer_load_dwordx4 v1, s[16:19], s15 offen offset:2048 lds
s_waitcnt lgkmcnt(4)
Mva 40,200,96
VF 115,115,11,11
TR 212,2,19456
Mva 56,200,100
v_rcp_f32 v113, v113
Mva 72,200,104
s_mov_b32 s15, 3835136
buffer_load_dwordx4 v1, s[20:23], s15 offen offset:3072 lds
Mva 88,200,108
TR 214,2,19968
s_waitcnt lgkmcnt(4)
Mva 44,204,96
v_exp_f32 v122, v122
Mva 60,204,100
VF 120,120,11,11
TR 192,3,18432
Mva 76,204,104
TR 194,3,18944
Mva 92,204,108
v_rcp_f32 v114, v114
s_waitcnt lgkmcnt(4)
Mva 32,208,112
VM 116,116,112
TR 196,3,19456
Mva 48,208,116
v_exp_f32 v123, v123
Mva 64,208,120
TR 198,3,19968
VF 121,121,11,11
Mva 80,208,124
v_rcp_f32 v115, v115
s_waitcnt lgkmcnt(4)
Mva 36,212,112
TR 200,2,32768
VM 117,117,113
Mva 52,212,116
TR 202,2,33280
VF 122,122,11,11
Mva 68,212,120
v_rcp_f32 v120, v120
Mva 84,212,124
TR 204,2,33792
VM 118,118,114
s_waitcnt lgkmcnt(5)
Mva 40,192,112
TR 206,2,34304
VF 123,123,11,11
Mva 56,192,116
v_rcp_f32 v121, v121
Mva 72,192,120
TR 208,3,32768
VM 119,119,115
Mva 88,192,124
TR 210,3,33280
s_waitcnt lgkmcnt(6)
Mva 44,196,112
v_rcp_f32 v122, v122
Mva 60,196,116
VM 124,124,120
Mva 76,196,120
TR 212,3,33792
Mva 92,196,124
TR 214,3,34304
)"
R"(s_waitcnt vmcnt(4)
s_barrier
s_waitcnt lgkmcnt(6)
Mva 32,200,128
s_mov_b32 m0, s69
Mva 48,200,132
s_mov_b32 s15, 4133120
buffer_load_dwordx4 v1, s[16:19], s15 offen offset:0 lds
Mva 64,200,136
v_rcp_f32 v123, v123
Mva 80,200,140
VM 125,125,121
VM 126,126,122
s_waitcnt lgkmcnt(4)
Mva 36,204,128
s_mov_b32 s15, 4132096
buffer_load_dwordx4 v1, s[20:23], s15 offen offset:1024 lds
Mva 52,204,132
TR 192,2,34816
VM 127,127,123
Mva 68,204,136
TR 194,2,35328
VC 112,116,117
Mva 84,204,140
s_mov_b32 s15, 4425984
buffer_load_dwordx4 v1, s[16:19], s15 offen offset:2048 lds
s_waitcnt lgkmcnt(4)
Mva 40,208,128
VC 113,118,119
VC 114,124,125
Mva 56,208,132
TR 196,2,35840
VC 115,126,127
Mva 72,208,136
s_mov_b32 s15, 4424960
buffer_load_dwordx4 v1, s[20:23], s15 offen offset:3072 lds
Mva 88,208,140
TR 198,2,36352
s_waitcnt lgkmcnt(4)
Mva 44,212,128
ds_write_b128 v5, v[112:115] offset:17408
Mva 60,212,132
TR 200,3,34816
VM 132,128,132
Mva 76,212,136
TR 202,3,35328
VM 133,129,133
Mva 92,212,140
VM 134,130,134
VM 128,8,128
s_waitcnt lgkmcnt(5)
Mva 32,192,144
TR 204,3,35840
VM 135,131,135
Mva 48,192,148
TR 206,3,36352
VM 129,8,129
Mva 64,192,152
VM 140,136,140
VM 130,8,130
Mva 80,192,156
TR 208,2,49152
s_waitcnt lgkmcnt(6)
Mva 36,196,144
TR 210,2,49664
Mva 52,196,148
v_exp_f32 v128, v128
Mva 68,196,152
VM 141,137,141
VM 131,8,131
Mva 84,196,156
TR 212,2,50176
s_waitcnt lgkmcnt(5)
Mva 40,200,144
TR 214,2,50688
Mva 56,200,148
v_exp_f32 v129, v129
Mva 72,200,152
VM 142,138,142
VM 136,8,136
Mva 88,200,156
TR 192,3,49152
s_waitcnt lgkmcnt(5)
Mva 44,204,144
TR 194,3,49664
Mva 60,204,148
v_exp_f32 v130, v130
Mva 76,204,152
VF 128,128,12,12
VM 143,139,143
Mva 92,204,156
TR 196,3,50176
VM 137,8,137
s_waitcnt vmcnt(4)
s_barrier
s_waitcnt lgkmcnt(5)
Mva 32,208,160
s_mov_b32 m0, s70
TR 198,3,50688
Mva 48,208,164
s_mov_b32 s15, 4722944
buffer_load_dwordx4 v1, s[16:19], s15 offen offset:0 lds
Mva 64,208,168
v_exp_f32 v131, v131
Mva 80,208,172
VF 129,129,12,12
TR 200,2,51200
s_waitcnt lgkmcnt(5)
Mva 36,212,160
s_mov_b32 s15, 4721920
buffer_load_dwordx4 v1, s[20:23], s15 offen offset:1024 lds
Mva 52,212,164
VM 138,8,138
TR 202,2,51712
Mva 68,212,168
v_exp_f32 v136, v136
Mva 84,212,172
s_mov_b32 s15, 5015808
buffer_load_dwordx4 v1, s[16:19], s15 offen offset:2048 lds
s_waitcnt lgkmcnt(4)
Mva 40,192,160
VF 130,130,12,12
TR 204,2,52224
Mva 56,192,164
TR 206,2,52736
Mva 72,192,168
s_mov_b32 s15, 5014784
buffer_load_dwordx4 v1, s[20:23], s15 offen offset:3072 lds
Mva 88,192,172
v_rcp_f32 v128, v128
s_waitcnt lgkmcnt(4)
Mva 44,196,160
VM 139,8,139
TR 208,3,51200
Mva 60,196,164
v_exp_f32 v137, v137
Mva 76,196,168
TR 210,3,51712
VF 131,131,12,12
Mva 92,196,172
v_rcp_f32 v129, v129
s_waitcnt lgkmcnt(4)
Mva 32,200,176
TR 212,3,52224
Mva 48,200,180
TR 214,3,52736
Mva 64,200,184
v_exp_f32 v138, v138
Mva 80,200,188
VF 136,136,12,12
TR 192,2,0
s_waitcnt lgkmcnt(5)
Mva 36,204,176
v_rcp_f32 v130, v130
Mva 52,204,180
TR 194,2,512
VM 132,132,128
Mva 68,204,184
v_exp_f32 v139, v139
Mva 84,204,188
TR 196,2,1024
VF 137,137,12,12
s_waitcnt lgkmcnt(5)
Mva 40,208,176
TR 198,2,1536
Mva 56,208,180
v_rcp_f32 v131, v131
Mva 72,208,184
VM 133,133,129
TR 200,3,0
Mva 88,208,188
VF 138,138,12,12
TR 202,3,512
s_waitcnt lgkmcnt(6)
Mva 44,212,176
v_rcp_f32 v136, v136
Mva 60,212,180
VM 134,134,130
VF 139,139,12,12
Mva 76,212,184
TR 204,3,1024
Mva 92,212,188
TR 206,3,1536
s_waitcnt vmcnt(4)
s_barrier
s_waitcnt lgkmcnt(6)
Mva 32,192,192
s_mov_b32 m0, s71
Mva 48,192,196
s_mov_b32 s15, 4480
buffer_load_dwordx4 v1, s[16:19], s15 offen offset:0 lds
Mva 64,192,200
v_rcp_f32 v137, v137
Mva 80,192,204
VM 135,135,131
TR 208,2,2048
s_waitcnt lgkmcnt(5)
Mva 36,196,192
s_mov_b32 s15, 3456
buffer_load_dwordx4 v1, s[20:23], s15 offen offset:1024 lds
Mva 52,196,196
TR 210,2,2560
Mva 68,196,200
v_rcp_f32 v138, v138
Mva 84,196,204
s_mov_b32 s15, 297344
buffer_load_dwordx4 v1, s[16:19], s15 offen offset:2048 lds
s_waitcnt lgkmcnt(4)
Mva 40,200,192
VM 140,140,136
TR 212,2,3072
Mva 56,200,196
v_rcp_f32 v139, v139
Mva 72,200,200
s_mov_b32 s15, 296320
buffer_load_dwordx4 v1, s[20:23], s15 offen offset:3072 lds
Mva 88,200,204
TR 214,2,3584
VM 141,141,137
s_waitcnt lgkmcnt(4)
Mva 44,204,192
VM 142,142,138
VM 143,143,139
Mva 60,204,196
TR 192,3,2048
VC 128,132,133
Mva 76,204,200
TR 194,3,2560
VC 129,134,135
Mva 92,204,204
VC 130,140,141
VC 131,142,143
s_waitcnt lgkmcnt(4)
Mva 32,208,208
TR 196,3,3072
Mva 48,208,212
TR 198,3,3584
Mva 64,208,216
ds_write_b128 v5, v[128:131] offset:18432
Mva 80,208,220
VM 148,144,148
TR 200,2,16384
s_waitcnt lgkmcnt(6)
Mva 36,212,208
VM 149,145,149
TR 202,2,16896
Mva 52,212,212
VM 150,146,150
VM 144,9,144
Mva 68,212,216
VM 151,147,151
TR 204,2,17408
Mva 84,212,220
VM 145,9,145
TR 206,2,17920
s_waitcnt lgkmcnt(7)
Mva 40,192,208
VM 156,152,156
VM 146,9,146
Mva 56,192,212
v_exp_f32 v144, v144
Mva 72,192,216
TR 208,3,16384
VM 157,153,157
Mva 88,192,220
TR 210,3,16896
VM 147,9,147
s_waitcnt lgkmcnt(7)
Mva 44,196,208
v_exp_f32 v145, v145
Mva 60,196,212
VM 158,154,158
VM 152,9,152
Mva 76,196,216
TR 212,3,17408
Mva 92,196,220
TR 214,3,17920
s_waitcnt vmcnt(4)
s_barrier
s_waitcnt lgkmcnt(6)
Mva 32,200,224
s_mov_b32 m0, s68
Mva 48,200,228
s_mov_b32 s15, 594304
buffer_load_dwordx4 v1, s[16:19], s15 offen offset:0 lds
Mva 64,200,232
TR 192,2,18432
Mva 80,200,236
v_exp_f32 v146, v146
s_waitcnt lgkmcnt(5)
Mva 36,204,224
s_mov_b32 s15, 593280
buffer_load_dwordx4 v1, s[20:23], s15 offen offset:1024 lds
Mva 52,204,228
TR 194,2,18944
VF 144,144,13,13
Mva 68,204,232
VM 159,155,159
VM 153,9,153
Mva 84,204,236
s_mov_b32 s15, 887168
buffer_load_dwordx4 v1, s[16:19], s15 offen offset:2048 lds
s_waitcnt lgkmcnt(4)
Mva 40,208,224
TR 196,2,19456
Mva 56,208,228
TR 198,2,19968
Mva 72,208,232
s_mov_b32 s15, 886144
buffer_load_dwordx4 v1, s[20:23], s15 offen offset:3072 lds
Mva 88,208,236
v_exp_f32 v147, v147
s_waitcnt lgkmcnt(4)
Mva 44,212,224
VF 145,145,13,13
VM 154,9,154
Mva 60,212,228
TR 200,3,18432
Mva 76,212,232
TR 202,3,18944
Mva 92,212,236
v_exp_f32 v152, v152
s_waitcnt lgkmcnt(4)
Mva 32,192,240
VF 146,146,13,13
TR 204,3,19456
Mva 48,192,244
v_rcp_f32 v144, v144
Mva 64,192,248
TR 206,3,19968
VM 155,9,155
Mva 80,192,252
v_exp_f32 v153, v153
s_waitcnt lgkmcnt(4)
Mva 36,196,240
TR 208,2,32768
VF 147,147,13,13
Mva 52,196,244
TR 210,2,33280
Mva 68,196,248
v_rcp_f32 v145, v145
Mva 84,196,252
TR 212,2,33792
s_waitcnt lgkmcnt(5)
Mva 40,200,240
v_exp_f32 v154, v154
Mva 56,200,244
TR 214,2,34304
VF 152,152,13,13
Mva 72,200,248
v_rcp_f32 v146, v146
Mva 88,200,252
TR 192,3,32768
VM 148,148,144
s_waitcnt lgkmcnt(5)
Mva 44,204,240
TR 194,3,33280
Mva 60,204,244
v_exp_f32 v155, v155
Mva 76,204,248
VF 153,153,13,13
TR 196,3,33792
Mva 92,204,252
TR 198,3,34304
s_waitcnt vmcnt(4)
s_barrier
s_waitcnt lgkmcnt(6)
Mvv 32,208,224
s_mov_b32 m0, s69
Mvv 48,208,228
s_mov_b32 s15, 1184128
buffer_load_dwordx4 v1, s[16:19], s15 offen offset:0 lds
Mvv 64,208,232
v_rcp_f32 v147, v147
Mvv 80,208,236
VM 149,149,145
VF 154,154,13,13
s_waitcnt lgkmcnt(4)
Mvv 36,212,224
s_mov_b32 s15, 1183104
buffer_load_dwordx4 v1, s[20:23], s15 offen offset:1024 lds
Mvv 52,212,228
TR 200,2,34816
Mvv 68,212,232
TR 202,2,35328
Mvv 84,212,236
s_mov_b32 s15, 1476992
buffer_load_dwordx4 v1, s[16:19], s15 offen offset:2048 lds
s_waitcnt lgkmcnt(4)
Mvv 40,192,224
v_rcp_f32 v152, v152
Mvv 56,192,228
VM 150,150,146
VF 155,155,13,13
Mvv 72,192,232
s_mov_b32 s15, 1475968
buffer_load_dwordx4 v1, s[20:23], s15 offen offset:3072 lds
Mvv 88,192,236
TR 204,2,35840
s_waitcnt lgkmcnt(3)
Mvv 44,196,224
TR 206,2,36352
Mvv 60,196,228
v_rcp_f32 v153, v153
Mvv 76,196,232
VM 151,151,147
TR 208,3,34816
Mvv 92,196,236
v_rcp_f32 v154, v154
s_waitcnt lgkmcnt(3)
Mvv 32,200,240
TR 210,3,35328
VM 156,156,152
Mvv 48,200,244
v_rcp_f32 v155, v155
Mvv 64,200,248
TR 212,3,35840
VM 157,157,153
Mvv 80,200,252
TR 214,3,36352
VM 158,158,154
s_waitcnt lgkmcnt(4)
Mvv 36,204,240
VM 159,159,155
VC 144,148,149
Mvv 52,204,244
TR 192,2,49152
VC 145,150,151
Mvv 68,204,248
TR 194,2,49664
VC 146,156,157
Mvv 84,204,252
VC 147,158,159
TR 196,2,50176
s_waitcnt lgkmcnt(5)
Mvv 40,208,240
TR 198,2,50688
Mvv 56,208,244
ds_write_b128 v5, v[144:147] offset:19456
Mvv 72,208,248
TR 200,3,49152
Mvv 88,208,252
TR 202,3,49664
s_waitcnt lgkmcnt(7)
Mvv 44,212,240
Mvv 60,212,244
Mvv 76,212,248
TR 204,3,50176
Mvv 92,212,252
TR 206,3,50688
s_waitcnt lgkmcnt(4)
s_waitcnt vmcnt(4)
s_barrier
Zva 96,192,0
s_mov_b32 m0, s70
Zva 112,192,4
s_mov_b32 s15, 1773952
buffer_load_dwordx4 v1, s[16:19], s15 offen offset:0 lds
Zva 128,192,8
VM 36,32,36
VM 37,33,37
Zva 144,192,12
VM 38,34,38
VM 32,6,32
Zva 100,196,0
s_mov_b32 s15, 1772928
buffer_load_dwordx4 v1, s[20:23], s15 offen offset:1024 lds
Zva 116,196,4
VM 39,35,39
VM 33,6,33
Zva 132,196,8
TR 208,2,51200
VM 44,40,44
Zva 148,196,12
s_mov_b32 s15, 2066816
buffer_load_dwordx4 v1, s[16:19], s15 offen offset:2048 lds
s_waitcnt lgkmcnt(3)
Zva 104,200,0
TR 210,2,51712
VM 34,6,34
Zva 120,200,4
v_exp_f32 v32, v32
Zva 136,200,8
s_mov_b32 s15, 2065792
buffer_load_dwordx4 v1, s[20:23], s15 offen offset:3072 lds
Zva 152,200,12
TR 212,2,52224
VM 45,41,45
s_waitcnt lgkmcnt(3)
Zva 108,204,0
TR 214,2,52736
VM 35,6,35
Zva 124,204,4
v_exp_f32 v33, v33
Zva 140,204,8
TR 192,3,51200
VM 46,42,46
Zva 156,204,12
TR 194,3,51712
VM 40,6,40
s_waitcnt lgkmcnt(4)
Mva 96,208,16
v_exp_f32 v34, v34
Mva 112,208,20
TR 196,3,52224
VF 32,32,10,10
Mva 128,208,24
TR 198,3,52736
VM 47,43,47
Mva 144,208,28
VM 41,6,41
TR 200,2,0
s_waitcnt lgkmcnt(5)
Mva 100,212,16
v_exp_f32 v35, v35
Mva 116,212,20
TR 202,2,512
VF 33,33,10,10
Mva 132,212,24
VM 42,6,42
TR 204,2,1024
Mva 148,212,28
v_exp_f32 v40, v40
s_waitcnt lgkmcnt(5)
Mva 104,192,16
TR 206,2,1536
VF 34,34,10,10
Mva 120,192,20
v_rcp_f32 v32, v32
Mva 136,192,24
TR 208,3,0
VM 43,6,43
Mva 152,192,28
TR 210,3,512
s_waitcnt lgkmcnt(6)
Mva 108,196,16
v_exp_f32 v41, v41
Mva 124,196,20
VF 35,35,10,10
Mva 140,196,24
TR 212,3,1024
Mva 156,196,28
TR 214,3,1536
s_waitcnt vmcnt(4)
s_barrier
s_waitcnt lgkmcnt(6)
Mva 96,200,32
s_mov_b32 m0, s71
Mva 112,200,36
s_mov_b32 s15, 2363776
buffer_load_dwordx4 v1, s[16:19], s15 offen offset:0 lds
Mva 128,200,40
v_rcp_f32 v33, v33
Mva 144,200,44
v_exp_f32 v42, v42
s_waitcnt lgkmcnt(4)
Mva 100,204,32
s_mov_b32 s15, 2362752
buffer_load_dwordx4 v1, s[20:23], s15 offen offset:1024 lds
Mva 116,204,36
TR 192,2,2048
VF 40,40,10,10
Mva 132,204,40
TR 194,2,2560
Mva 148,204,44
s_mov_b32 s15, 2656640
buffer_load_dwordx4 v1, s[16:19], s15 offen offset:2048 lds
s_waitcnt lgkmcnt(4)
Mva 104,208,32
v_rcp_f32 v34, v34
Mva 120,208,36
VM 36,36,32
TR 196,2,3072
Mva 136,208,40
s_mov_b32 s15, 2655616
buffer_load_dwordx4 v1, s[20:23], s15 offen offset:3072 lds
Mva 152,208,44
TR 198,2,3584
s_waitcnt lgkmcnt(4)
Mva 108,212,32
v_exp_f32 v43, v43
Mva 124,212,36
VF 41,41,10,10
TR 200,3,2048
Mva 140,212,40
v_rcp_f32 v35, v35
Mva 156,212,44
TR 202,3,2560
VM 37,37,33
s_waitcnt lgkmcnt(4)
Mva 96,192,48
VF 42,42,10,10
TR 204,3,3072
Mva 112,192,52
v_rcp_f32 v40, v40
Mva 128,192,56
TR 206,3,3584
VM 38,38,34
Mva 144,192,60
VF 43,43,10,10
)"
R"(TR 208,2,16384
s_waitcnt lgkmcnt(5)
Mva 100,196,48
v_rcp_f32 v41, v41
Mva 116,196,52
TR 210,2,16896
VM 39,39,35
Mva 132,196,56
v_rcp_f32 v42, v42
Mva 148,196,60
TR 212,2,17408
VM 44,44,40
s_waitcnt lgkmcnt(5)
Mva 104,200,48
TR 214,2,17920
Mva 120,200,52
v_rcp_f32 v43, v43
Mva 136,200,56
VM 45,45,41
TR 192,3,16384
Mva 152,200,60
VM 46,46,42
TR 194,3,16896
s_waitcnt lgkmcnt(6)
Mva 108,204,48
VM 47,47,43
VC 32,36,37
Mva 124,204,52
VC 33,38,39
VC 34,44,45
Mva 140,204,56
TR 196,3,17408
VC 35,46,47
Mva 156,204,60
TR 198,3,17920
s_waitcnt vmcnt(4)
s_barrier
s_waitcnt lgkmcnt(6)
Mva 96,208,64
s_mov_b32 m0, s68
Mva 112,208,68
s_mov_b32 s15, 2953600
buffer_load_dwordx4 v1, s[16:19], s15 offen offset:0 lds
Mva 128,208,72
TR 200,2,18432
Mva 144,208,76
ds_write_b128 v5, v[32:35] offset:32768
s_waitcnt lgkmcnt(6)
Mva 100,212,64
s_mov_b32 s15, 2952576
buffer_load_dwordx4 v1, s[20:23], s15 offen offset:1024 lds
Mva 116,212,68
TR 202,2,18944
VM 52,48,52
Mva 132,212,72
VM 53,49,53
VM 54,50,54
Mva 148,212,76
s_mov_b32 s15, 3246464
buffer_load_dwordx4 v1, s[16:19], s15 offen offset:2048 lds
s_waitcnt lgkmcnt(5)
Mva 104,192,64
TR 204,2,19456
VM 48,7,48
Mva 120,192,68
TR 206,2,19968
VM 55,51,55
Mva 136,192,72
s_mov_b32 s15, 3245440
buffer_load_dwordx4 v1, s[20:23], s15 offen offset:3072 lds
Mva 152,192,76
VM 49,7,49
VM 60,56,60
s_waitcnt lgkmcnt(5)
Mva 108,196,64
TR 208,3,18432
VM 50,7,50
Mva 124,196,68
TR 210,3,18944
Mva 140,196,72
v_exp_f32 v48, v48
Mva 156,196,76
VM 61,57,61
TR 212,3,19456
s_waitcnt lgkmcnt(5)
Mva 96,200,80
VM 51,7,51
TR 214,3,19968
Mva 112,200,84
v_exp_f32 v49, v49
Mva 128,200,88
VM 62,58,62
TR 192,2,32768
Mva 144,200,92
VM 56,7,56
TR 194,2,33280
s_waitcnt lgkmcnt(6)
Mva 100,204,80
v_exp_f32 v50, v50
Mva 116,204,84
VF 48,48,11,11
VM 63,59,63
Mva 132,204,88
TR 196,2,33792
VM 57,7,57
Mva 148,204,92
TR 198,2,34304
s_waitcnt lgkmcnt(6)
Mva 104,208,80
v_exp_f32 v51, v51
Mva 120,208,84
VF 49,49,11,11
VM 58,7,58
Mva 136,208,88
TR 200,3,32768
Mva 152,208,92
TR 202,3,33280
s_waitcnt lgkmcnt(6)
Mva 108,212,80
v_exp_f32 v56, v56
Mva 124,212,84
VF 50,50,11,11
Mva 140,212,88
TR 204,3,33792
Mva 156,212,92
TR 206,3,34304
s_waitcnt vmcnt(4)
s_barrier
s_waitcnt lgkmcnt(6)
Mva 96,192,96
s_mov_b32 m0, s69
Mva 112,192,100
s_mov_b32 s15, 3543424
buffer_load_dwordx4 v1, s[16:19], s15 offen offset:0 lds
Mva 128,192,104
v_rcp_f32 v48, v48
Mva 144,192,108
VM 59,7,59
TR 208,2,34816
s_waitcnt lgkmcnt(5)
Mva 100,196,96
s_mov_b32 s15, 3542400
buffer_load_dwordx4 v1, s[20:23], s15 offen offset:1024 lds
Mva 116,196,100
TR 210,2,35328
Mva 132,196,104
v_exp_f32 v57, v57
Mva 148,196,108
s_mov_b32 s15, 3836288
buffer_load_dwordx4 v1, s[16:19], s15 offen offset:2048 lds
s_waitcnt lgkmcnt(4)
Mva 104,200,96
VF 51,51,11,11
TR 212,2,35840
Mva 120,200,100
v_rcp_f32 v49, v49
Mva 136,200,104
s_mov_b32 s15, 3835264
buffer_load_dwordx4 v1, s[20:23], s15 offen offset:3072 lds
Mva 152,200,108
TR 214,2,36352
s_waitcnt lgkmcnt(4)
Mva 108,204,96
v_exp_f32 v58, v58
Mva 124,204,100
VF 56,56,11,11
TR 192,3,34816
Mva 140,204,104
TR 194,3,35328
Mva 156,204,108
v_rcp_f32 v50, v50
s_waitcnt lgkmcnt(4)
Mva 96,208,112
VM 52,52,48
TR 196,3,35840
Mva 112,208,116
v_exp_f32 v59, v59
Mva 128,208,120
TR 198,3,36352
VF 57,57,11,11
Mva 144,208,124
v_rcp_f32 v51, v51
s_waitcnt lgkmcnt(4)
Mva 100,212,112
TR 200,2,49152
VM 53,53,49
Mva 116,212,116
TR 202,2,49664
VF 58,58,11,11
Mva 132,212,120
v_rcp_f32 v56, v56
Mva 148,212,124
TR 204,2,50176
VM 54,54,50
s_waitcnt lgkmcnt(5)
Mva 104,192,112
TR 206,2,50688
VF 59,59,11,11
Mva 120,192,116
v_rcp_f32 v57, v57
Mva 136,192,120
TR 208,3,49152
VM 55,55,51
Mva 152,192,124
TR 210,3,49664
s_waitcnt lgkmcnt(6)
Mva 108,196,112
v_rcp_f32 v58, v58
Mva 124,196,116
VM 60,60,56
Mva 140,196,120
TR 212,3,50176
Mva 156,196,124
TR 214,3,50688
s_waitcnt vmcnt(4)
s_barrier
s_waitcnt lgkmcnt(6)
Mva 96,200,128
s_mov_b32 m0, s70
Mva 112,200,132
s_mov_b32 s15, 4133248
buffer_load_dwordx4 v1, s[16:19], s15 offen offset:0 lds
Mva 128,200,136
v_rcp_f32 v59, v59
Mva 144,200,140
VM 61,61,57
VM 62,62,58
s_waitcnt lgkmcnt(4)
Mva 100,204,128
s_mov_b32 s15, 4132224
buffer_load_dwordx4 v1, s[20:23], s15 offen offset:1024 lds
Mva 116,204,132
TR 192,2,51200
VM 63,63,59
Mva 132,204,136
TR 194,2,51712
VC 48,52,53
Mva 148,204,140
s_mov_b32 s15, 4426112
buffer_load_dwordx4 v1, s[16:19], s15 offen offset:2048 lds
s_waitcnt lgkmcnt(4)
Mva 104,208,128
VC 49,54,55
VC 50,60,61
Mva 120,208,132
TR 196,2,52224
VC 51,62,63
Mva 136,208,136
s_mov_b32 s15, 4425088
buffer_load_dwordx4 v1, s[20:23], s15 offen offset:3072 lds
Mva 152,208,140
TR 198,2,52736
s_waitcnt lgkmcnt(4)
Mva 108,212,128
ds_write_b128 v5, v[48:51] offset:33792
Mva 124,212,132
TR 200,3,51200
VM 68,64,68
Mva 140,212,136
TR 202,3,51712
VM 69,65,69
Mva 156,212,140
VM 70,66,70
VM 64,8,64
s_waitcnt lgkmcnt(5)
Mva 96,192,144
TR 204,3,52224
VM 71,67,71
Mva 112,192,148
TR 206,3,52736
VM 65,8,65
Mva 128,192,152
VM 76,72,76
VM 66,8,66
Mva 144,192,156
TR 208,2,0
s_waitcnt lgkmcnt(6)
Mva 100,196,144
TR 210,2,512
Mva 116,196,148
v_exp_f32 v64, v64
Mva 132,196,152
VM 77,73,77
VM 67,8,67
Mva 148,196,156
TR 212,2,1024
s_waitcnt lgkmcnt(5)
Mva 104,200,144
TR 214,2,1536
Mva 120,200,148
v_exp_f32 v65, v65
Mva 136,200,152
VM 78,74,78
VM 72,8,72
Mva 152,200,156
TR 192,3,0
s_waitcnt lgkmcnt(5)
Mva 108,204,144
TR 194,3,512
Mva 124,204,148
v_exp_f32 v66, v66
Mva 140,204,152
VF 64,64,12,12
VM 79,75,79
Mva 156,204,156
TR 196,3,1024
VM 73,8,73
s_waitcnt vmcnt(4)
s_barrier
s_waitcnt lgkmcnt(5)
Mva 96,208,160
s_mov_b32 m0, s71
TR 198,3,1536
Mva 112,208,164
s_mov_b32 s15, 4723072
buffer_load_dwordx4 v1, s[16:19], s15 offen offset:0 lds
Mva 128,208,168
v_exp_f32 v67, v67
Mva 144,208,172
VF 65,65,12,12
TR 200,2,2048
s_waitcnt lgkmcnt(5)
Mva 100,212,160
s_mov_b32 s15, 4722048
buffer_load_dwordx4 v1, s[20:23], s15 offen offset:1024 lds
Mva 116,212,164
VM 74,8,74
TR 202,2,2560
Mva 132,212,168
v_exp_f32 v72, v72
Mva 148,212,172
s_mov_b32 s15, 5015936
buffer_load_dwordx4 v1, s[16:19], s15 offen offset:2048 lds
s_waitcnt lgkmcnt(4)
Mva 104,192,160
VF 66,66,12,12
TR 204,2,3072
Mva 120,192,164
TR 206,2,3584
Mva 136,192,168
s_mov_b32 s15, 5014912
buffer_load_dwordx4 v1, s[20:23], s15 offen offset:3072 lds
Mva 152,192,172
v_rcp_f32 v64, v64
s_waitcnt lgkmcnt(4)
Mva 108,196,160
VM 75,8,75
TR 208,3,2048
Mva 124,196,164
v_exp_f32 v73, v73
Mva 140,196,168
TR 210,3,2560
VF 67,67,12,12
Mva 156,196,172
v_rcp_f32 v65, v65
s_waitcnt lgkmcnt(4)
Mva 96,200,176
TR 212,3,3072
Mva 112,200,180
TR 214,3,3584
Mva 128,200,184
v_exp_f32 v74, v74
Mva 144,200,188
VF 72,72,12,12
TR 192,2,16384
s_waitcnt lgkmcnt(5)
Mva 100,204,176
v_rcp_f32 v66, v66
Mva 116,204,180
TR 194,2,16896
VM 68,68,64
Mva 132,204,184
v_exp_f32 v75, v75
Mva 148,204,188
TR 196,2,17408
VF 73,73,12,12
s_waitcnt lgkmcnt(5)
Mva 104,208,176
TR 198,2,17920
Mva 120,208,180
v_rcp_f32 v67, v67
Mva 136,208,184
VM 69,69,65
TR 200,3,16384
Mva 152,208,188
VF 74,74,12,12
TR 202,3,16896
s_waitcnt lgkmcnt(6)
Mva 108,212,176
v_rcp_f32 v72, v72
Mva 124,212,180
VM 70,70,66
VF 75,75,12,12
Mva 140,212,184
TR 204,3,17408
Mva 156,212,188
TR 206,3,17920
s_waitcnt vmcnt(4)
s_barrier
s_waitcnt lgkmcnt(6)
Mva 96,192,192
s_mov_b32 m0, s68
Mva 112,192,196
s_mov_b32 s15, 4608
buffer_load_dwordx4 v1, s[16:19], s15 offen offset:0 lds
Mva 128,192,200
v_rcp_f32 v73, v73
Mva 144,192,204
VM 71,71,67
TR 208,2,18432
s_waitcnt lgkmcnt(5)
Mva 100,196,192
s_mov_b32 s15, 3584
buffer_load_dwordx4 v1, s[20:23], s15 offen offset:1024 lds
Mva 116,196,196
TR 210,2,18944
Mva 132,196,200
v_rcp_f32 v74, v74
Mva 148,196,204
s_mov_b32 s15, 297472
buffer_load_dwordx4 v1, s[16:19], s15 offen offset:2048 lds
s_waitcnt lgkmcnt(4)
Mva 104,200,192
VM 76,76,72
TR 212,2,19456
Mva 120,200,196
v_rcp_f32 v75, v75
Mva 136,200,200
s_mov_b32 s15, 296448
buffer_load_dwordx4 v1, s[20:23], s15 offen offset:3072 lds
Mva 152,200,204
TR 214,2,19968
VM 77,77,73
s_waitcnt lgkmcnt(4)
Mva 108,204,192
VM 78,78,74
VM 79,79,75
Mva 124,204,196
TR 192,3,18432
VC 64,68,69
Mva 140,204,200
TR 194,3,18944
VC 65,70,71
Mva 156,204,204
VC 66,76,77
VC 67,78,79
s_waitcnt lgkmcnt(4)
Mva 96,208,208
TR 196,3,19456
Mva 112,208,212
TR 198,3,19968
Mva 128,208,216
ds_write_b128 v5, v[64:67] offset:34816
Mva 144,208,220
VM 84,80,84
TR 200,2,32768
s_waitcnt lgkmcnt(6)
Mva 100,212,208
VM 85,81,85
TR 202,2,33280
Mva 116,212,212
VM 86,82,86
VM 80,9,80
Mva 132,212,216
VM 87,83,87
TR 204,2,33792
Mva 148,212,220
VM 81,9,81
TR 206,2,34304
s_waitcnt lgkmcnt(7)
Mva 104,192,208
VM 92,88,92
VM 82,9,82
Mva 120,192,212
v_exp_f32 v80, v80
Mva 136,192,216
TR 208,3,32768
VM 93,89,93
Mva 152,192,220
TR 210,3,33280
VM 83,9,83
s_waitcnt lgkmcnt(7)
Mva 108,196,208
v_exp_f32 v81, v81
Mva 124,196,212
VM 94,90,94
VM 88,9,88
Mva 140,196,216
TR 212,3,33792
Mva 156,196,220
TR 214,3,34304
s_waitcnt vmcnt(4)
s_barrier
s_waitcnt lgkmcnt(6)
Mva 96,200,224
s_mov_b32 m0, s69
Mva 112,200,228
s_mov_b32 s15, 594432
buffer_load_dwordx4 v1, s[16:19], s15 offen offset:0 lds
Mva 128,200,232
TR 192,2,34816
Mva 144,200,236
v_exp_f32 v82, v82
s_waitcnt lgkmcnt(5)
Mva 100,204,224
s_mov_b32 s15, 593408
buffer_load_dwordx4 v1, s[20:23], s15 offen offset:1024 lds
Mva 116,204,228
TR 194,2,35328
VF 80,80,13,13
Mva 132,204,232
VM 95,91,95
VM 89,9,89
Mva 148,204,236
s_mov_b32 s15, 887296
buffer_load_dwordx4 v1, s[16:19], s15 offen offset:2048 lds
s_waitcnt lgkmcnt(4)
Mva 104,208,224
TR 196,2,35840
Mva 120,208,228
TR 198,2,36352
Mva 136,208,232
s_mov_b32 s15, 886272
buffer_load_dwordx4 v1, s[20:23], s15 offen offset:3072 lds
Mva 152,208,236
v_exp_f32 v83, v83
s_waitcnt lgkmcnt(4)
Mva 108,212,224
VF 81,81,13,13
VM 90,9,90
Mva 124,212,228
TR 200,3,34816
Mva 140,212,232
TR 202,3,35328
Mva 156,212,236
v_exp_f32 v88, v88
s_waitcnt lgkmcnt(4)
Mva 96,192,240
VF 82,82,13,13
TR 204,3,35840
Mva 112,192,244
v_rcp_f32 v80, v80
Mva 128,192,248
TR 206,3,36352
VM 91,9,91
Mva 144,192,252
v_exp_f32 v89, v89
s_waitcnt lgkmcnt(4)
Mva 100,196,240
TR 208,2,49152
VF 83,83,13,13
Mva 116,196,244
TR 210,2,49664
Mva 132,196,248
v_rcp_f32 v81, v81
Mva 148,196,252
TR 212,2,50176
s_waitcnt lgkmcnt(5)
Mva 104,200,240
v_exp_f32 v90, v90
Mva 120,200,244
TR 214,2,50688
VF 88,88,13,13
Mva 136,200,248
v_rcp_f32 v82, v82
Mva 152,200,252
TR 192,3,49152
VM 84,84,80
s_waitcnt lgkmcnt(5)
Mva 108,204,240
TR 194,3,49664
Mva 124,204,244
v_exp_f32 v91, v91
Mva 140,204,248
VF 89,89,13,13
TR 196,3,50176
Mva 156,204,252
TR 198,3,50688
s_waitcnt vmcnt(4)
s_barrier
s_waitcnt lgkmcnt(6)
Mvv 96,208,224
s_mov_b32 m0, s70
Mvv 112,208,228
s_mov_b32 s15, 1184256
buffer_load_dwordx4 v1, s[16:19], s15 offen offset:0 lds
Mvv 128,208,232
v_rcp_f32 v83, v83
Mvv 144,208,236
VM 85,85,81
VF 90,90,13,13
s_waitcnt lgkmcnt(4)
Mvv 100,212,224
s_mov_b32 s15, 1183232
buffer_load_dwordx4 v1, s[20:23], s15 offen offset:1024 lds
Mvv 116,212,228
TR 200,2,51200
Mvv 132,212,232
TR 202,2,51712
Mvv 148,212,236
)"
R"(s_mov_b32 s15, 1477120
buffer_load_dwordx4 v1, s[16:19], s15 offen offset:2048 lds
s_waitcnt lgkmcnt(4)
Mvv 104,192,224
v_rcp_f32 v88, v88
Mvv 120,192,228
VM 86,86,82
VF 91,91,13,13
Mvv 136,192,232
s_mov_b32 s15, 1476096
buffer_load_dwordx4 v1, s[20:23], s15 offen offset:3072 lds
Mvv 152,192,236
TR 204,2,52224
s_waitcnt lgkmcnt(3)
Mvv 108,196,224
TR 206,2,52736
Mvv 124,196,228
v_rcp_f32 v89, v89
Mvv 140,196,232
VM 87,87,83
TR 208,3,51200
Mvv 156,196,236
v_rcp_f32 v90, v90
s_waitcnt lgkmcnt(3)
Mvv 96,200,240
TR 210,3,51712
VM 92,92,88
Mvv 112,200,244
v_rcp_f32 v91, v91
Mvv 128,200,248
TR 212,3,52224
VM 93,93,89
Mvv 144,200,252
TR 214,3,52736
VM 94,94,90
s_waitcnt lgkmcnt(4)
Mvv 100,204,240
VM 95,95,91
VC 80,84,85
Mvv 116,204,244
TR 192,2,0
VC 81,86,87
Mvv 132,204,248
TR 194,2,512
VC 82,92,93
Mvv 148,204,252
VC 83,94,95
TR 196,2,1024
s_waitcnt lgkmcnt(5)
Mvv 104,208,240
TR 198,2,1536
Mvv 120,208,244
ds_write_b128 v5, v[80:83] offset:35840
Mvv 136,208,248
TR 200,3,0
Mvv 152,208,252
TR 202,3,512
s_waitcnt lgkmcnt(7)
Mvv 108,212,240
Mvv 124,212,244
Mvv 140,212,248
TR 204,3,1024
Mvv 156,212,252
TR 206,3,1536
s_waitcnt lgkmcnt(4)
s_waitcnt vmcnt(4)
s_barrier
Zva 32,192,0
s_mov_b32 m0, s71
Zva 48,192,4
s_mov_b32 s15, 1774080
buffer_load_dwordx4 v1, s[16:19], s15 offen offset:0 lds
Zva 64,192,8
VM 100,96,100
VM 101,97,101
Zva 80,192,12
VM 102,98,102
VM 96,6,96
Zva 36,196,0
s_mov_b32 s15, 1773056
buffer_load_dwordx4 v1, s[20:23], s15 offen offset:1024 lds
Zva 52,196,4
VM 103,99,103
VM 97,6,97
Zva 68,196,8
TR 208,2,2048
VM 108,104,108
Zva 84,196,12
s_mov_b32 s15, 2066944
buffer_load_dwordx4 v1, s[16:19], s15 offen offset:2048 lds
s_waitcnt lgkmcnt(3)
Zva 40,200,0
TR 210,2,2560
VM 98,6,98
Zva 56,200,4
v_exp_f32 v96, v96
Zva 72,200,8
s_mov_b32 s15, 2065920
buffer_load_dwordx4 v1, s[20:23], s15 offen offset:3072 lds
Zva 88,200,12
TR 212,2,3072
VM 109,105,109
s_waitcnt lgkmcnt(3)
Zva 44,204,0
TR 214,2,3584
VM 99,6,99
Zva 60,204,4
v_exp_f32 v97, v97
Zva 76,204,8
TR 192,3,2048
VM 110,106,110
Zva 92,204,12
TR 194,3,2560
VM 104,6,104
s_waitcnt lgkmcnt(4)
Mva 32,208,16
v_exp_f32 v98, v98
Mva 48,208,20
TR 196,3,3072
VF 96,96,10,10
Mva 64,208,24
TR 198,3,3584
VM 111,107,111
Mva 80,208,28
VM 105,6,105
TR 200,2,16384
s_waitcnt lgkmcnt(5)
Mva 36,212,16
v_exp_f32 v99, v99
Mva 52,212,20
TR 202,2,16896
VF 97,97,10,10
Mva 68,212,24
VM 106,6,106
TR 204,2,17408
Mva 84,212,28
v_exp_f32 v104, v104
s_waitcnt lgkmcnt(5)
Mva 40,192,16
TR 206,2,17920
VF 98,98,10,10
Mva 56,192,20
v_rcp_f32 v96, v96
Mva 72,192,24
TR 208,3,16384
VM 107,6,107
Mva 88,192,28
TR 210,3,16896
s_waitcnt lgkmcnt(6)
Mva 44,196,16
v_exp_f32 v105, v105
Mva 60,196,20
VF 99,99,10,10
Mva 76,196,24
TR 212,3,17408
Mva 92,196,28
TR 214,3,17920
s_waitcnt vmcnt(4)
s_barrier
s_waitcnt lgkmcnt(6)
Mva 32,200,32
s_mov_b32 m0, s68
Mva 48,200,36
s_mov_b32 s15, 2363904
buffer_load_dwordx4 v1, s[16:19], s15 offen offset:0 lds
Mva 64,200,40
v_rcp_f32 v97, v97
Mva 80,200,44
v_exp_f32 v106, v106
s_waitcnt lgkmcnt(4)
Mva 36,204,32
s_mov_b32 s15, 2362880
buffer_load_dwordx4 v1, s[20:23], s15 offen offset:1024 lds
Mva 52,204,36
TR 192,2,18432
VF 104,104,10,10
Mva 68,204,40
TR 194,2,18944
Mva 84,204,44
s_mov_b32 s15, 2656768
buffer_load_dwordx4 v1, s[16:19], s15 offen offset:2048 lds
s_waitcnt lgkmcnt(4)
Mva 40,208,32
v_rcp_f32 v98, v98
Mva 56,208,36
VM 100,100,96
TR 196,2,19456
Mva 72,208,40
s_mov_b32 s15, 2655744
buffer_load_dwordx4 v1, s[20:23], s15 offen offset:3072 lds
Mva 88,208,44
TR 198,2,19968
s_waitcnt lgkmcnt(4)
Mva 44,212,32
v_exp_f32 v107, v107
Mva 60,212,36
VF 105,105,10,10
TR 200,3,18432
Mva 76,212,40
v_rcp_f32 v99, v99
Mva 92,212,44
TR 202,3,18944
VM 101,101,97
s_waitcnt lgkmcnt(4)
Mva 32,192,48
VF 106,106,10,10
TR 204,3,19456
Mva 48,192,52
v_rcp_f32 v104, v104
Mva 64,192,56
TR 206,3,19968
VM 102,102,98
Mva 80,192,60
VF 107,107,10,10
TR 208,2,32768
s_waitcnt lgkmcnt(5)
Mva 36,196,48
v_rcp_f32 v105, v105
Mva 52,196,52
TR 210,2,33280
VM 103,103,99
Mva 68,196,56
v_rcp_f32 v106, v106
Mva 84,196,60
TR 212,2,33792
VM 108,108,104
s_waitcnt lgkmcnt(5)
Mva 40,200,48
TR 214,2,34304
Mva 56,200,52
v_rcp_f32 v107, v107
Mva 72,200,56
VM 109,109,105
TR 192,3,32768
Mva 88,200,60
VM 110,110,106
TR 194,3,33280
s_waitcnt lgkmcnt(6)
Mva 44,204,48
VM 111,111,107
VC 96,100,101
Mva 60,204,52
VC 97,102,103
VC 98,108,109
Mva 76,204,56
TR 196,3,33792
VC 99,110,111
Mva 92,204,60
TR 198,3,34304
s_waitcnt vmcnt(4)
s_barrier
s_waitcnt lgkmcnt(6)
Mva 32,208,64
s_mov_b32 m0, s69
Mva 48,208,68
s_mov_b32 s15, 2953728
buffer_load_dwordx4 v1, s[16:19], s15 offen offset:0 lds
Mva 64,208,72
TR 200,2,34816
Mva 80,208,76
ds_write_b128 v5, v[96:99] offset:49152
s_waitcnt lgkmcnt(6)
Mva 36,212,64
s_mov_b32 s15, 2952704
buffer_load_dwordx4 v1, s[20:23], s15 offen offset:1024 lds
Mva 52,212,68
TR 202,2,35328
VM 116,112,116
Mva 68,212,72
VM 117,113,117
VM 118,114,118
Mva 84,212,76
s_mov_b32 s15, 3246592
buffer_load_dwordx4 v1, s[16:19], s15 offen offset:2048 lds
s_waitcnt lgkmcnt(5)
Mva 40,192,64
TR 204,2,35840
VM 112,7,112
Mva 56,192,68
TR 206,2,36352
VM 119,115,119
Mva 72,192,72
s_mov_b32 s15, 3245568
buffer_load_dwordx4 v1, s[20:23], s15 offen offset:3072 lds
Mva 88,192,76
VM 113,7,113
VM 124,120,124
s_waitcnt lgkmcnt(5)
Mva 44,196,64
TR 208,3,34816
VM 114,7,114
Mva 60,196,68
TR 210,3,35328
Mva 76,196,72
v_exp_f32 v112, v112
Mva 92,196,76
VM 125,121,125
TR 212,3,35840
s_waitcnt lgkmcnt(5)
Mva 32,200,80
VM 115,7,115
TR 214,3,36352
Mva 48,200,84
v_exp_f32 v113, v113
Mva 64,200,88
VM 126,122,126
TR 192,2,49152
Mva 80,200,92
VM 120,7,120
TR 194,2,49664
s_waitcnt lgkmcnt(6)
Mva 36,204,80
v_exp_f32 v114, v114
Mva 52,204,84
VF 112,112,11,11
VM 127,123,127
Mva 68,204,88
TR 196,2,50176
VM 121,7,121
Mva 84,204,92
TR 198,2,50688
s_waitcnt lgkmcnt(6)
Mva 40,208,80
v_exp_f32 v115, v115
Mva 56,208,84
VF 113,113,11,11
VM 122,7,122
Mva 72,208,88
TR 200,3,49152
Mva 88,208,92
TR 202,3,49664
s_waitcnt lgkmcnt(6)
Mva 44,212,80
v_exp_f32 v120, v120
Mva 60,212,84
VF 114,114,11,11
Mva 76,212,88
TR 204,3,50176
Mva 92,212,92
TR 206,3,50688
s_waitcnt vmcnt(4)
s_barrier
s_waitcnt lgkmcnt(6)
Mva 32,192,96
s_mov_b32 m0, s70
Mva 48,192,100
s_mov_b32 s15, 3543552
buffer_load_dwordx4 v1, s[16:19], s15 offen offset:0 lds
Mva 64,192,104
v_rcp_f32 v112, v112
Mva 80,192,108
VM 123,7,123
TR 208,2,51200
s_waitcnt lgkmcnt(5)
Mva 36,196,96
s_mov_b32 s15, 3542528
buffer_load_dwordx4 v1, s[20:23], s15 offen offset:1024 lds
Mva 52,196,100
TR 210,2,51712
Mva 68,196,104
v_exp_f32 v121, v121
Mva 84,196,108
s_mov_b32 s15, 3836416
buffer_load_dwordx4 v1, s[16:19], s15 offen offset:2048 lds
s_waitcnt lgkmcnt(4)
Mva 40,200,96
VF 115,115,11,11
TR 212,2,52224
Mva 56,200,100
v_rcp_f32 v113, v113
Mva 72,200,104
s_mov_b32 s15, 3835392
buffer_load_dwordx4 v1, s[20:23], s15 offen offset:3072 lds
Mva 88,200,108
TR 214,2,52736
s_waitcnt lgkmcnt(4)
Mva 44,204,96
v_exp_f32 v122, v122
Mva 60,204,100
VF 120,120,11,11
TR 192,3,51200
Mva 76,204,104
TR 194,3,51712
Mva 92,204,108
v_rcp_f32 v114, v114
s_waitcnt lgkmcnt(4)
Mva 32,208,112
VM 116,116,112
TR 196,3,52224
Mva 48,208,116
v_exp_f32 v123, v123
Mva 64,208,120
TR 198,3,52736
VF 121,121,11,11
Mva 80,208,124
v_rcp_f32 v115, v115
s_waitcnt lgkmcnt(4)
Mva 36,212,112
TR 200,2,0
VM 117,117,113
Mva 52,212,116
TR 202,2,512
VF 122,122,11,11
Mva 68,212,120
v_rcp_f32 v120, v120
Mva 84,212,124
TR 204,2,1024
VM 118,118,114
s_waitcnt lgkmcnt(5)
Mva 40,192,112
TR 206,2,1536
VF 123,123,11,11
Mva 56,192,116
v_rcp_f32 v121, v121
Mva 72,192,120
TR 208,3,0
VM 119,119,115
Mva 88,192,124
TR 210,3,512
s_waitcnt lgkmcnt(6)
Mva 44,196,112
v_rcp_f32 v122, v122
Mva 60,196,116
VM 124,124,120
Mva 76,196,120
TR 212,3,1024
Mva 92,196,124
TR 214,3,1536
s_waitcnt vmcnt(4)
s_barrier
s_waitcnt lgkmcnt(6)
Mva 32,200,128
s_mov_b32 m0, s71
Mva 48,200,132
s_mov_b32 s15, 4133376
buffer_load_dwordx4 v1, s[16:19], s15 offen offset:0 lds
Mva 64,200,136
v_rcp_f32 v123, v123
Mva 80,200,140
VM 125,125,121
VM 126,126,122
s_waitcnt lgkmcnt(4)
Mva 36,204,128
s_mov_b32 s15, 4132352
buffer_load_dwordx4 v1, s[20:23], s15 offen offset:1024 lds
Mva 52,204,132
TR 192,2,2048
VM 127,127,123
Mva 68,204,136
TR 194,2,2560
VC 112,116,117
Mva 84,204,140
s_mov_b32 s15, 4426240
buffer_load_dwordx4 v1, s[16:19], s15 offen offset:2048 lds
s_waitcnt lgkmcnt(4)
Mva 40,208,128
VC 113,118,119
VC 114,124,125
Mva 56,208,132
TR 196,2,3072
VC 115,126,127
Mva 72,208,136
s_mov_b32 s15, 4425216
buffer_load_dwordx4 v1, s[20:23], s15 offen offset:3072 lds
Mva 88,208,140
TR 198,2,3584
s_waitcnt lgkmcnt(4)
Mva 44,212,128
ds_write_b128 v5, v[112:115] offset:50176
Mva 60,212,132
TR 200,3,2048
VM 132,128,132
Mva 76,212,136
TR 202,3,2560
VM 133,129,133
Mva 92,212,140
VM 134,130,134
VM 128,8,128
s_waitcnt lgkmcnt(5)
Mva 32,192,144
TR 204,3,3072
VM 135,131,135
Mva 48,192,148
TR 206,3,3584
VM 129,8,129
Mva 64,192,152
VM 140,136,140
VM 130,8,130
Mva 80,192,156
TR 208,2,16384
s_waitcnt lgkmcnt(6)
Mva 36,196,144
TR 210,2,16896
Mva 52,196,148
v_exp_f32 v128, v128
Mva 68,196,152
VM 141,137,141
VM 131,8,131
Mva 84,196,156
TR 212,2,17408
s_waitcnt lgkmcnt(5)
Mva 40,200,144
TR 214,2,17920
Mva 56,200,148
v_exp_f32 v129, v129
Mva 72,200,152
VM 142,138,142
VM 136,8,136
Mva 88,200,156
TR 192,3,16384
s_waitcnt lgkmcnt(5)
Mva 44,204,144
TR 194,3,16896
Mva 60,204,148
v_exp_f32 v130, v130
Mva 76,204,152
VF 128,128,12,12
VM 143,139,143
Mva 92,204,156
TR 196,3,17408
VM 137,8,137
s_waitcnt vmcnt(4)
s_barrier
s_waitcnt lgkmcnt(5)
Mva 32,208,160
s_mov_b32 m0, s68
TR 198,3,17920
Mva 48,208,164
s_mov_b32 s15, 4723200
buffer_load_dwordx4 v1, s[16:19], s15 offen offset:0 lds
Mva 64,208,168
v_exp_f32 v131, v131
Mva 80,208,172
VF 129,129,12,12
TR 200,2,18432
s_waitcnt lgkmcnt(5)
Mva 36,212,160
s_mov_b32 s15, 4722176
buffer_load_dwordx4 v1, s[20:23], s15 offen offset:1024 lds
Mva 52,212,164
VM 138,8,138
TR 202,2,18944
Mva 68,212,168
v_exp_f32 v136, v136
Mva 84,212,172
s_mov_b32 s15, 5016064
buffer_load_dwordx4 v1, s[16:19], s15 offen offset:2048 lds
s_waitcnt lgkmcnt(4)
Mva 40,192,160
VF 130,130,12,12
TR 204,2,19456
Mva 56,192,164
TR 206,2,19968
Mva 72,192,168
s_mov_b32 s15, 5015040
buffer_load_dwordx4 v1, s[20:23], s15 offen offset:3072 lds
Mva 88,192,172
v_rcp_f32 v128, v128
s_waitcnt lgkmcnt(4)
Mva 44,196,160
VM 139,8,139
TR 208,3,18432
Mva 60,196,164
v_exp_f32 v137, v137
Mva 76,196,168
TR 210,3,18944
VF 131,131,12,12
Mva 92,196,172
v_rcp_f32 v129, v129
s_waitcnt lgkmcnt(4)
Mva 32,200,176
TR 212,3,19456
Mva 48,200,180
TR 214,3,19968
Mva 64,200,184
v_exp_f32 v138, v138
Mva 80,200,188
VF 136,136,12,12
TR 192,2,32768
s_waitcnt lgkmcnt(5)
Mva 36,204,176
v_rcp_f32 v130, v130
Mva 52,204,180
TR 194,2,33280
VM 132,132,128
Mva 68,204,184
v_exp_f32 v139, v139
Mva 84,204,188
TR 196,2,33792
VF 137,137,12,12
s_waitcnt lgkmcnt(5)
Mva 40,208,176
TR 198,2,34304
Mva 56,208,180
v_rcp_f32 v131, v131
Mva 72,208,184
VM 133,133,129
TR 200,3,32768
Mva 88,208,188
VF 138,138,12,12
)"
R"(TR 202,3,33280
s_waitcnt lgkmcnt(6)
Mva 44,212,176
v_rcp_f32 v136, v136
Mva 60,212,180
VM 134,134,130
VF 139,139,12,12
Mva 76,212,184
TR 204,3,33792
Mva 92,212,188
TR 206,3,34304
s_waitcnt vmcnt(4)
s_barrier
s_waitcnt lgkmcnt(6)
Mva 32,192,192
s_mov_b32 m0, s69
Mva 48,192,196
s_mov_b32 s15, 4736
buffer_load_dwordx4 v1, s[16:19], s15 offen offset:0 lds
Mva 64,192,200
v_rcp_f32 v137, v137
Mva 80,192,204
VM 135,135,131
TR 208,2,34816
s_waitcnt lgkmcnt(5)
Mva 36,196,192
s_mov_b32 s15, 3712
buffer_load_dwordx4 v1, s[20:23], s15 offen offset:1024 lds
Mva 52,196,196
TR 210,2,35328
Mva 68,196,200
v_rcp_f32 v138, v138
Mva 84,196,204
s_mov_b32 s15, 297600
buffer_load_dwordx4 v1, s[16:19], s15 offen offset:2048 lds
s_waitcnt lgkmcnt(4)
Mva 40,200,192
VM 140,140,136
TR 212,2,35840
Mva 56,200,196
v_rcp_f32 v139, v139
Mva 72,200,200
s_mov_b32 s15, 296576
buffer_load_dwordx4 v1, s[20:23], s15 offen offset:3072 lds
Mva 88,200,204
TR 214,2,36352
VM 141,141,137
s_waitcnt lgkmcnt(4)
Mva 44,204,192
VM 142,142,138
VM 143,143,139
Mva 60,204,196
TR 192,3,34816
VC 128,132,133
Mva 76,204,200
TR 194,3,35328
VC 129,134,135
Mva 92,204,204
VC 130,140,141
VC 131,142,143
s_waitcnt lgkmcnt(4)
Mva 32,208,208
TR 196,3,35840
Mva 48,208,212
TR 198,3,36352
Mva 64,208,216
ds_write_b128 v5, v[128:131] offset:51200
Mva 80,208,220
VM 148,144,148
TR 200,2,49152
s_waitcnt lgkmcnt(6)
Mva 36,212,208
VM 149,145,149
TR 202,2,49664
Mva 52,212,212
VM 150,146,150
VM 144,9,144
Mva 68,212,216
VM 151,147,151
TR 204,2,50176
Mva 84,212,220
VM 145,9,145
TR 206,2,50688
s_waitcnt lgkmcnt(7)
Mva 40,192,208
VM 156,152,156
VM 146,9,146
Mva 56,192,212
v_exp_f32 v144, v144
Mva 72,192,216
TR 208,3,49152
VM 157,153,157
Mva 88,192,220
TR 210,3,49664
VM 147,9,147
s_waitcnt lgkmcnt(7)
Mva 44,196,208
v_exp_f32 v145, v145
Mva 60,196,212
VM 158,154,158
VM 152,9,152
Mva 76,196,216
TR 212,3,50176
Mva 92,196,220
TR 214,3,50688
s_waitcnt vmcnt(4)
s_barrier
s_waitcnt lgkmcnt(6)
Mva 32,200,224
s_mov_b32 m0, s70
Mva 48,200,228
s_mov_b32 s15, 594560
buffer_load_dwordx4 v1, s[16:19], s15 offen offset:0 lds
Mva 64,200,232
TR 192,2,51200
Mva 80,200,236
v_exp_f32 v146, v146
s_waitcnt lgkmcnt(5)
Mva 36,204,224
s_mov_b32 s15, 593536
buffer_load_dwordx4 v1, s[20:23], s15 offen offset:1024 lds
Mva 52,204,228
TR 194,2,51712
VF 144,144,13,13
Mva 68,204,232
VM 159,155,159
VM 153,9,153
Mva 84,204,236
s_mov_b32 s15, 887424
buffer_load_dwordx4 v1, s[16:19], s15 offen offset:2048 lds
s_waitcnt lgkmcnt(4)
Mva 40,208,224
TR 196,2,52224
Mva 56,208,228
TR 198,2,52736
Mva 72,208,232
s_mov_b32 s15, 886400
buffer_load_dwordx4 v1, s[20:23], s15 offen offset:3072 lds
Mva 88,208,236
v_exp_f32 v147, v147
s_waitcnt lgkmcnt(4)
Mva 44,212,224
VF 145,145,13,13
VM 154,9,154
Mva 60,212,228
TR 200,3,51200
Mva 76,212,232
TR 202,3,51712
Mva 92,212,236
v_exp_f32 v152, v152
s_waitcnt lgkmcnt(4)
Mva 32,192,240
VF 146,146,13,13
TR 204,3,52224
Mva 48,192,244
v_rcp_f32 v144, v144
Mva 64,192,248
TR 206,3,52736
VM 155,9,155
Mva 80,192,252
v_exp_f32 v153, v153
s_waitcnt lgkmcnt(4)
Mva 36,196,240
TR 208,2,0
VF 147,147,13,13
Mva 52,196,244
TR 210,2,512
Mva 68,196,248
v_rcp_f32 v145, v145
Mva 84,196,252
TR 212,2,1024
s_waitcnt lgkmcnt(5)
Mva 40,200,240
v_exp_f32 v154, v154
Mva 56,200,244
TR 214,2,1536
VF 152,152,13,13
Mva 72,200,248
v_rcp_f32 v146, v146
Mva 88,200,252
TR 192,3,0
VM 148,148,144
s_waitcnt lgkmcnt(5)
Mva 44,204,240
TR 194,3,512
Mva 60,204,244
v_exp_f32 v155, v155
Mva 76,204,248
VF 153,153,13,13
TR 196,3,1024
Mva 92,204,252
TR 198,3,1536
s_waitcnt vmcnt(4)
s_barrier
s_waitcnt lgkmcnt(6)
Mvv 32,208,224
s_mov_b32 m0, s71
Mvv 48,208,228
s_mov_b32 s15, 1184384
buffer_load_dwordx4 v1, s[16:19], s15 offen offset:0 lds
Mvv 64,208,232
v_rcp_f32 v147, v147
Mvv 80,208,236
VM 149,149,145
VF 154,154,13,13
s_waitcnt lgkmcnt(4)
Mvv 36,212,224
s_mov_b32 s15, 1183360
buffer_load_dwordx4 v1, s[20:23], s15 offen offset:1024 lds
Mvv 52,212,228
TR 200,2,2048
Mvv 68,212,232
TR 202,2,2560
Mvv 84,212,236
s_mov_b32 s15, 1477248
buffer_load_dwordx4 v1, s[16:19], s15 offen offset:2048 lds
s_waitcnt lgkmcnt(4)
Mvv 40,192,224
v_rcp_f32 v152, v152
Mvv 56,192,228
VM 150,150,146
VF 155,155,13,13
Mvv 72,192,232
s_mov_b32 s15, 1476224
buffer_load_dwordx4 v1, s[20:23], s15 offen offset:3072 lds
Mvv 88,192,236
TR 204,2,3072
s_waitcnt lgkmcnt(3)
Mvv 44,196,224
TR 206,2,3584
Mvv 60,196,228
v_rcp_f32 v153, v153
Mvv 76,196,232
VM 151,151,147
TR 208,3,2048
Mvv 92,196,236
v_rcp_f32 v154, v154
s_waitcnt lgkmcnt(3)
Mvv 32,200,240
TR 210,3,2560
VM 156,156,152
Mvv 48,200,244
v_rcp_f32 v155, v155
Mvv 64,200,248
TR 212,3,3072
VM 157,157,153
Mvv 80,200,252
TR 214,3,3584
VM 158,158,154
s_waitcnt lgkmcnt(4)
Mvv 36,204,240
VM 159,159,155
VC 144,148,149
Mvv 52,204,244
TR 192,2,16384
VC 145,150,151
Mvv 68,204,248
TR 194,2,16896
VC 146,156,157
Mvv 84,204,252
VC 147,158,159
TR 196,2,17408
s_waitcnt lgkmcnt(5)
Mvv 40,208,240
TR 198,2,17920
Mvv 56,208,244
ds_write_b128 v5, v[144:147] offset:52224
Mvv 72,208,248
TR 200,3,16384
Mvv 88,208,252
TR 202,3,16896
s_waitcnt lgkmcnt(7)
Mvv 44,212,240
Mvv 60,212,244
Mvv 76,212,248
TR 204,3,17408
Mvv 92,212,252
TR 206,3,17920
s_waitcnt lgkmcnt(4)
s_waitcnt vmcnt(4)
s_barrier
Zva 96,192,0
s_mov_b32 m0, s68
Zva 112,192,4
s_mov_b32 s15, 1774208
buffer_load_dwordx4 v1, s[16:19], s15 offen offset:0 lds
Zva 128,192,8
v_add_u32 v5, 0x10000, v5
VM 36,32,36
Zva 144,192,12
VM 37,33,37
VM 38,34,38
Zva 100,196,0
s_mov_b32 s15, 1773184
buffer_load_dwordx4 v1, s[20:23], s15 offen offset:1024 lds
Zva 116,196,4
VM 32,6,32
VM 39,35,39
Zva 132,196,8
TR 208,2,18432
VM 33,6,33
Zva 148,196,12
s_mov_b32 s15, 2067072
buffer_load_dwordx4 v1, s[16:19], s15 offen offset:2048 lds
s_waitcnt lgkmcnt(3)
Zva 104,200,0
TR 210,2,18944
VM 44,40,44
Zva 120,200,4
VM 34,6,34
TR 212,2,19456
Zva 136,200,8
s_mov_b32 s15, 2066048
buffer_load_dwordx4 v1, s[20:23], s15 offen offset:3072 lds
Zva 152,200,12
v_exp_f32 v32, v32
s_waitcnt lgkmcnt(3)
Zva 108,204,0
TR 214,2,19968
VM 45,41,45
Zva 124,204,4
VM 35,6,35
TR 192,3,18432
Zva 140,204,8
v_exp_f32 v33, v33
Zva 156,204,12
TR 194,3,18944
VM 46,42,46
s_waitcnt lgkmcnt(4)
Mva 96,208,16
VM 40,6,40
TR 196,3,19456
Mva 112,208,20
v_exp_f32 v34, v34
Mva 128,208,24
TR 198,3,19968
VF 32,32,10,10
Mva 144,208,28
VM 47,43,47
VM 41,6,41
s_waitcnt lgkmcnt(4)
Mva 100,212,16
TR 200,2,32768
Mva 116,212,20
TR 202,2,33280
Mva 132,212,24
v_exp_f32 v35, v35
Mva 148,212,28
VF 33,33,10,10
VM 42,6,42
s_waitcnt lgkmcnt(4)
Mva 104,192,16
TR 204,2,33792
Mva 120,192,20
TR 206,2,34304
Mva 136,192,24
v_exp_f32 v40, v40
Mva 152,192,28
VF 34,34,10,10
TR 208,3,32768
s_waitcnt lgkmcnt(5)
Mva 108,196,16
v_rcp_f32 v32, v32
Mva 124,196,20
TR 210,3,33280
VM 43,6,43
Mva 140,196,24
v_exp_f32 v41, v41
Mva 156,196,28
TR 212,3,33792
VF 35,35,10,10
s_waitcnt vmcnt(4)
s_barrier
s_waitcnt lgkmcnt(5)
Mva 96,200,32
s_mov_b32 m0, s69
TR 214,3,34304
Mva 112,200,36
s_mov_b32 s15, 2364032
buffer_load_dwordx4 v1, s[16:19], s15 offen offset:0 lds
Mva 128,200,40
v_rcp_f32 v33, v33
Mva 144,200,44
ds_read_b128 a[0:3], v4 offset:0
s_waitcnt lgkmcnt(5)
Mva 100,204,32
s_mov_b32 s15, 2363008
buffer_load_dwordx4 v1, s[20:23], s15 offen offset:1024 lds
Mva 116,204,36
ds_read_b128 a[4:7], v4 offset:1024
ds_read_b128 a[8:11], v4 offset:2048
Mva 132,204,40
ds_read_b128 a[12:15], v4 offset:3072
Mva 148,204,44
s_mov_b32 s15, 2656896
buffer_load_dwordx4 v1, s[16:19], s15 offen offset:2048 lds
s_waitcnt lgkmcnt(6)
Mva 104,208,32
TR 192,2,34816
TR 194,2,35328
Mva 120,208,36
v_exp_f32 v42, v42
Mva 136,208,40
VF 40,40,10,10
TR 196,2,35840
Mva 152,208,44
s_mov_b32 s15, 2655872
buffer_load_dwordx4 v1, s[20:23], s15 offen offset:3072 lds
s_waitcnt lgkmcnt(7)
Mva 108,212,32
v_rcp_f32 v34, v34
Mva 124,212,36
VM 36,36,32
TR 198,2,36352
Mva 140,212,40
v_exp_f32 v43, v43
Mva 156,212,44
VF 41,41,10,10
TR 200,3,34816
s_waitcnt lgkmcnt(3)
Mva 96,192,48
TR 202,3,35328
Mva 112,192,52
v_rcp_f32 v35, v35
Mva 128,192,56
VM 37,37,33
VF 42,42,10,10
Mva 144,192,60
TR 204,3,35840
s_waitcnt lgkmcnt(3)
Mva 100,196,48
TR 206,3,36352
Mva 116,196,52
v_rcp_f32 v40, v40
Mva 132,196,56
VM 38,38,34
ds_read_b128 a[16:19], v4 offset:4096
Mva 148,196,60
ds_read_b128 a[20:23], v4 offset:5120
ds_read_b128 a[24:27], v4 offset:6144
ds_read_b128 a[28:31], v4 offset:7168
s_waitcnt lgkmcnt(6)
Mva 104,200,48
VF 43,43,10,10
TR 208,2,49152
Mva 120,200,52
TR 210,2,49664
Mva 136,200,56
v_rcp_f32 v41, v41
Mva 152,200,60
VM 39,39,35
TR 212,2,50176
s_waitcnt lgkmcnt(7)
Mva 108,204,48
v_rcp_f32 v42, v42
Mva 124,204,52
TR 214,2,50688
VM 44,44,40
Mva 140,204,56
v_rcp_f32 v43, v43
Mva 156,204,60
TR 192,3,49152
VM 45,45,41
s_waitcnt vmcnt(4)
s_barrier
s_waitcnt lgkmcnt(3)
Mva 96,208,64
s_mov_b32 m0, s70
TR 194,3,49664
Mva 112,208,68
s_mov_b32 s15, 2953856
buffer_load_dwordx4 v1, s[16:19], s15 offen offset:0 lds
Mva 128,208,72
VM 46,46,42
VM 47,47,43
Mva 144,208,76
VC 32,36,37
TR 196,3,50176
s_waitcnt lgkmcnt(3)
Mva 100,212,64
s_mov_b32 s15, 2952832
buffer_load_dwordx4 v1, s[20:23], s15 offen offset:1024 lds
Mva 116,212,68
VC 33,38,39
TR 198,3,50688
Mva 132,212,72
VC 34,44,45
VC 35,46,47
Mva 148,212,76
ds_read_b128 a[32:35], v4 offset:16384
ds_read_b128 a[36:39], v4 offset:17408
ds_read_b128 a[40:43], v4 offset:18432
ds_read_b128 a[44:47], v4 offset:19456
s_waitcnt lgkmcnt(6)
Mva 104,192,64
s_mov_b32 s15, 3246720
buffer_load_dwordx4 v1, s[16:19], s15 offen offset:2048 lds
Mva 120,192,68
TR 200,2,51200
TR 202,2,51712
Mva 136,192,72
ds_write_b128 v5, v[32:35] offset:0
Mva 152,192,76
s_mov_b32 s15, 3245696
buffer_load_dwordx4 v1, s[20:23], s15 offen offset:3072 lds
VM 52,48,52
s_waitcnt lgkmcnt(7)
Mva 108,196,64
VM 53,49,53
TR 204,2,52224
Mva 124,196,68
VM 54,50,54
TR 206,2,52736
Mva 140,196,72
VM 48,7,48
VM 55,51,55
Mva 156,196,76
VM 49,7,49
TR 208,3,51200
s_waitcnt lgkmcnt(4)
Mva 96,200,80
VM 60,56,60
TR 210,3,51712
Mva 112,200,84
VM 50,7,50
TR 212,3,52224
Mva 128,200,88
v_exp_f32 v48, v48
Mva 144,200,92
VM 61,57,61
TR 214,3,52736
s_waitcnt lgkmcnt(4)
Mva 100,204,80
VM 51,7,51
ds_read_b128 a[48:51], v4 offset:20480
Mva 116,204,84
ds_read_b128 a[52:55], v4 offset:21504
ds_read_b128 a[56:59], v4 offset:22528
Mva 132,204,88
ds_read_b128 a[60:63], v4 offset:23552
Mva 148,204,92
v_exp_f32 v49, v49
s_waitcnt lgkmcnt(6)
Mva 104,208,80
TR 192,2,0
VM 62,58,62
Mva 120,208,84
TR 194,2,512
VM 56,7,56
Mva 136,208,88
v_exp_f32 v50, v50
Mva 152,208,92
TR 196,2,1024
VF 48,48,11,11
s_waitcnt lgkmcnt(7)
Mva 108,212,80
TR 198,2,1536
VM 63,59,63
Mva 124,212,84
VM 57,7,57
TR 200,3,0
Mva 140,212,88
v_exp_f32 v51, v51
Mva 156,212,92
TR 202,3,512
VF 49,49,11,11
s_waitcnt vmcnt(4)
s_barrier
s_waitcnt lgkmcnt(4)
Mva 96,192,96
s_mov_b32 m0, s71
VM 58,7,58
Mva 112,192,100
s_mov_b32 s15, 3543680
buffer_load_dwordx4 v1, s[16:19], s15 offen offset:0 lds
Mva 128,192,104
TR 204,3,1024
Mva 144,192,108
v_exp_f32 v56, v56
s_waitcnt lgkmcnt(3)
Mva 100,196,96
s_mov_b32 s15, 3542656
buffer_load_dwordx4 v1, s[20:23], s15 offen offset:1024 lds
Mva 116,196,100
TR 206,3,1536
VF 50,50,11,11
Mva 132,196,104
ds_read_b128 a[64:67], v4 offset:32768
ds_read_b128 a[68:71], v4 offset:33792
Mva 148,196,108
ds_read_b128 a[72:75], v4 offset:34816
ds_read_b128 a[76:79], v4 offset:35840
s_waitcnt lgkmcnt(6)
Mva 104,200,96
s_mov_b32 s15, 3836544
buffer_load_dwordx4 v1, s[16:19], s15 offen offset:2048 lds
v_rcp_f32 v48, v48
Mva 120,200,100
TR 208,2,2048
VM 59,7,59
)"
R"(Mva 136,200,104
TR 210,2,2560
Mva 152,200,108
s_mov_b32 s15, 3835520
buffer_load_dwordx4 v1, s[20:23], s15 offen offset:3072 lds
v_exp_f32 v57, v57
s_waitcnt lgkmcnt(6)
Mva 108,204,96
VF 51,51,11,11
TR 212,2,3072
Mva 124,204,100
TR 214,2,3584
Mva 140,204,104
v_rcp_f32 v49, v49
Mva 156,204,108
v_exp_f32 v58, v58
s_waitcnt lgkmcnt(2)
Mva 96,208,112
TR 192,3,2048
VF 56,56,11,11
Mva 112,208,116
TR 194,3,2560
Mva 128,208,120
v_rcp_f32 v50, v50
Mva 144,208,124
VM 52,52,48
TR 196,3,3072
s_waitcnt lgkmcnt(3)
Mva 100,212,112
TR 198,3,3584
Mva 116,212,116
v_exp_f32 v59, v59
Mva 132,212,120
VF 57,57,11,11
ds_read_b128 a[80:83], v4 offset:36864
Mva 148,212,124
ds_read_b128 a[84:87], v4 offset:37888
ds_read_b128 a[88:91], v4 offset:38912
ds_read_b128 a[92:95], v4 offset:39936
s_waitcnt lgkmcnt(6)
Mva 104,192,112
TR 200,2,16384
TR 202,2,16896
Mva 120,192,116
v_rcp_f32 v51, v51
Mva 136,192,120
VM 53,53,49
VF 58,58,11,11
Mva 152,192,124
TR 204,2,17408
s_waitcnt lgkmcnt(7)
Mva 108,196,112
v_rcp_f32 v56, v56
Mva 124,196,116
TR 206,2,17920
VM 54,54,50
Mva 140,196,120
VF 59,59,11,11
TR 208,3,16384
Mva 156,196,124
v_rcp_f32 v57, v57
s_waitcnt vmcnt(4)
s_barrier
s_waitcnt lgkmcnt(3)
Mva 96,200,128
s_mov_b32 m0, s68
TR 210,3,16896
Mva 112,200,132
s_mov_b32 s15, 4133504
buffer_load_dwordx4 v1, s[16:19], s15 offen offset:0 lds
Mva 128,200,136
VM 55,55,51
TR 212,3,17408
Mva 144,200,140
v_rcp_f32 v58, v58
s_waitcnt lgkmcnt(3)
Mva 100,204,128
s_mov_b32 s15, 4132480
buffer_load_dwordx4 v1, s[20:23], s15 offen offset:1024 lds
Mva 116,204,132
VM 60,60,56
TR 214,3,17920
Mva 132,204,136
v_rcp_f32 v59, v59
Mva 148,204,140
ds_read_b128 a[96:99], v4 offset:49152
ds_read_b128 a[100:103], v4 offset:50176
ds_read_b128 a[104:107], v4 offset:51200
ds_read_b128 a[108:111], v4 offset:52224
s_waitcnt lgkmcnt(6)
Mva 104,208,128
s_mov_b32 s15, 4426368
buffer_load_dwordx4 v1, s[16:19], s15 offen offset:2048 lds
VM 61,61,57
Mva 120,208,132
TR 192,2,18432
VM 62,62,58
Mva 136,208,136
TR 194,2,18944
VM 63,63,59
Mva 152,208,140
s_mov_b32 s15, 4425344
buffer_load_dwordx4 v1, s[20:23], s15 offen offset:3072 lds
VC 48,52,53
s_waitcnt lgkmcnt(6)
Mva 108,212,128
VC 49,54,55
TR 196,2,19456
Mva 124,212,132
VC 50,60,61
TR 198,2,19968
Mva 140,212,136
VC 51,62,63
TR 200,3,18432
Mva 156,212,140
ds_write_b128 v5, v[48:51] offset:1024
s_waitcnt lgkmcnt(4)
Mva 96,192,144
TR 202,3,18944
VM 68,64,68
Mva 112,192,148
VM 69,65,69
VM 70,66,70
Mva 128,192,152
TR 204,3,19456
VM 64,8,64
Mva 144,192,156
TR 206,3,19968
VM 71,67,71
s_waitcnt lgkmcnt(5)
Mva 100,196,144
VM 65,8,65
ds_read_b128 a[112:115], v4 offset:53248
Mva 116,196,148
ds_read_b128 a[116:119], v4 offset:54272
ds_read_b128 a[120:123], v4 offset:55296
Mva 132,196,152
ds_read_b128 a[124:127], v4 offset:56320
VM 76,72,76
Mva 148,196,156
TR 208,2,32768
VM 66,8,66
s_waitcnt lgkmcnt(7)
Mva 104,200,144
TR 210,2,33280
Mva 120,200,148
v_exp_f32 v64, v64
Mva 136,200,152
VM 77,73,77
TR 212,2,33792
Mva 152,200,156
VM 67,8,67
TR 214,2,34304
s_waitcnt lgkmcnt(8)
Mva 108,204,144
v_exp_f32 v65, v65
Mva 124,204,148
VM 78,74,78
TR 192,3,32768
Mva 140,204,152
VM 72,8,72
TR 194,3,33280
Mva 156,204,156
v_exp_f32 v66, v66
s_waitcnt vmcnt(4)
s_barrier
s_waitcnt lgkmcnt(4)
Mva 96,208,160
s_mov_b32 m0, s69
VF 64,64,12,12
Mva 112,208,164
s_mov_b32 s15, 4723328
buffer_load_dwordx4 v1, s[16:19], s15 offen offset:0 lds
Mva 128,208,168
TR 196,3,33792
VM 79,75,79
Mva 144,208,172
TR 198,3,34304
VM 73,8,73
s_waitcnt lgkmcnt(4)
Mva 100,212,160
s_mov_b32 s15, 4722304
buffer_load_dwordx4 v1, s[20:23], s15 offen offset:1024 lds
Mva 116,212,164
v_exp_f32 v67, v67
Mva 132,212,168
TR 200,2,34816
VF 65,65,12,12
Mva 148,212,172
s_mov_b32 s15, 5016192
buffer_load_dwordx4 v1, s[16:19], s15 offen offset:2048 lds
s_waitcnt lgkmcnt(3)
Mva 104,192,160
TR 202,2,35328
VM 74,8,74
Mva 120,192,164
v_exp_f32 v72, v72
Mva 136,192,168
s_mov_b32 s15, 5015168
buffer_load_dwordx4 v1, s[20:23], s15 offen offset:3072 lds
Mva 152,192,172
TR 204,2,35840
VF 66,66,12,12
s_waitcnt lgkmcnt(3)
Mva 108,196,160
TR 206,2,36352
Mva 124,196,164
v_rcp_f32 v64, v64
Mva 140,196,168
VM 75,8,75
TR 208,3,34816
Mva 156,196,172
TR 210,3,35328
s_waitcnt lgkmcnt(4)
Mva 96,200,176
v_exp_f32 v73, v73
Mva 112,200,180
VF 67,67,12,12
TR 212,3,35840
Mva 128,200,184
v_rcp_f32 v65, v65
Mva 144,200,188
TR 214,3,36352
s_waitcnt lgkmcnt(4)
Mva 100,204,176
v_exp_f32 v74, v74
Mva 116,204,180
VF 72,72,12,12
TR 192,2,49152
Mva 132,204,184
TR 194,2,49664
Mva 148,204,188
v_rcp_f32 v66, v66
s_waitcnt lgkmcnt(4)
Mva 104,208,176
VM 68,68,64
TR 196,2,50176
Mva 120,208,180
v_exp_f32 v75, v75
Mva 136,208,184
TR 198,2,50688
VF 73,73,12,12
Mva 152,208,188
v_rcp_f32 v67, v67
s_waitcnt lgkmcnt(4)
Mva 108,212,176
TR 200,3,49152
VM 69,69,65
Mva 124,212,180
TR 202,3,49664
VF 74,74,12,12
Mva 140,212,184
v_rcp_f32 v72, v72
Mva 156,212,188
TR 204,3,50176
VM 70,70,66
s_waitcnt vmcnt(4)
s_barrier
s_waitcnt lgkmcnt(5)
Mva 96,192,192
s_mov_b32 m0, s70
TR 206,3,50688
Mva 112,192,196
s_mov_b32 s15, 4096
buffer_load_dwordx4 v18, s[24:27], s15 offen offset:0 lds
Mva 128,192,200
VF 75,75,12,12
TR 208,2,51200
Mva 144,192,204
v_rcp_f32 v73, v73
s_waitcnt lgkmcnt(5)
Mva 100,196,192
s_mov_b32 s15, 39936
buffer_load_dwordx4 v18, s[24:27], s15 offen offset:1024 lds
Mva 116,196,196
VM 71,71,67
TR 210,2,51712
Mva 132,196,200
v_rcp_f32 v74, v74
Mva 148,196,204
s_mov_b32 s15, 75776
buffer_load_dwordx4 v18, s[24:27], s15 offen offset:2048 lds
s_waitcnt lgkmcnt(4)
Mva 104,200,192
VM 76,76,72
TR 212,2,52224
Mva 120,200,196
TR 214,2,52736
Mva 136,200,200
s_mov_b32 s15, 111616
buffer_load_dwordx4 v18, s[24:27], s15 offen offset:3072 lds
Mva 152,200,204
v_rcp_f32 v75, v75
s_waitcnt lgkmcnt(4)
Mva 108,204,192
VM 77,77,73
VM 78,78,74
Mva 124,204,196
TR 192,3,51200
VM 79,79,75
Mva 140,204,200
TR 194,3,51712
VC 64,68,69
Mva 156,204,204
VC 65,70,71
VC 66,76,77
s_waitcnt lgkmcnt(4)
Mva 96,208,208
TR 196,3,52224
VC 67,78,79
Mva 112,208,212
TR 198,3,52736
Mva 128,208,216
ds_write_b128 v5, v[64:67] offset:2048
Mva 144,208,220
TR 200,2,0
VM 84,80,84
s_waitcnt lgkmcnt(6)
Mva 100,212,208
TR 202,2,512
VM 85,81,85
Mva 116,212,212
VM 86,82,86
VM 80,9,80
Mva 132,212,216
TR 204,2,1024
VM 87,83,87
Mva 148,212,220
TR 206,2,1536
VM 81,9,81
s_waitcnt lgkmcnt(7)
Mva 104,192,208
VM 92,88,92
VM 82,9,82
Mva 120,192,212
v_exp_f32 v80, v80
Mva 136,192,216
TR 208,3,0
VM 93,89,93
Mva 152,192,220
TR 210,3,512
VM 83,9,83
s_waitcnt lgkmcnt(7)
Mva 108,196,208
v_exp_f32 v81, v81
Mva 124,196,212
VM 94,90,94
VM 88,9,88
Mva 140,196,216
TR 212,3,1024
Mva 156,196,220
TR 214,3,1536
s_waitcnt vmcnt(4)
s_barrier
s_waitcnt lgkmcnt(6)
Mva 96,200,224
s_mov_b32 m0, s71
Mva 112,200,228
s_mov_b32 s15, 151552
buffer_load_dwordx4 v18, s[24:27], s15 offen offset:0 lds
Mva 128,200,232
TR 192,2,2048
Mva 144,200,236
TR 194,2,2560
s_waitcnt lgkmcnt(6)
Mva 100,204,224
s_mov_b32 s15, 187392
buffer_load_dwordx4 v18, s[24:27], s15 offen offset:1024 lds
Mva 116,204,228
v_exp_f32 v82, v82
Mva 132,204,232
VF 80,80,13,13
VM 95,91,95
Mva 148,204,236
s_mov_b32 s15, 223232
buffer_load_dwordx4 v18, s[24:27], s15 offen offset:2048 lds
s_waitcnt lgkmcnt(4)
Mva 104,208,224
TR 196,2,3072
VM 89,9,89
Mva 120,208,228
TR 198,2,3584
Mva 136,208,232
s_mov_b32 s15, 259072
buffer_load_dwordx4 v18, s[24:27], s15 offen offset:3072 lds
Mva 152,208,236
v_exp_f32 v83, v83
s_waitcnt lgkmcnt(4)
Mva 108,212,224
VF 81,81,13,13
TR 200,3,2048
Mva 124,212,228
VM 90,9,90
TR 202,3,2560
Mva 140,212,232
v_exp_f32 v88, v88
Mva 156,212,236
VF 82,82,13,13
TR 204,3,3072
s_waitcnt lgkmcnt(5)
Mva 96,192,240
TR 206,3,3584
Mva 112,192,244
v_rcp_f32 v80, v80
Mva 128,192,248
VM 91,9,91
TR 208,2,16384
Mva 144,192,252
v_exp_f32 v89, v89
s_waitcnt lgkmcnt(5)
Mva 100,196,240
TR 210,2,16896
VF 83,83,13,13
Mva 116,196,244
v_rcp_f32 v81, v81
Mva 132,196,248
TR 212,2,17408
Mva 148,196,252
TR 214,2,17920
s_waitcnt lgkmcnt(6)
Mva 104,200,240
v_exp_f32 v90, v90
Mva 120,200,244
VF 88,88,13,13
Mva 136,200,248
TR 192,3,16384
Mva 152,200,252
v_rcp_f32 v82, v82
s_waitcnt lgkmcnt(5)
Mva 108,204,240
TR 194,3,16896
VM 84,84,80
Mva 124,204,244
v_exp_f32 v91, v91
Mva 140,204,248
TR 196,3,17408
VF 89,89,13,13
Mva 156,204,252
TR 198,3,17920
s_waitcnt vmcnt(4)
s_barrier
s_waitcnt lgkmcnt(6)
Mvv 96,208,224
s_mov_b32 m0, s68
Mvv 112,208,228
s_mov_b32 s15, 299008
buffer_load_dwordx4 v18, s[24:27], s15 offen offset:0 lds
Mvv 128,208,232
v_rcp_f32 v83, v83
Mvv 144,208,236
VM 85,85,81
TR 200,2,18432
s_waitcnt lgkmcnt(5)
Mvv 100,212,224
s_mov_b32 s15, 334848
buffer_load_dwordx4 v18, s[24:27], s15 offen offset:1024 lds
Mvv 116,212,228
VF 90,90,13,13
TR 202,2,18944
Mvv 132,212,232
v_rcp_f32 v88, v88
Mvv 148,212,236
s_mov_b32 s15, 2176
buffer_load_dwordx4 v18, s[24:27], s15 offen offset:2048 lds
s_waitcnt lgkmcnt(4)
Mvv 104,192,224
VM 86,86,82
TR 204,2,19456
Mvv 120,192,228
VF 91,91,13,13
TR 206,2,19968
Mvv 136,192,232
s_mov_b32 s15, 38016
buffer_load_dwordx4 v18, s[24:27], s15 offen offset:3072 lds
Mvv 152,192,236
v_rcp_f32 v89, v89
s_waitcnt lgkmcnt(4)
Mvv 108,196,224
VM 87,87,83
TR 208,3,18432
Mvv 124,196,228
TR 210,3,18944
Mvv 140,196,232
v_rcp_f32 v90, v90
Mvv 156,196,236
VM 92,92,88
TR 212,3,19456
s_waitcnt lgkmcnt(5)
Mvv 96,200,240
v_rcp_f32 v91, v91
Mvv 112,200,244
TR 214,3,19968
VM 93,93,89
Mvv 128,200,248
VM 94,94,90
VM 95,95,91
Mvv 144,200,252
v_add_u32 v2, 0x10000, v19
v_add_u32 v3, 0x10000, v20
s_waitcnt lgkmcnt(4)
Mvv 100,204,240
TR 192,19,32768
VC 80,84,85
Mvv 116,204,244
TR 194,19,40960
VC 81,86,87
Mvv 132,204,248
VC 82,92,93
VC 83,94,95
Mvv 148,204,252
TR 196,20,32768
s_waitcnt lgkmcnt(5)
Mvv 104,208,240
TR 198,20,40960
Mvv 120,208,244
ds_write_b128 v5, v[80:83] offset:3072
Mvv 136,208,248
TR 200,19,33792
Mvv 152,208,252
TR 202,19,41984
s_waitcnt lgkmcnt(7)
Mvv 108,212,240
Mvv 124,212,244
Mvv 140,212,248
TR 204,20,33792
Mvv 156,212,252
TR 206,20,41984
s_waitcnt lgkmcnt(4)
s_waitcnt vmcnt(4)
s_barrier
Zva 160,192,0
s_mov_b32 m0, s69
v_add_u32 v4, 0x10000, v4
Zva 168,192,4
s_mov_b32 s15, 77952
buffer_load_dwordx4 v18, s[24:27], s15 offen offset:0 lds
Zva 176,192,8
VM 100,96,100
VM 101,97,101
Zva 184,192,12
VM 102,98,102
VM 96,6,96
Zva 164,196,0
s_mov_b32 s15, 113792
buffer_load_dwordx4 v18, s[24:27], s15 offen offset:1024 lds
Zva 172,196,4
VM 103,99,103
VM 97,6,97
Zva 180,196,8
TR 208,19,34816
VM 108,104,108
Zva 188,196,12
s_mov_b32 s15, 149632
buffer_load_dwordx4 v18, s[24:27], s15 offen offset:2048 lds
s_waitcnt lgkmcnt(3)
Mva 160,200,16
TR 210,19,43008
VM 98,6,98
Mva 168,200,20
v_exp_f32 v96, v96
Mva 176,200,24
s_mov_b32 s15, 185472
buffer_load_dwordx4 v18, s[24:27], s15 offen offset:3072 lds
Mva 184,200,28
TR 212,20,34816
VM 109,105,109
s_waitcnt lgkmcnt(3)
Mva 164,204,16
TR 214,20,43008
VM 99,6,99
Mva 172,204,20
v_exp_f32 v97, v97
Mva 180,204,24
TR 192,19,35840
VM 110,106,110
Mva 188,204,28
TR 194,19,44032
VM 104,6,104
s_waitcnt lgkmcnt(4)
Mva 160,208,32
v_exp_f32 v98, v98
Mva 168,208,36
TR 196,20,35840
VF 96,96,10,10
Mva 176,208,40
TR 198,20,44032
VM 111,107,111
Mva 184,208,44
VM 105,6,105
ds_read_b128 a[128:131], v4 offset:0
s_waitcnt lgkmcnt(5)
Mva 164,212,32
ds_read_b128 a[132:135], v4 offset:1024
Mva 172,212,36
ds_read_b128 a[136:139], v4 offset:2048
Mva 180,212,40
ds_read_b128 a[140:143], v4 offset:3072
ds_read_b128 a[144:147], v4 offset:4096
Mva 188,212,44
ds_read_b128 a[148:151], v4 offset:5120
)"
R"(ds_read_b128 a[152:155], v4 offset:6144
s_waitcnt lgkmcnt(9)
Mva 160,192,48
ds_read_b128 a[156:159], v4 offset:7168
TR 200,19,49152
Mva 168,192,52
v_exp_f32 v99, v99
Mva 176,192,56
TR 202,19,57344
VF 97,97,10,10
Mva 184,192,60
VM 106,6,106
TR 204,20,49152
s_waitcnt lgkmcnt(11)
Mva 164,196,48
v_exp_f32 v104, v104
Mva 172,196,52
s_sub_u32 s52, s56, 42991616
s_subb_u32 s53, s57, 0
s_mov_b32 s54, 37748736
Mva 180,196,56
TR 206,20,57344
VF 98,98,10,10
Mva 188,196,60
v_rcp_f32 v96, v96
s_waitcnt vmcnt(4)
s_barrier
s_waitcnt lgkmcnt(2)
Mva 160,200,64
s_mov_b32 m0, s70
TR 208,19,50176
Mva 168,200,68
VM 107,6,107
TR 210,19,58368
Mva 176,200,72
s_mov_b32 s15, 225408
buffer_load_dwordx4 v18, s[24:27], s15 offen offset:0 lds
Mva 184,200,76
v_exp_f32 v105, v105
s_waitcnt lgkmcnt(2)
Mva 164,204,64
VF 99,99,10,10
TR 212,20,50176
Mva 172,204,68
s_mov_b32 s15, 261248
buffer_load_dwordx4 v18, s[24:27], s15 offen offset:1024 lds
Mva 180,204,72
TR 214,20,58368
v_rcp_f32 v97, v97
Mva 188,204,76
s_mov_b32 s15, 297088
buffer_load_dwordx4 v18, s[24:27], s15 offen offset:2048 lds
s_waitcnt lgkmcnt(2)
Mva 160,208,80
v_exp_f32 v106, v106
Mva 168,208,84
TR 192,19,51200
VF 104,104,10,10
Mva 176,208,88
TR 194,19,59392
Mva 184,208,92
s_mov_b32 s15, 332928
buffer_load_dwordx4 v18, s[24:27], s15 offen offset:3072 lds
v_rcp_f32 v98, v98
s_waitcnt lgkmcnt(2)
Mva 164,212,80
VM 100,100,96
TR 196,20,51200
Mva 172,212,84
TR 198,20,59392
Mva 180,212,88
v_exp_f32 v107, v107
Mva 188,212,92
VF 105,105,10,10
TR 200,19,52224
s_waitcnt lgkmcnt(3)
Mva 160,192,96
v_rcp_f32 v99, v99
Mva 168,192,100
TR 202,19,60416
VM 101,101,97
Mva 176,192,104
VF 106,106,10,10
TR 204,20,52224
Mva 184,192,108
v_rcp_f32 v104, v104
s_waitcnt lgkmcnt(3)
Mva 164,196,96
TR 206,20,60416
VM 102,102,98
Mva 172,196,100
VF 107,107,10,10
TR 208,19,0
Mva 180,196,104
v_rcp_f32 v105, v105
Mva 188,196,108
TR 210,19,8192
VM 103,103,99
s_waitcnt lgkmcnt(4)
Mva 160,200,112
v_rcp_f32 v106, v106
Mva 168,200,116
TR 212,20,0
VM 108,108,104
Mva 176,200,120
TR 214,20,8192
Mva 184,200,124
v_rcp_f32 v107, v107
s_waitcnt lgkmcnt(4)
Mva 164,204,112
VM 109,109,105
TR 192,19,1024
Mva 172,204,116
VM 110,110,106
TR 194,19,9216
Mva 180,204,120
VM 111,111,107
VC 96,100,101
Mva 188,204,124
VC 97,102,103
TR 196,20,1024
s_waitcnt vmcnt(4)
s_barrier
s_waitcnt lgkmcnt(5)
Mva 160,208,128
s_mov_b32 m0, s71
VC 98,108,109
Mva 168,208,132
s_mov_b32 s15, 4352
buffer_load_dwordx4 v18, s[24:27], s15 offen offset:0 lds
Mva 176,208,136
TR 198,20,9216
VC 99,110,111
Mva 184,208,140
TR 200,19,2048
s_waitcnt lgkmcnt(5)
Mva 164,212,128
s_mov_b32 s15, 40192
buffer_load_dwordx4 v18, s[24:27], s15 offen offset:1024 lds
Mva 172,212,132
ds_write_b128 v5, v[96:99] offset:16384
Mva 180,212,136
TR 202,19,10240
VM 116,112,116
Mva 188,212,140
s_mov_b32 s15, 76032
buffer_load_dwordx4 v18, s[24:27], s15 offen offset:2048 lds
s_waitcnt lgkmcnt(5)
Mva 160,192,144
VM 117,113,117
VM 118,114,118
Mva 168,192,148
TR 204,20,2048
VM 112,7,112
Mva 176,192,152
s_mov_b32 s15, 111872
buffer_load_dwordx4 v18, s[24:27], s15 offen offset:3072 lds
Mva 184,192,156
TR 206,20,10240
VM 119,115,119
s_waitcnt lgkmcnt(5)
Mva 164,196,144
VM 113,7,113
VM 124,120,124
Mva 172,196,148
TR 208,19,3072
VM 114,7,114
Mva 180,196,152
TR 210,19,11264
Mva 188,196,156
v_exp_f32 v112, v112
s_waitcnt lgkmcnt(4)
Zva 32,200,0
VM 125,121,125
TR 212,20,3072
Zva 40,200,4
VM 115,7,115
TR 214,20,11264
Zva 48,200,8
v_exp_f32 v113, v113
Zva 56,200,12
VM 126,122,126
TR 192,19,16384
s_waitcnt lgkmcnt(5)
Zva 36,204,0
VM 120,7,120
TR 194,19,24576
Zva 44,204,4
v_exp_f32 v114, v114
Zva 52,204,8
VF 112,112,11,11
TR 196,20,16384
Zva 60,204,12
VM 127,123,127
TR 198,20,24576
s_waitcnt lgkmcnt(6)
Mva 32,208,16
VM 121,7,121
Mva 40,208,20
v_exp_f32 v115, v115
Mva 48,208,24
TR 200,19,17408
VF 113,113,11,11
Mva 56,208,28
TR 202,19,25600
VM 122,7,122
s_waitcnt lgkmcnt(6)
Mva 36,212,16
v_exp_f32 v120, v120
Mva 44,212,20
VF 114,114,11,11
Mva 52,212,24
TR 204,20,17408
Mva 60,212,28
TR 206,20,25600
s_waitcnt vmcnt(4)
s_barrier
s_waitcnt lgkmcnt(6)
Mva 32,192,32
s_mov_b32 m0, s68
Mva 40,192,36
s_mov_b32 s15, 151808
buffer_load_dwordx4 v18, s[24:27], s15 offen offset:0 lds
Mva 48,192,40
v_rcp_f32 v112, v112
Mva 56,192,44
VM 123,7,123
TR 208,19,18432
s_waitcnt lgkmcnt(5)
Mva 36,196,32
s_mov_b32 s15, 187648
buffer_load_dwordx4 v18, s[24:27], s15 offen offset:1024 lds
Mva 44,196,36
TR 210,19,26624
Mva 52,196,40
v_exp_f32 v121, v121
Mva 60,196,44
s_mov_b32 s15, 223488
buffer_load_dwordx4 v18, s[24:27], s15 offen offset:2048 lds
s_waitcnt lgkmcnt(4)
Mva 32,200,48
VF 115,115,11,11
TR 212,20,18432
Mva 40,200,52
v_rcp_f32 v113, v113
Mva 48,200,56
s_mov_b32 s15, 259328
buffer_load_dwordx4 v18, s[24:27], s15 offen offset:3072 lds
Mva 56,200,60
TR 214,20,26624
s_waitcnt lgkmcnt(4)
Mva 36,204,48
v_exp_f32 v122, v122
Mva 44,204,52
VF 120,120,11,11
TR 192,19,19456
Mva 52,204,56
TR 194,19,27648
Mva 60,204,60
v_rcp_f32 v114, v114
s_waitcnt lgkmcnt(4)
Mva 32,208,64
VM 116,116,112
TR 196,20,19456
Mva 40,208,68
v_exp_f32 v123, v123
Mva 48,208,72
TR 198,20,27648
VF 121,121,11,11
Mva 56,208,76
v_rcp_f32 v115, v115
s_waitcnt lgkmcnt(4)
Mva 36,212,64
TR 200,19,32768
VM 117,117,113
Mva 44,212,68
TR 202,19,40960
VF 122,122,11,11
Mva 52,212,72
v_rcp_f32 v120, v120
Mva 60,212,76
TR 204,20,32768
VM 118,118,114
s_waitcnt lgkmcnt(5)
Mva 32,192,80
TR 206,20,40960
VF 123,123,11,11
Mva 40,192,84
v_rcp_f32 v121, v121
Mva 48,192,88
TR 208,19,33792
VM 119,119,115
Mva 56,192,92
TR 210,19,41984
s_waitcnt lgkmcnt(6)
Mva 36,196,80
v_rcp_f32 v122, v122
Mva 44,196,84
VM 124,124,120
Mva 52,196,88
TR 212,20,33792
Mva 60,196,92
TR 214,20,41984
s_waitcnt vmcnt(4)
s_barrier
s_waitcnt lgkmcnt(6)
Mva 32,200,96
s_mov_b32 m0, s69
Mva 40,200,100
s_mov_b32 s15, 299264
buffer_load_dwordx4 v18, s[24:27], s15 offen offset:0 lds
Mva 48,200,104
v_rcp_f32 v123, v123
Mva 56,200,108
VM 125,125,121
VM 126,126,122
s_waitcnt lgkmcnt(4)
Mva 36,204,96
s_mov_b32 s15, 335104
buffer_load_dwordx4 v18, s[24:27], s15 offen offset:1024 lds
Mva 44,204,100
TR 192,19,34816
VM 127,127,123
Mva 52,204,104
TR 194,19,43008
VC 112,116,117
Mva 60,204,108
s_mov_b32 s15, 2560
buffer_load_dwordx4 v18, s[24:27], s15 offen offset:2048 lds
s_waitcnt lgkmcnt(4)
Mva 32,208,112
VC 113,118,119
VC 114,124,125
Mva 40,208,116
TR 196,20,34816
VC 115,126,127
Mva 48,208,120
s_mov_b32 s15, 38400
buffer_load_dwordx4 v18, s[24:27], s15 offen offset:3072 lds
Mva 56,208,124
TR 198,20,43008
s_waitcnt lgkmcnt(4)
Mva 36,212,112
ds_write_b128 v5, v[112:115] offset:17408
Mva 44,212,116
TR 200,19,35840
VM 132,128,132
Mva 52,212,120
TR 202,19,44032
VM 133,129,133
Mva 60,212,124
VM 134,130,134
VM 128,8,128
s_waitcnt lgkmcnt(5)
Mva 32,192,128
TR 204,20,35840
VM 135,131,135
Mva 40,192,132
TR 206,20,44032
VM 129,8,129
Mva 48,192,136
VM 140,136,140
VM 130,8,130
Mva 56,192,140
TR 208,19,49152
s_waitcnt lgkmcnt(6)
Mva 36,196,128
TR 210,19,57344
Mva 44,196,132
v_exp_f32 v128, v128
Mva 52,196,136
VM 141,137,141
VM 131,8,131
Mva 60,196,140
TR 212,20,49152
s_waitcnt lgkmcnt(5)
Mva 32,200,144
TR 214,20,57344
Mva 40,200,148
v_exp_f32 v129, v129
Mva 48,200,152
VM 142,138,142
VM 136,8,136
Mva 56,200,156
TR 192,19,50176
s_waitcnt lgkmcnt(5)
Mva 36,204,144
TR 194,19,58368
Mva 44,204,148
v_exp_f32 v130, v130
Mva 52,204,152
VF 128,128,12,12
VM 143,139,143
Mva 60,204,156
TR 196,20,50176
VM 137,8,137
s_waitcnt vmcnt(4)
s_barrier
s_waitcnt lgkmcnt(5)
Zva 64,208,0
s_mov_b32 m0, s70
TR 198,20,58368
Zva 72,208,4
s_mov_b32 s15, 78336
buffer_load_dwordx4 v18, s[24:27], s15 offen offset:0 lds
Zva 80,208,8
v_exp_f32 v131, v131
Zva 88,208,12
VF 129,129,12,12
TR 200,19,51200
s_waitcnt lgkmcnt(5)
Zva 68,212,0
s_mov_b32 s15, 114176
buffer_load_dwordx4 v18, s[24:27], s15 offen offset:1024 lds
Zva 76,212,4
VM 138,8,138
TR 202,19,59392
Zva 84,212,8
v_exp_f32 v136, v136
Zva 92,212,12
s_mov_b32 s15, 150016
buffer_load_dwordx4 v18, s[24:27], s15 offen offset:2048 lds
s_waitcnt lgkmcnt(4)
Mva 64,192,16
VF 130,130,12,12
TR 204,20,51200
Mva 72,192,20
TR 206,20,59392
Mva 80,192,24
s_mov_b32 s15, 185856
buffer_load_dwordx4 v18, s[24:27], s15 offen offset:3072 lds
Mva 88,192,28
v_rcp_f32 v128, v128
s_waitcnt lgkmcnt(4)
Mva 68,196,16
VM 139,8,139
TR 208,19,52224
Mva 76,196,20
v_exp_f32 v137, v137
Mva 84,196,24
TR 210,19,60416
VF 131,131,12,12
Mva 92,196,28
v_rcp_f32 v129, v129
s_waitcnt lgkmcnt(4)
Mva 64,200,32
TR 212,20,52224
Mva 72,200,36
TR 214,20,60416
Mva 80,200,40
v_exp_f32 v138, v138
Mva 88,200,44
VF 136,136,12,12
TR 192,19,0
s_waitcnt lgkmcnt(5)
Mva 68,204,32
v_rcp_f32 v130, v130
Mva 76,204,36
TR 194,19,8192
VM 132,132,128
Mva 84,204,40
v_exp_f32 v139, v139
Mva 92,204,44
TR 196,20,0
VF 137,137,12,12
s_waitcnt lgkmcnt(5)
Mva 64,208,48
TR 198,20,8192
Mva 72,208,52
v_rcp_f32 v131, v131
Mva 80,208,56
VM 133,133,129
TR 200,19,1024
Mva 88,208,60
VF 138,138,12,12
TR 202,19,9216
s_waitcnt lgkmcnt(6)
Mva 68,212,48
v_rcp_f32 v136, v136
Mva 76,212,52
VM 134,134,130
VF 139,139,12,12
Mva 84,212,56
TR 204,20,1024
Mva 92,212,60
TR 206,20,9216
s_waitcnt vmcnt(4)
s_barrier
s_waitcnt lgkmcnt(6)
Mva 64,192,64
s_mov_b32 m0, s71
Mva 72,192,68
s_mov_b32 s15, 225792
buffer_load_dwordx4 v18, s[24:27], s15 offen offset:0 lds
Mva 80,192,72
v_rcp_f32 v137, v137
Mva 88,192,76
VM 135,135,131
TR 208,19,2048
s_waitcnt lgkmcnt(5)
Mva 68,196,64
s_mov_b32 s15, 261632
buffer_load_dwordx4 v18, s[24:27], s15 offen offset:1024 lds
Mva 76,196,68
TR 210,19,10240
Mva 84,196,72
v_rcp_f32 v138, v138
Mva 92,196,76
s_mov_b32 s15, 297472
buffer_load_dwordx4 v18, s[24:27], s15 offen offset:2048 lds
s_waitcnt lgkmcnt(4)
Mva 64,200,80
VM 140,140,136
TR 212,20,2048
Mva 72,200,84
v_rcp_f32 v139, v139
Mva 80,200,88
s_mov_b32 s15, 333312
buffer_load_dwordx4 v18, s[24:27], s15 offen offset:3072 lds
Mva 88,200,92
TR 214,20,10240
VM 141,141,137
s_waitcnt lgkmcnt(4)
Mva 68,204,80
VM 142,142,138
VM 143,143,139
Mva 76,204,84
TR 192,19,3072
VC 128,132,133
Mva 84,204,88
TR 194,19,11264
VC 129,134,135
Mva 92,204,92
VC 130,140,141
VC 131,142,143
s_waitcnt lgkmcnt(4)
Mva 64,208,96
TR 196,20,3072
Mva 72,208,100
TR 198,20,11264
Mva 80,208,104
ds_write_b128 v5, v[128:131] offset:18432
Mva 88,208,108
VM 148,144,148
TR 200,19,16384
s_waitcnt lgkmcnt(6)
Mva 68,212,96
VM 149,145,149
TR 202,19,24576
Mva 76,212,100
VM 150,146,150
VM 144,9,144
Mva 84,212,104
VM 151,147,151
TR 204,20,16384
Mva 92,212,108
VM 145,9,145
TR 206,20,24576
s_waitcnt lgkmcnt(7)
Mva 64,192,112
VM 156,152,156
VM 146,9,146
Mva 72,192,116
v_exp_f32 v144, v144
Mva 80,192,120
TR 208,19,17408
VM 157,153,157
Mva 88,192,124
TR 210,19,25600
VM 147,9,147
s_waitcnt lgkmcnt(7)
Mva 68,196,112
v_exp_f32 v145, v145
Mva 76,196,116
VM 158,154,158
VM 152,9,152
Mva 84,196,120
TR 212,20,17408
Mva 92,196,124
TR 214,20,25600
s_waitcnt vmcnt(4)
s_barrier
s_waitcnt lgkmcnt(6)
Mva 64,200,128
s_mov_b32 m0, s68
Mva 72,200,132
s_mov_b32 s15, 372736
buffer_load_dwordx4 v18, s[24:27], s15 offen offset:0 lds
Mva 80,200,136
TR 192,19,18432
Mva 88,200,140
v_exp_f32 v146, v146
s_waitcnt lgkmcnt(5)
Mva 68,204,128
s_mov_b32 s15, 408576
buffer_load_dwordx4 v18, s[24:27], s15 offen offset:1024 lds
Mva 76,204,132
TR 194,19,26624
VF 144,144,13,13
Mva 84,204,136
)"
R"(VM 159,155,159
VM 153,9,153
Mva 92,204,140
s_mov_b32 s15, 370816
buffer_load_dwordx4 v18, s[24:27], s15 offen offset:2048 lds
s_waitcnt lgkmcnt(4)
Mva 64,208,144
TR 196,20,18432
Mva 72,208,148
TR 198,20,26624
Mva 80,208,152
s_mov_b32 s15, 406656
buffer_load_dwordx4 v18, s[24:27], s15 offen offset:3072 lds
Mva 88,208,156
v_exp_f32 v147, v147
s_waitcnt lgkmcnt(4)
Mva 68,212,144
VF 145,145,13,13
VM 154,9,154
Mva 76,212,148
TR 200,19,19456
Mva 84,212,152
TR 202,19,27648
Mva 92,212,156
v_exp_f32 v152, v152
s_waitcnt lgkmcnt(4)
Zva 224,192,0
VF 146,146,13,13
TR 204,20,19456
Zva 232,192,4
v_rcp_f32 v144, v144
Zva 240,192,8
TR 206,20,27648
VM 155,9,155
Zva 248,192,12
v_exp_f32 v153, v153
s_waitcnt lgkmcnt(4)
Zva 228,196,0
TR 208,19,32768
VF 147,147,13,13
Zva 236,196,4
TR 210,19,40960
Zva 244,196,8
v_rcp_f32 v145, v145
Zva 252,196,12
TR 212,20,32768
s_waitcnt lgkmcnt(5)
Mva 224,200,16
v_exp_f32 v154, v154
Mva 232,200,20
TR 214,20,40960
VF 152,152,13,13
Mva 240,200,24
v_rcp_f32 v146, v146
Mva 248,200,28
TR 192,19,33792
VM 148,148,144
s_waitcnt lgkmcnt(5)
Mva 228,204,16
TR 194,19,41984
Mva 236,204,20
v_exp_f32 v155, v155
Mva 244,204,24
VF 153,153,13,13
TR 196,20,33792
Mva 252,204,28
TR 198,20,41984
s_waitcnt vmcnt(4)
s_barrier
s_waitcnt lgkmcnt(6)
Mva 224,208,32
s_mov_b32 m0, s69
Mva 232,208,36
s_mov_b32 s15, 372992
buffer_load_dwordx4 v18, s[24:27], s15 offen offset:0 lds
Mva 240,208,40
v_rcp_f32 v147, v147
Mva 248,208,44
VM 149,149,145
VF 154,154,13,13
s_waitcnt lgkmcnt(4)
Mva 228,212,32
s_mov_b32 s15, 408832
buffer_load_dwordx4 v18, s[24:27], s15 offen offset:1024 lds
Mva 236,212,36
TR 200,19,34816
Mva 244,212,40
TR 202,19,43008
Mva 252,212,44
s_mov_b32 s15, 371200
buffer_load_dwordx4 v18, s[24:27], s15 offen offset:2048 lds
s_waitcnt lgkmcnt(4)
Mva 224,192,48
v_rcp_f32 v152, v152
Mva 232,192,52
VM 150,150,146
VF 155,155,13,13
Mva 240,192,56
s_mov_b32 s15, 407040
buffer_load_dwordx4 v18, s[24:27], s15 offen offset:3072 lds
Mva 248,192,60
TR 204,20,34816
s_waitcnt lgkmcnt(3)
Mva 228,196,48
TR 206,20,43008
Mva 236,196,52
v_rcp_f32 v153, v153
Mva 244,196,56
VM 151,151,147
TR 208,19,35840
Mva 252,196,60
v_rcp_f32 v154, v154
s_waitcnt lgkmcnt(3)
Mva 224,200,64
TR 210,19,44032
VM 156,156,152
Mva 232,200,68
v_rcp_f32 v155, v155
Mva 240,200,72
TR 212,20,35840
VM 157,157,153
Mva 248,200,76
TR 214,20,44032
VM 158,158,154
s_waitcnt lgkmcnt(4)
Mva 228,204,64
VM 159,159,155
VC 144,148,149
Mva 236,204,68
TR 192,19,49152
VC 145,150,151
Mva 244,204,72
TR 194,19,57344
VC 146,156,157
Mva 252,204,76
VC 147,158,159
TR 196,20,49152
s_waitcnt lgkmcnt(5)
Mva 224,208,80
TR 198,20,57344
Mva 232,208,84
ds_write_b128 v5, v[144:147] offset:19456
Mva 240,208,88
TR 200,19,50176
Mva 248,208,92
TR 202,19,58368
s_waitcnt lgkmcnt(7)
Mva 228,212,80
Mva 236,212,84
Mva 244,212,88
TR 204,20,50176
Mva 252,212,92
TR 206,20,58368
s_waitcnt lgkmcnt(4)
s_waitcnt vmcnt(4)
s_barrier
Mva 224,192,96
s_mov_b32 m0, s70
ds_read_b128 a[160:163], v4 offset:16384
Mva 232,192,100
s_mov_b32 s15, 4480
buffer_load_dwordx4 v18, s[24:27], s15 offen offset:0 lds
Mva 240,192,104
TR 208,19,51200
Mva 248,192,108
TR 210,19,59392
Mva 228,196,96
s_mov_b32 s15, 40320
buffer_load_dwordx4 v18, s[24:27], s15 offen offset:1024 lds
Mva 236,196,100
ds_read_b128 a[164:167], v4 offset:17408
Mva 244,196,104
TR 212,20,51200
Mva 252,196,108
s_mov_b32 s15, 76160
buffer_load_dwordx4 v18, s[24:27], s15 offen offset:2048 lds
s_waitcnt lgkmcnt(7)
Mva 224,200,112
TR 214,20,59392
Mva 232,200,116
ds_read_b128 a[168:171], v4 offset:18432
Mva 240,200,120
s_mov_b32 s15, 112000
buffer_load_dwordx4 v18, s[24:27], s15 offen offset:3072 lds
Mva 248,200,124
TR 192,19,52224
s_waitcnt lgkmcnt(8)
Mva 228,204,112
TR 194,19,60416
Mva 236,204,116
ds_read_b128 a[172:175], v4 offset:19456
Mva 244,204,120
TR 196,20,52224
Mva 252,204,124
TR 198,20,60416
s_waitcnt lgkmcnt(9)
Mva 224,208,128
ds_read_b128 a[176:179], v4 offset:20480
Mva 232,208,132
ds_read_b128 a[180:183], v4 offset:21504
Mva 240,208,136
ds_read_b128 a[184:187], v4 offset:22528
Mva 248,208,140
ds_read_b128 a[188:191], v4 offset:23552
s_waitcnt lgkmcnt(10)
Mva 228,212,128
TR 200,19,0
Mva 236,212,132
TR 202,19,8192
Mva 244,212,136
TR 204,20,0
Mva 252,212,140
TR 206,20,8192
s_waitcnt lgkmcnt(11)
Mva 224,192,144
Mva 232,192,148
Mva 240,192,152
TR 208,19,1024
Mva 248,192,156
TR 210,19,9216
s_waitcnt lgkmcnt(10)
Mva 228,196,144
Mva 236,196,148
Mva 244,196,152
TR 212,20,1024
Mva 252,196,156
TR 214,20,9216
s_waitcnt vmcnt(4)
s_barrier
s_waitcnt lgkmcnt(6)
Mva 160,200,160
s_mov_b32 m0, s71
Mva 168,200,164
s_mov_b32 s15, 151936
buffer_load_dwordx4 v18, s[24:27], s15 offen offset:0 lds
Mva 176,200,168
TR 192,19,2048
Mva 184,200,172
TR 194,19,10240
s_waitcnt lgkmcnt(6)
Mva 164,204,160
s_mov_b32 s15, 187776
buffer_load_dwordx4 v18, s[24:27], s15 offen offset:1024 lds
Mva 172,204,164
Mva 180,204,168
TR 196,20,2048
Mva 188,204,172
s_mov_b32 s15, 223616
buffer_load_dwordx4 v18, s[24:27], s15 offen offset:2048 lds
s_waitcnt lgkmcnt(5)
Mva 160,208,176
TR 198,20,10240
Mva 168,208,180
Mva 176,208,184
s_mov_b32 s15, 259456
buffer_load_dwordx4 v18, s[24:27], s15 offen offset:3072 lds
Mva 184,208,188
TR 200,19,3072
s_waitcnt lgkmcnt(5)
Mva 164,212,176
TR 202,19,11264
Mva 172,212,180
Mva 180,212,184
TR 204,20,3072
Mva 188,212,188
TR 206,20,11264
s_waitcnt lgkmcnt(6)
Mva 32,192,160
Mva 40,192,164
Mva 48,192,168
VC 160,160,161
VC 161,162,163
Mva 56,192,172
VC 162,164,165
VC 163,166,167
s_waitcnt lgkmcnt(4)
Mva 36,196,160
s_mov_b32 s67, 0
buffer_store_dwordx4 v[160:163], v21, s[52:55], s67 offen sc1
Mva 44,196,164
TR 208,19,16384
VC 168,168,169
Mva 52,196,168
TR 210,19,24576
VC 169,170,171
Mva 60,196,172
VC 170,172,173
VC 171,174,175
s_waitcnt lgkmcnt(4)
Mva 32,200,176
TR 212,20,16384
Mva 40,200,180
TR 214,20,24576
Mva 48,200,184
s_mov_b32 s67, 9216
buffer_store_dwordx4 v[168:171], v21, s[52:55], s67 offen sc1
Mva 56,200,188
VC 176,176,177
VC 177,178,179
s_waitcnt lgkmcnt(4)
Mva 36,204,176
TR 192,19,17408
VC 178,180,181
Mva 44,204,180
TR 194,19,25600
VC 179,182,183
Mva 52,204,184
s_mov_b32 s67, 18432
buffer_store_dwordx4 v[176:179], v21, s[52:55], s67 offen sc1
Mva 60,204,188
TR 196,20,17408
VC 184,184,185
s_waitcnt vmcnt(7)
s_barrier
s_waitcnt lgkmcnt(5)
Mva 64,208,160
s_mov_b32 m0, s68
TR 198,20,25600
Mva 72,208,164
s_mov_b32 s15, 299392
buffer_load_dwordx4 v18, s[24:27], s15 offen offset:0 lds
Mva 80,208,168
VC 185,186,187
VC 186,188,189
Mva 88,208,172
VC 187,190,191
TR 200,19,18432
s_waitcnt lgkmcnt(5)
Mva 68,212,160
s_mov_b32 s15, 335232
buffer_load_dwordx4 v18, s[24:27], s15 offen offset:1024 lds
Mva 76,212,164
TR 202,19,26624
Mva 84,212,168
s_mov_b32 s67, 27648
buffer_store_dwordx4 v[184:187], v21, s[52:55], s67 offen sc1
Mva 92,212,172
s_mov_b32 s15, 371072
buffer_load_dwordx4 v18, s[24:27], s15 offen offset:2048 lds
s_waitcnt lgkmcnt(4)
Mva 64,192,176
TR 204,20,18432
Mva 72,192,180
TR 206,20,26624
Mva 80,192,184
s_mov_b32 s15, 406912
buffer_load_dwordx4 v18, s[24:27], s15 offen offset:3072 lds
Mva 88,192,188
VC 32,32,33
VC 33,34,35
s_waitcnt lgkmcnt(4)
Mva 68,196,176
VC 34,36,37
s_load_dwordx4 s[72:75], s[8:9], 96
Mva 76,196,180
VC 35,38,39
v_and_b32 v180, 7, v0
Mva 84,196,184
TR 208,19,19456
v_lshrrev_b32 v181, 5, v0
Mva 92,196,188
s_mov_b32 s67, 1024
buffer_store_dwordx4 v[32:35], v21, s[52:55], s67 offen sc1
s_waitcnt lgkmcnt(0)
Mva 224,200,160
TR 210,19,27648
VC 40,40,41
Mva 232,200,164
VC 41,42,43
v_bfe_u32 v182, v0, 3, 2
Mva 240,200,168
VC 42,44,45
TR 212,20,19456
Mva 248,200,172
VC 43,46,47
v_and_b32 v183, 4, v0
Mva 228,204,160
TR 214,20,27648
v_and_b32 v184, 3, v0
Mva 236,204,164
s_mov_b32 s67, 10240
buffer_store_dwordx4 v[40:43], v21, s[52:55], s67 offen sc1
Mva 244,204,168
VC 48,48,49
TR 192,19,32768
Mva 252,204,172
VC 49,50,51
v_lshl_add_u32 v183, v183, 2, v184
s_waitcnt lgkmcnt(3)
Mva 224,208,176
TR 194,19,40960
VC 50,52,53
Mva 232,208,180
s_lshl_b32 s76, s64, 3
VC 51,54,55
Mva 240,208,184
v_lshl_add_u32 v188, v182, 4, v180
TR 196,20,32768
Mva 248,208,188
s_mov_b32 s67, 19456
buffer_store_dwordx4 v[48:51], v21, s[52:55], s67 offen sc1
s_waitcnt lgkmcnt(3)
Mva 228,212,176
TR 198,20,40960
VC 56,56,57
Mva 236,212,180
v_add_lshl_u32 v188, v188, s76, 4
VC 57,58,59
Mva 244,212,184
VC 58,60,61
v_mul_u32_u24 v185, 147456, v181
Mva 252,212,188
TR 200,19,33792
VC 59,62,63
s_waitcnt vmcnt(11)
s_barrier
s_waitcnt lgkmcnt(3)
Zva 96,192,0
s_mov_b32 m0, s69
TR 202,19,41984
Zva 104,192,4
s_mov_b32 s15, 4736
buffer_load_dwordx4 v18, s[24:27], s15 offen offset:0 lds
Zva 112,192,8
v_add_u32 v188, v185, v188
s_lshr_b32 s90, s13, 1
s_and_b32 s91, s13, 1
Zva 120,192,12
s_mov_b32 s67, 28672
buffer_store_dwordx4 v[56:59], v21, s[52:55], s67 offen sc1
s_waitcnt lgkmcnt(2)
Zva 100,196,0
VC 64,64,65
s_lshr_b32 s77, s13, 3
s_lshl_b32 s93, s14, 6
Zva 108,196,4
s_mov_b32 s15, 40576
buffer_load_dwordx4 v18, s[24:27], s15 offen offset:1024 lds
TR 204,20,33792
VC 65,66,67
Zva 116,196,8
TR 206,20,41984
VC 66,68,69
s_add_u32 s93, s93, s77
Zva 124,196,12
VC 67,70,71
s_add_u32 s93, s93, s91
s_add_u32 s93, s93, s91
s_waitcnt lgkmcnt(2)
Mva 96,200,16
s_mov_b32 s15, 76416
buffer_load_dwordx4 v18, s[24:27], s15 offen offset:2048 lds
Mva 104,200,20
TR 208,19,34816
s_lshl_b32 s93, s93, 2
s_and_b32 s92, s90, 3
Mva 112,200,24
TR 210,19,43008
s_mov_b32 s67, 2048
buffer_store_dwordx4 v[64:67], v21, s[52:55], s67 offen sc1
Mva 120,200,28
s_mov_b32 s15, 112256
buffer_load_dwordx4 v18, s[24:27], s15 offen offset:3072 lds
VC 72,72,73
s_add_u32 s93, s93, s92
s_waitcnt lgkmcnt(2)
Mva 100,204,16
VC 73,74,75
s_mul_i32 s93, s93, 9
s_add_u32 s93, s93, s65
Mva 108,204,20
TR 212,20,34816
VC 74,76,77
Mva 116,204,24
TR 214,20,43008
VC 75,78,79
s_lshl_b32 s93, s93, 10
Mva 124,204,28
s_mov_b32 s67, 11264
buffer_store_dwordx4 v[72:75], v21, s[52:55], s67 offen sc1
s_waitcnt lgkmcnt(2)
Mva 96,208,32
v_add_u32 v188, s93, v188
TR 192,19,35840
Mva 104,208,36
VC 80,80,81
s_lshl_b32 s93, s77, 4
s_and_b32 s90, s90, 3
Mva 112,208,40
TR 194,19,44032
VC 81,82,83
Mva 120,208,44
VC 82,84,85
s_add_u32 s93, s93, s90
s_add_u32 s93, s93, s76
s_waitcnt lgkmcnt(2)
Mva 100,212,32
VC 83,86,87
TR 196,20,35840
Mva 108,212,36
s_lshl_b32 s93, s93, 2
TR 198,20,44032
Mva 116,212,40
s_mov_b32 s67, 20480
buffer_store_dwordx4 v[80:83], v21, s[52:55], s67 offen sc1
Mva 124,212,44
VC 88,88,89
v_add_u32 v183, s93, v183
s_waitcnt lgkmcnt(2)
Mva 96,192,48
VC 89,90,91
TR 200,19,49152
Mva 104,192,52
VC 90,92,93
v_and_b32 v185, 16, v0
Mva 112,192,56
TR 202,19,57344
VC 91,94,95
Mva 120,192,60
v_lshl_add_u32 v185, v181, 3, v185
TR 204,20,49152
s_waitcnt lgkmcnt(3)
Mva 100,196,48
s_mov_b32 s67, 29696
buffer_store_dwordx4 v[88:91], v21, s[52:55], s67 offen sc1
Mva 108,196,52
v_and_b32 v182, 1, v182
TR 206,20,57344
Mva 116,196,56
v_lshl_add_u32 v185, v182, 2, v185
s_lshl_b32 s93, s65, 1
s_add_u32 s93, s93, s91
Mva 124,196,60
TR 208,19,50176
s_lshl_b32 s93, s93, 5
s_waitcnt vmcnt(12)
s_barrier
s_waitcnt lgkmcnt(3)
Mva 96,200,64
s_mov_b32 m0, s70
TR 210,19,58368
Mva 104,200,68
s_mov_b32 s15, 152192
buffer_load_dwordx4 v18, s[24:27], s15 offen offset:0 lds
Mva 112,200,72
v_add_lshl_u32 v185, v185, s93, 1
s_lshl_b32 s93, s14, 7
Mva 120,200,76
TR 212,20,50176
v_add_u32 v184, s93, v183
s_waitcnt lgkmcnt(3)
Mva 100,204,64
s_mov_b32 s15, 188032
buffer_load_dwordx4 v18, s[24:27], s15 offen offset:1024 lds
Mva 108,204,68
TR 214,20,58368
v_mul_u32_u24 v184, 1152, v184
Mva 116,204,72
TR 192,19,51200
v_add_u32 v184, v184, v185
Mva 124,204,76
s_mov_b32 s15, 223872
buffer_load_dwordx4 v18, s[24:27], s15 offen offset:2048 lds
s_waitcnt lgkmcnt(3)
Mva 96,208,80
TR 194,19,59392
v_lshlrev_b32 v189, 1, v184
Mva 104,208,84
TR 196,20,51200
v_lshlrev_b32 v183, 3, v183
Mva 112,208,88
s_mov_b32 s15, 259712
buffer_load_dwordx4 v18, s[24:27], s15 offen offset:3072 lds
Mva 120,208,92
TR 198,20,59392
s_lshl_b32 s91, s91, 1
s_add_u32 s91, s91, s77
s_waitcnt lgkmcnt(4)
Mva 100,212,80
TR 200,19,52224
s_lshl_b32 s93, s14, 2
s_add_u32 s91, s91, s93
Mva 108,212,84
TR 202,19,60416
s_lshl_b32 s91, s91, 9
s_add_u32 s91, s91, 163840
Mva 116,212,88
TR 204,20,52224
v_lshl_add_u32 v190, v0, 3, s91
Mva 124,212,92
TR 206,20,60416
s_add_u32 s93, s93, s11
s_lshl_b32 s93, s93, 6
s_waitcnt lgkmcnt(6)
Mva 96,192,96
s_add_u32 s93, s93, s13
s_lshl_b32 s93, s93, 3
s_add_u32 s93, s93, 163840
Mva 104,192,100
v_bfrev_b32 v191, 1
)"
R"(v_mov_b32 v186, s93
Mva 112,192,104
TR 208,19,0
v_cmp_eq_u32 vcc, 0, v0
Mva 120,192,108
TR 210,19,8192
v_cndmask_b32 v191, v191, v186, vcc
s_waitcnt lgkmcnt(6)
Mva 100,196,96
v_mov_b32 v186, s100
v_mov_b32 v187, s100
Mva 108,196,100
s_mov_b32 s92, 0x40000
s_mov_b32 s16, s74
s_mov_b32 s17, s75
s_mov_b32 s18, 4718592
Mva 116,196,104
TR 212,20,0
s_mov_b32 s19, 0x20000
s_mov_b32 s20, s72
Mva 124,196,108
TR 214,20,8192
s_mov_b32 s21, s73
s_mov_b32 s22, 1152
s_waitcnt lgkmcnt(6)
Mva 96,200,112
s_mov_b32 s23, 0x20000
Mva 104,200,116
Mva 112,200,120
TR 192,19,1024
Mva 120,200,124
TR 194,19,9216
s_waitcnt lgkmcnt(6)
Mva 100,204,112
Mva 108,204,116
Mva 116,204,120
TR 196,20,1024
Mva 124,204,124
TR 198,20,9216
s_waitcnt vmcnt(7)
s_barrier
s_waitcnt lgkmcnt(6)
Mva 96,208,128
s_mov_b32 m0, s71
Mva 104,208,132
s_mov_b32 s15, 299648
buffer_load_dwordx4 v18, s[24:27], s15 offen offset:0 lds
Mva 112,208,136
TR 200,19,2048
Mva 120,208,140
TR 202,19,10240
s_waitcnt lgkmcnt(6)
Mva 100,212,128
s_mov_b32 s15, 335488
buffer_load_dwordx4 v18, s[24:27], s15 offen offset:1024 lds
Mva 108,212,132
Mva 116,212,136
TR 204,20,2048
Mva 124,212,140
s_mov_b32 s15, 371328
buffer_load_dwordx4 v18, s[24:27], s15 offen offset:2048 lds
s_waitcnt lgkmcnt(5)
Mva 96,192,144
TR 206,20,10240
Mva 104,192,148
Mva 112,192,152
s_mov_b32 s15, 407168
buffer_load_dwordx4 v18, s[24:27], s15 offen offset:3072 lds
Mva 120,192,156
TR 208,19,3072
s_waitcnt lgkmcnt(5)
Mva 100,196,144
TR 210,19,11264
Mva 108,196,148
Mva 116,196,152
TR 212,20,3072
Mva 124,196,156
TR 214,20,11264
s_waitcnt lgkmcnt(6)
Mva 96,200,160
Mva 104,200,164
Mva 112,200,168
TR 192,19,16384
Mva 120,200,172
TR 194,19,24576
s_waitcnt lgkmcnt(6)
Mva 100,204,160
Mva 108,204,164
Mva 116,204,168
TR 196,20,16384
Mva 124,204,172
TR 198,20,24576
s_waitcnt lgkmcnt(6)
Mva 96,208,176
Mva 104,208,180
Mva 112,208,184
TR 200,19,17408
Mva 120,208,188
TR 202,19,25600
s_waitcnt lgkmcnt(6)
Mva 100,212,176
Mva 108,212,180
Mva 116,212,184
TR 204,20,17408
Mva 124,212,188
TR 206,20,25600
s_waitcnt vmcnt(4)
s_barrier
s_waitcnt lgkmcnt(6)
Zva 128,192,0
s_mov_b32 m0, s68
Zva 136,192,4
s_mov_b32 s15, 4864
buffer_load_dwordx4 v18, s[24:27], s15 offen offset:0 lds
Zva 144,192,8
VC 96,96,97
VC 97,98,99
Zva 152,192,12
VC 98,100,101
VC 99,102,103
s_waitcnt lgkmcnt(4)
Zva 132,196,0
s_mov_b32 s15, 40704
buffer_load_dwordx4 v18, s[24:27], s15 offen offset:1024 lds
Zva 140,196,4
s_mov_b32 s67, 3072
buffer_store_dwordx4 v[96:99], v21, s[52:55], s67 offen sc1
Zva 148,196,8
TR 208,19,18432
VC 104,104,105
Zva 156,196,12
s_mov_b32 s15, 76544
buffer_load_dwordx4 v18, s[24:27], s15 offen offset:2048 lds
s_waitcnt lgkmcnt(3)
Mva 128,200,16
TR 210,19,26624
VC 105,106,107
Mva 136,200,20
VC 106,108,109
VC 107,110,111
Mva 144,200,24
s_mov_b32 s15, 112384
buffer_load_dwordx4 v18, s[24:27], s15 offen offset:3072 lds
Mva 152,200,28
TR 212,20,18432
s_waitcnt lgkmcnt(3)
Mva 132,204,16
TR 214,20,26624
Mva 140,204,20
s_mov_b32 s67, 12288
buffer_store_dwordx4 v[104:107], v21, s[52:55], s67 offen sc1
Mva 148,204,24
VC 112,112,113
VC 113,114,115
Mva 156,204,28
TR 192,19,19456
VC 114,116,117
s_waitcnt lgkmcnt(3)
Mva 128,208,32
TR 194,19,27648
VC 115,118,119
Mva 136,208,36
s_mov_b32 s67, 21504
buffer_store_dwordx4 v[112:115], v21, s[52:55], s67 offen sc1
Mva 144,208,40
TR 196,20,19456
VC 120,120,121
Mva 152,208,44
TR 198,20,27648
VC 121,122,123
s_waitcnt lgkmcnt(4)
Mva 132,212,32
VC 122,124,125
VC 123,126,127
Mva 140,212,36
TR 200,19,32768
Mva 148,212,40
TR 202,19,40960
Mva 156,212,44
s_mov_b32 s67, 30720
buffer_store_dwordx4 v[120:123], v21, s[52:55], s67 offen sc1
s_waitcnt lgkmcnt(4)
Mva 128,192,48
TR 204,20,32768
Mva 136,192,52
TR 206,20,40960
Mva 144,192,56
TR 208,19,33792
Mva 152,192,60
VC 224,224,225
VC 225,226,227
s_waitcnt lgkmcnt(5)
Mva 132,196,48
VC 226,228,229
VC 227,230,231
Mva 140,196,52
buffer_load_dwordx2 v[116:117], v183, s[60:63], 0 offen sc1
Mva 148,196,56
s_mov_b32 s67, 4096
buffer_store_dwordx4 v[224:227], v21, s[52:55], s67 offen sc1
Mva 156,196,60
buffer_load_dwordx2 v[96:97], v184, s[48:51], 0 offen offset:0
s_waitcnt vmcnt(11)
s_barrier
s_waitcnt lgkmcnt(3)
Mva 128,200,64
s_mov_b32 m0, s69
TR 210,19,41984
Mva 136,200,68
s_mov_b32 s15, 152320
buffer_load_dwordx4 v18, s[24:27], s15 offen offset:0 lds
Mva 144,200,72
VC 232,232,233
VC 233,234,235
Mva 152,200,76
VC 234,236,237
TR 212,20,33792
s_waitcnt lgkmcnt(3)
Mva 132,204,64
buffer_load_dwordx2 v[106:107], v185, s[20:23], 0 offen offset:0
Mva 140,204,68
s_mov_b32 s15, 188160
buffer_load_dwordx4 v18, s[24:27], s15 offen offset:1024 lds
VC 235,238,239
Mva 148,204,72
TR 214,20,41984
Mva 156,204,76
s_mov_b32 s67, 13312
buffer_store_dwordx4 v[232:235], v21, s[52:55], s67 offen sc1
buffer_load_dwordx2 v[98:99], v184, s[48:51], 0 offen offset:256
s_waitcnt lgkmcnt(2)
Mva 128,208,80
s_mov_b32 s15, 224000
buffer_load_dwordx4 v18, s[24:27], s15 offen offset:2048 lds
VC 240,240,241
Mva 136,208,84
TR 192,19,34816
VC 241,242,243
Mva 144,208,88
TR 194,19,43008
VC 242,244,245
buffer_load_dwordx2 v[108:109], v185, s[20:23], 0 offen offset:256
Mva 152,208,92
s_mov_b32 s15, 259840
buffer_load_dwordx4 v18, s[24:27], s15 offen offset:3072 lds
VC 243,246,247
s_waitcnt lgkmcnt(2)
Mva 132,212,80
buffer_load_dwordx2 v[100:101], v184, s[48:51], 0 offen offset:512
Mva 140,212,84
TR 196,20,34816
s_mov_b32 s67, 22528
buffer_store_dwordx4 v[240:243], v21, s[52:55], s67 offen sc1
Mva 148,212,88
TR 198,20,43008
VC 248,248,249
Mva 156,212,92
VC 249,250,251
buffer_load_dwordx2 v[110:111], v185, s[20:23], 0 offen offset:512
s_waitcnt lgkmcnt(2)
Mva 128,192,96
VC 250,252,253
TR 200,19,35840
Mva 136,192,100
VC 251,254,255
TR 202,19,44032
Mva 144,192,104
buffer_load_dwordx2 v[102:103], v184, s[48:51], 0 offen offset:768
Mva 152,192,108
s_mov_b32 s67, 31744
buffer_store_dwordx4 v[248:251], v21, s[52:55], s67 offen sc1
s_waitcnt lgkmcnt(2)
Mva 132,196,96
TR 204,20,35840
TR 206,20,44032
Mva 140,196,100
buffer_load_dwordx2 v[112:113], v185, s[20:23], 0 offen offset:768
Mva 148,196,104
buffer_load_dwordx2 v[104:105], v184, s[48:51], 0 offen offset:1024
Mva 156,196,108
TR 208,19,49152
s_waitcnt lgkmcnt(3)
Mva 128,200,112
TR 210,19,57344
Mva 136,200,116
buffer_load_dwordx2 v[114:115], v185, s[20:23], 0 offen offset:1024
Mva 144,200,120
TR 212,20,49152
Mva 152,200,124
TR 214,20,57344
s_waitcnt lgkmcnt(4)
Mva 132,204,112
TR 192,19,50176
Mva 140,204,116
TR 194,19,58368
Mva 148,204,120
TR 196,20,50176
Mva 156,204,124
TR 198,20,58368
s_waitcnt vmcnt(22)
s_barrier
s_waitcnt lgkmcnt(6)
Mva 128,208,128
s_mov_b32 m0, s70
Mva 136,208,132
s_mov_b32 s15, 299776
buffer_load_dwordx4 v18, s[24:27], s15 offen offset:0 lds
Mva 144,208,136
TR 200,19,51200
Mva 152,208,140
TR 202,19,59392
s_waitcnt lgkmcnt(6)
Mva 132,212,128
s_mov_b32 s15, 335616
buffer_load_dwordx4 v18, s[24:27], s15 offen offset:1024 lds
Mva 140,212,132
Mva 148,212,136
TR 204,20,51200
Mva 156,212,140
s_mov_b32 s15, 371456
buffer_load_dwordx4 v18, s[24:27], s15 offen offset:2048 lds
s_waitcnt lgkmcnt(5)
Mva 128,192,144
TR 206,20,59392
Mva 136,192,148
Mva 144,192,152
s_mov_b32 s15, 407296
buffer_load_dwordx4 v18, s[24:27], s15 offen offset:3072 lds
Mva 152,192,156
TR 208,19,52224
s_waitcnt lgkmcnt(5)
Mva 132,196,144
TR 210,19,60416
Mva 140,196,148
Mva 148,196,152
TR 212,20,52224
Mva 156,196,156
TR 214,20,60416
s_waitcnt lgkmcnt(6)
Mva 128,200,160
Mva 136,200,164
Mva 144,200,168
TR 192,19,0
Mva 152,200,172
TR 194,19,8192
s_waitcnt lgkmcnt(6)
Mva 132,204,160
Mva 140,204,164
Mva 148,204,168
TR 196,20,0
Mva 156,204,172
TR 198,20,8192
s_waitcnt lgkmcnt(6)
Mva 128,208,176
Mva 136,208,180
Mva 144,208,184
TR 200,19,1024
Mva 152,208,188
s_waitcnt vmcnt(23)
buffer_store_dwordx2 v[186:187], v191, s[56:59], 0 offen offset:0 sc1
s_waitcnt lgkmcnt(5)
Mva 132,212,176
TR 202,19,9216
Mva 140,212,180
Mva 148,212,184
TR 204,20,1024
Mva 156,212,188
TR 206,20,9216
s_waitcnt vmcnt(13)
s_barrier
s_waitcnt lgkmcnt(6)
Zva 32,192,0
s_mov_b32 m0, s71
Zva 40,192,4
s_mov_b32 s15, 4992
buffer_load_dwordx4 v18, s[24:27], s15 offen offset:0 lds
Zva 48,192,8
VC 128,128,129
VC 129,130,131
Zva 56,192,12
VC 130,132,133
VC 131,134,135
s_waitcnt lgkmcnt(4)
Zva 36,196,0
s_mov_b32 s15, 40832
buffer_load_dwordx4 v18, s[24:27], s15 offen offset:1024 lds
Zva 44,196,4
s_mov_b32 s67, 5120
buffer_store_dwordx4 v[128:131], v21, s[52:55], s67 offen sc1
Zva 52,196,8
TR 208,19,2048
VC 136,136,137
Zva 60,196,12
s_mov_b32 s15, 76672
buffer_load_dwordx4 v18, s[24:27], s15 offen offset:2048 lds
s_waitcnt lgkmcnt(3)
Mva 32,200,16
TR 210,19,10240
VC 137,138,139
Mva 40,200,20
VC 138,140,141
VC 139,142,143
Mva 48,200,24
s_mov_b32 s15, 112512
buffer_load_dwordx4 v18, s[24:27], s15 offen offset:3072 lds
Mva 56,200,28
TR 212,20,2048
s_waitcnt lgkmcnt(3)
Mva 36,204,16
TR 214,20,10240
Mva 44,204,20
s_mov_b32 s67, 14336
buffer_store_dwordx4 v[136:139], v21, s[52:55], s67 offen sc1
Mva 52,204,24
VC 144,144,145
VC 145,146,147
Mva 60,204,28
TR 192,19,3072
VC 146,148,149
s_waitcnt lgkmcnt(3)
Mva 32,208,32
TR 194,19,11264
VC 147,150,151
Mva 40,208,36
s_mov_b32 s67, 23552
buffer_store_dwordx4 v[144:147], v21, s[52:55], s67 offen sc1
Mva 48,208,40
TR 196,20,3072
VC 152,152,153
Mva 56,208,44
TR 198,20,11264
VC 153,154,155
s_waitcnt lgkmcnt(4)
Mva 36,212,32
VC 154,156,157
VC 155,158,159
Mva 44,212,36
TR 200,19,16384
Mva 52,212,40
TR 202,19,24576
Mva 60,212,44
s_mov_b32 s67, 32768
buffer_store_dwordx4 v[152:155], v21, s[52:55], s67 offen sc1
s_waitcnt lgkmcnt(4)
Mva 32,192,48
TR 204,20,16384
Mva 40,192,52
TR 206,20,24576
Mva 48,192,56
TR 208,19,17408
Mva 56,192,60
TR 210,19,25600
s_waitcnt lgkmcnt(6)
Mva 36,196,48
v_rcp_f32 v117, v117
Mva 44,196,52
VM 116,116,117
v_lshlrev_b32 v160, 16, v96
Mva 52,196,56
TR 212,20,17408
v_and_b32 v161, 0xffff0000, v96
Mva 60,196,60
TR 214,20,25600
v_lshlrev_b32 v120, 16, v106
s_waitcnt vmcnt(9)
s_barrier
s_waitcnt lgkmcnt(6)
Mva 32,200,64
s_mov_b32 m0, s68
v_and_b32 v106, 0xffff0000, v106
Mva 40,200,68
s_mov_b32 s15, 152448
buffer_load_dwordx4 v18, s[24:27], s15 offen offset:0 lds
Mva 48,200,72
TR 192,19,18432
v_fma_f32 v96, v160, v117, -v116
Mva 56,200,76
TR 194,19,26624
v_fma_f32 v121, v161, v117, -v116
s_waitcnt lgkmcnt(6)
Mva 36,204,64
s_mov_b32 s15, 188288
buffer_load_dwordx4 v18, s[24:27], s15 offen offset:1024 lds
Mva 44,204,68
VF 160,96,120,160
VF 161,121,106,161
Mva 52,204,72
TR 196,20,18432
v_lshlrev_b32 v162, 16, v97
Mva 60,204,76
s_mov_b32 s15, 224128
buffer_load_dwordx4 v18, s[24:27], s15 offen offset:2048 lds
s_waitcnt lgkmcnt(5)
Mva 32,208,80
TR 198,20,26624
v_and_b32 v163, 0xffff0000, v97
Mva 40,208,84
v_lshlrev_b32 v120, 16, v107
v_and_b32 v107, 0xffff0000, v107
Mva 48,208,88
s_mov_b32 s15, 259968
buffer_load_dwordx4 v18, s[24:27], s15 offen offset:3072 lds
Mva 56,208,92
TR 200,19,19456
v_fma_f32 v97, v162, v117, -v116
s_waitcnt lgkmcnt(5)
Mva 36,212,80
TR 202,19,27648
v_fma_f32 v121, v163, v117, -v116
Mva 44,212,84
VF 162,97,120,162
VF 163,121,107,163
Mva 52,212,88
TR 204,20,19456
v_lshlrev_b32 v164, 16, v98
Mva 60,212,92
TR 206,20,27648
v_and_b32 v165, 0xffff0000, v98
s_waitcnt lgkmcnt(6)
Mva 32,192,96
v_lshlrev_b32 v120, 16, v108
v_and_b32 v108, 0xffff0000, v108
Mva 40,192,100
v_fma_f32 v98, v164, v117, -v116
v_fma_f32 v121, v165, v117, -v116
Mva 48,192,104
TR 208,19,32768
VF 164,98,120,164
Mva 56,192,108
TR 210,19,40960
VF 165,121,108,165
s_waitcnt lgkmcnt(6)
Mva 36,196,96
v_lshlrev_b32 v166, 16, v99
v_and_b32 v167, 0xffff0000, v99
Mva 44,196,100
v_lshlrev_b32 v120, 16, v109
v_and_b32 v109, 0xffff0000, v109
Mva 52,196,104
TR 212,20,32768
v_fma_f32 v99, v166, v117, -v116
Mva 60,196,108
TR 214,20,40960
v_fma_f32 v121, v167, v117, -v116
s_waitcnt lgkmcnt(6)
Mva 32,200,112
VF 166,99,120,166
VF 167,121,109,167
Mva 40,200,116
v_lshlrev_b32 v168, 16, v100
v_and_b32 v169, 0xffff0000, v100
Mva 48,200,120
TR 192,19,33792
v_lshlrev_b32 v120, 16, v110
Mva 56,200,124
TR 194,19,41984
v_and_b32 v110, 0xffff0000, v110
s_waitcnt lgkmcnt(6)
Mva 36,204,112
v_fma_f32 v100, v168, v117, -v116
v_fma_f32 v121, v169, v117, -v116
Mva 44,204,116
VF 168,100,120,168
VF 169,121,110,169
Mva 52,204,120
TR 196,20,33792
v_lshlrev_b32 v170, 16, v101
Mva 60,204,124
TR 198,20,41984
v_and_b32 v171, 0xffff0000, v101
s_waitcnt vmcnt(7)
s_barrier
s_waitcnt lgkmcnt(6)
Mva 32,208,128
s_mov_b32 m0, s69
v_lshlrev_b32 v120, 16, v111
Mva 40,208,132
s_mov_b32 s15, 299904
buffer_load_dwordx4 v18, s[24:27], s15 offen offset:0 lds
Mva 48,208,136
TR 200,19,34816
v_and_b32 v111, 0xffff0000, v111
Mva 56,208,140
TR 202,19,43008
v_fma_f32 v101, v170, v117, -v116
s_waitcnt lgkmcnt(6)
Mva 36,212,128
s_mov_b32 s15, 335744
buffer_load_dwordx4 v18, s[24:27], s15 offen offset:1024 lds
Mva 44,212,132
v_fma_f32 v121, v171, v117, -v116
VF 170,101,120,170
Mva 52,212,136
TR 204,20,34816
VF 171,121,111,171
Mva 60,212,140
s_mov_b32 s15, 371584
buffer_load_dwordx4 v18, s[24:27], s15 offen offset:2048 lds
s_waitcnt lgkmcnt(5)
Mva 32,192,144
)"
R"(TR 206,20,43008
v_lshlrev_b32 v172, 16, v102
Mva 40,192,148
v_and_b32 v173, 0xffff0000, v102
v_lshlrev_b32 v120, 16, v112
Mva 48,192,152
s_mov_b32 s15, 407424
buffer_load_dwordx4 v18, s[24:27], s15 offen offset:3072 lds
Mva 56,192,156
TR 208,19,35840
v_and_b32 v112, 0xffff0000, v112
s_waitcnt lgkmcnt(5)
Mva 36,196,144
TR 210,19,44032
v_fma_f32 v102, v172, v117, -v116
Mva 44,196,148
v_fma_f32 v121, v173, v117, -v116
VF 172,102,120,172
Mva 52,196,152
TR 212,20,35840
VF 173,121,112,173
Mva 60,196,156
TR 214,20,44032
v_lshlrev_b32 v174, 16, v103
s_waitcnt lgkmcnt(6)
Mva 32,200,160
v_and_b32 v175, 0xffff0000, v103
v_lshlrev_b32 v120, 16, v113
Mva 40,200,164
v_and_b32 v113, 0xffff0000, v113
v_fma_f32 v103, v174, v117, -v116
Mva 48,200,168
TR 192,19,49152
v_fma_f32 v121, v175, v117, -v116
Mva 56,200,172
TR 194,19,57344
VF 174,103,120,174
s_waitcnt lgkmcnt(6)
Mva 36,204,160
VF 175,121,113,175
v_lshlrev_b32 v176, 16, v104
Mva 44,204,164
v_and_b32 v177, 0xffff0000, v104
v_lshlrev_b32 v120, 16, v114
Mva 52,204,168
TR 196,20,49152
v_and_b32 v114, 0xffff0000, v114
Mva 60,204,172
TR 198,20,57344
v_fma_f32 v104, v176, v117, -v116
s_waitcnt lgkmcnt(6)
Mva 32,208,176
v_fma_f32 v121, v177, v117, -v116
VF 176,104,120,176
Mva 40,208,180
VF 177,121,114,177
v_lshlrev_b32 v178, 16, v105
Mva 48,208,184
TR 200,19,50176
v_and_b32 v179, 0xffff0000, v105
Mva 56,208,188
s_waitcnt vmcnt(8)
buffer_store_dwordx2 v[186:187], v191, s[56:59], 0 offen offset:128 sc1
s_waitcnt lgkmcnt(5)
Mva 36,212,176
TR 202,19,58368
v_lshlrev_b32 v120, 16, v115
Mva 44,212,180
v_and_b32 v115, 0xffff0000, v115
v_fma_f32 v105, v178, v117, -v116
Mva 52,212,184
TR 204,20,50176
v_fma_f32 v121, v179, v117, -v116
Mva 60,212,188
TR 206,20,58368
VF 178,105,120,178
s_waitcnt vmcnt(5)
s_barrier
s_waitcnt lgkmcnt(6)
Zva 64,192,0
s_mov_b32 m0, s70
VF 179,121,115,179
Zva 72,192,4
s_mov_b32 s15, 5120
buffer_load_dwordx4 v18, s[24:27], s15 offen offset:0 lds
Zva 80,192,8
VC 32,32,33
VC 33,34,35
Zva 88,192,12
VC 34,36,37
VC 35,38,39
s_waitcnt lgkmcnt(4)
Zva 68,196,0
s_mov_b32 s15, 40960
buffer_load_dwordx4 v18, s[24:27], s15 offen offset:1024 lds
Zva 76,196,4
s_mov_b32 s67, 6144
buffer_store_dwordx4 v[32:35], v21, s[52:55], s67 offen sc1
Zva 84,196,8
TR 208,19,51200
VC 40,40,41
Zva 92,196,12
s_mov_b32 s15, 76800
buffer_load_dwordx4 v18, s[24:27], s15 offen offset:2048 lds
s_waitcnt lgkmcnt(3)
Mva 64,200,16
TR 210,19,59392
VC 41,42,43
Mva 72,200,20
VC 42,44,45
VC 43,46,47
Mva 80,200,24
s_mov_b32 s15, 112640
buffer_load_dwordx4 v18, s[24:27], s15 offen offset:3072 lds
Mva 88,200,28
TR 212,20,51200
v_and_b32 v120, 1, v0
s_waitcnt lgkmcnt(3)
Mva 68,204,16
TR 214,20,59392
v_lshlrev_b32 v120, 4, v120
Mva 76,204,20
s_mov_b32 s67, 15360
buffer_store_dwordx4 v[40:43], v21, s[52:55], s67 offen sc1
Mva 84,204,24
VC 48,48,49
VC 49,50,51
Mva 92,204,28
TR 192,19,52224
VC 50,52,53
s_waitcnt lgkmcnt(3)
Mva 64,208,32
TR 194,19,60416
VC 51,54,55
Mva 72,208,36
s_mov_b32 s67, 24576
buffer_store_dwordx4 v[48:51], v21, s[52:55], s67 offen sc1
Mva 80,208,40
TR 196,20,52224
VC 56,56,57
Mva 88,208,44
TR 198,20,60416
VC 57,58,59
s_waitcnt lgkmcnt(4)
Mva 68,212,32
VC 58,60,61
VC 59,62,63
Mva 76,212,36
TR 200,19,0
v_mov_b32 v121, 0x3f80
Mva 84,212,40
TR 202,19,8192
v_lshlrev_b32 v121, v120, v121
Mva 92,212,44
s_mov_b32 s67, 33792
buffer_store_dwordx4 v[56:59], v21, s[52:55], s67 offen sc1
s_waitcnt lgkmcnt(4)
Mva 64,192,48
TR 204,20,0
v_bfe_u32 v120, v0, 1, 3
Mva 72,192,52
TR 206,20,8192
v_lshrrev_b32 v96, 2, v0
Mva 80,192,56
TR 208,19,1024
v_and_b32 v96, 4, v96
Mva 88,192,60
TR 210,19,9216
v_xor_b32 v120, v120, v96
s_waitcnt lgkmcnt(6)
Mva 68,196,48
v_cmp_eq_u32 vcc, 0, v120
s_nop 1
v_cndmask_b32 v96, 0, v121, vcc
Mva 76,196,52
v_cmp_eq_u32 vcc, 1, v120
s_nop 1
v_cndmask_b32 v97, 0, v121, vcc
Mva 84,196,56
TR 212,20,1024
v_cmp_eq_u32 vcc, 2, v120
Mva 92,196,60
TR 214,20,9216
v_cndmask_b32 v98, 0, v121, vcc
s_waitcnt vmcnt(9)
s_barrier
s_waitcnt lgkmcnt(6)
Mva 64,200,64
s_mov_b32 m0, s71
v_cmp_eq_u32 vcc, 3, v120
Mva 72,200,68
s_mov_b32 s15, 152576
buffer_load_dwordx4 v18, s[24:27], s15 offen offset:0 lds
Mva 80,200,72
TR 192,19,2048
v_cndmask_b32 v99, 0, v121, vcc
Mva 88,200,76
TR 194,19,10240
s_waitcnt lgkmcnt(6)
Mva 68,204,64
s_mov_b32 s15, 188416
buffer_load_dwordx4 v18, s[24:27], s15 offen offset:1024 lds
Mva 76,204,68
Mva 84,204,72
TR 196,20,2048
Mva 92,204,76
s_mov_b32 s15, 224256
buffer_load_dwordx4 v18, s[24:27], s15 offen offset:2048 lds
s_waitcnt lgkmcnt(5)
Mva 64,208,80
TR 198,20,10240
Mva 72,208,84
Mva 80,208,88
s_mov_b32 s15, 260096
buffer_load_dwordx4 v18, s[24:27], s15 offen offset:3072 lds
Mva 88,208,92
TR 200,19,3072
s_waitcnt lgkmcnt(5)
Mva 68,212,80
TR 202,19,11264
Mva 76,212,84
Mva 84,212,88
TR 204,20,3072
Mva 92,212,92
TR 206,20,11264
s_waitcnt lgkmcnt(6)
Mva 64,192,96
Mva 72,192,100
Mva 80,192,104
TR 208,19,16384
Mva 88,192,108
TR 210,19,24576
s_waitcnt lgkmcnt(6)
Mva 68,196,96
Mva 76,196,100
Mva 84,196,104
TR 212,20,16384
Mva 92,196,108
TR 214,20,24576
s_waitcnt lgkmcnt(6)
Mva 64,200,112
Mva 72,200,116
Mva 80,200,120
TR 192,19,17408
Mva 88,200,124
TR 194,19,25600
s_waitcnt lgkmcnt(6)
Mva 68,204,112
Mva 76,204,116
Mva 84,204,120
TR 196,20,17408
Mva 92,204,124
TR 198,20,25600
s_waitcnt vmcnt(7)
s_barrier
s_waitcnt lgkmcnt(6)
Mva 64,208,128
s_mov_b32 m0, s68
Mva 72,208,132
s_mov_b32 s15, 300032
buffer_load_dwordx4 v18, s[24:27], s15 offen offset:0 lds
Mva 80,208,136
TR 200,19,18432
Mva 88,208,140
TR 202,19,26624
s_waitcnt lgkmcnt(6)
Mva 68,212,128
s_mov_b32 s15, 335872
buffer_load_dwordx4 v18, s[24:27], s15 offen offset:1024 lds
Mva 76,212,132
Mva 84,212,136
TR 204,20,18432
Mva 92,212,140
s_mov_b32 s15, 371712
buffer_load_dwordx4 v18, s[24:27], s15 offen offset:2048 lds
s_waitcnt lgkmcnt(5)
Mva 64,192,144
TR 206,20,26624
Mva 72,192,148
Mva 80,192,152
s_mov_b32 s15, 407552
buffer_load_dwordx4 v18, s[24:27], s15 offen offset:3072 lds
Mva 88,192,156
TR 208,19,19456
s_waitcnt lgkmcnt(5)
Mva 68,196,144
TR 210,19,27648
Mva 76,196,148
Mva 84,196,152
TR 212,20,19456
Mva 92,196,156
TR 214,20,27648
s_waitcnt lgkmcnt(6)
Mva 64,200,160
Mva 72,200,164
Mva 80,200,168
buffer_load_dwordx2 v[180:181], v190, s[56:59], 0 offen sc1
Mva 88,200,172
TR 192,19,32768
s_waitcnt lgkmcnt(5)
Mva 68,204,160
TR 194,19,40960
Mva 76,204,164
Mva 84,204,168
TR 196,20,32768
Mva 92,204,172
TR 198,20,40960
s_waitcnt lgkmcnt(6)
Mva 64,208,176
Mva 72,208,180
Mva 80,208,184
TR 200,19,33792
Mva 88,208,188
TR 202,19,41984
s_waitcnt lgkmcnt(6)
Mva 68,212,176
Mva 76,212,180
Mva 84,212,184
TR 204,20,33792
Mva 92,212,188
TR 206,20,41984
s_waitcnt vmcnt(5)
s_barrier
s_waitcnt lgkmcnt(6)
Zva 224,192,0
Zva 232,192,4
Zva 240,192,8
VC 64,64,65
VC 65,66,67
Zva 248,192,12
VC 66,68,69
VC 67,70,71
s_waitcnt lgkmcnt(4)
Zva 228,196,0
s_mov_b32 s67, 7168
buffer_store_dwordx4 v[64:67], v21, s[52:55], s67 offen sc1
Zva 236,196,4
TR 208,19,34816
VC 72,72,73
Zva 244,196,8
TR 210,19,43008
VC 73,74,75
Zva 252,196,12
VC 74,76,77
VC 75,78,79
s_waitcnt lgkmcnt(4)
Mva 224,200,16
TR 212,20,34816
Mva 232,200,20
TR 214,20,43008
Mva 240,200,24
s_mov_b32 s67, 16384
buffer_store_dwordx4 v[72:75], v21, s[52:55], s67 offen sc1
Mva 248,200,28
VC 80,80,81
VC 81,82,83
s_waitcnt lgkmcnt(4)
Mva 228,204,16
TR 192,19,35840
VC 82,84,85
Mva 236,204,20
TR 194,19,44032
VC 83,86,87
Mva 244,204,24
s_mov_b32 s67, 25600
buffer_store_dwordx4 v[80:83], v21, s[52:55], s67 offen sc1
Mva 252,204,28
TR 196,20,35840
VC 88,88,89
s_waitcnt lgkmcnt(5)
Mva 224,208,32
s_waitcnt vmcnt(3)
v_cmp_eq_u32 vcc, s100, v180
v_cmp_eq_u32 s[76:77], s100, v181
s_nop 0
s_and_b64 s[76:77], s[76:77], vcc
s_andn2_b32 s76, 0xffff, s76
s_cselect_b32 s38, 0, s54
s_mov_b64 s[36:37], s[52:53]
s_mov_b32 s39, s55
Mva 232,208,36
TR 198,20,44032
VC 89,90,91
Mva 240,208,40
s_mov_b32 s67, 0
buffer_load_dwordx4 a[192:195], v188, s[36:39], s67 offen sc1
Mva 248,208,44
VC 90,92,93
VC 91,94,95
s_waitcnt lgkmcnt(4)
Mva 228,212,32
TR 200,19,49152
Mva 236,212,36
TR 202,19,57344
Mva 244,212,40
s_mov_b32 s67, 294912
buffer_load_dwordx4 a[196:199], v188, s[36:39], s67 offen sc1
Mva 252,212,44
s_mov_b32 s67, 34816
buffer_store_dwordx4 v[88:91], v21, s[52:55], s67 offen sc1
s_waitcnt lgkmcnt(4)
Mva 224,192,48
TR 204,20,49152
Mva 232,192,52
TR 206,20,57344
Mva 240,192,56
s_mov_b32 s67, 589824
buffer_load_dwordx4 a[200:203], v188, s[36:39], s67 offen sc1
Mva 248,192,60
TR 208,19,50176
s_waitcnt lgkmcnt(5)
Mva 228,196,48
TR 210,19,58368
Mva 236,196,52
Mva 244,196,56
s_mov_b32 s67, 884736
buffer_load_dwordx4 a[204:207], v188, s[36:39], s67 offen sc1
Mva 252,196,60
TR 212,20,50176
s_barrier
s_waitcnt lgkmcnt(5)
Mva 224,200,64
TR 214,20,58368
Mva 232,200,68
Mva 240,200,72
s_mov_b32 s67, 1179648
buffer_load_dwordx4 a[208:211], v188, s[36:39], s67 offen sc1
Mva 248,200,76
TR 192,19,51200
s_waitcnt lgkmcnt(5)
Mva 228,204,64
TR 194,19,59392
Mva 236,204,68
Mva 244,204,72
s_mov_b32 s67, 1474560
buffer_load_dwordx4 a[212:215], v188, s[36:39], s67 offen sc1
Mva 252,204,76
TR 196,20,51200
s_waitcnt lgkmcnt(5)
Mva 224,208,80
TR 198,20,59392
Mva 232,208,84
Mva 240,208,88
s_mov_b32 s67, 1769472
buffer_load_dwordx4 a[216:219], v188, s[36:39], s67 offen sc1
Mva 248,208,92
TR 200,19,52224
s_waitcnt lgkmcnt(5)
Mva 228,212,80
TR 202,19,60416
Mva 236,212,84
Mva 244,212,88
s_mov_b32 s67, 2064384
buffer_load_dwordx4 a[220:223], v188, s[36:39], s67 offen sc1
Mva 252,212,92
TR 204,20,52224
s_waitcnt lgkmcnt(5)
Mva 224,192,96
TR 206,20,60416
Mva 232,192,100
Mva 240,192,104
s_mov_b32 s67, 2048
buffer_load_dwordx4 a[224:227], v188, s[36:39], s67 offen sc1
Mva 248,192,108
TR 208,19,0
s_waitcnt lgkmcnt(5)
Mva 228,196,96
TR 210,19,8192
Mva 236,196,100
Mva 244,196,104
s_mov_b32 s67, 296960
buffer_load_dwordx4 a[228:231], v188, s[36:39], s67 offen sc1
Mva 252,196,108
TR 212,20,0
s_waitcnt lgkmcnt(5)
Mva 224,200,112
buffer_load_dwordx2 v[118:119], v190, s[56:59], 0 offen sc1
Mva 232,200,116
TR 214,20,8192
Mva 240,200,120
s_mov_b32 s67, 591872
buffer_load_dwordx4 a[232:235], v188, s[36:39], s67 offen sc1
Mva 248,200,124
TR 192,19,1024
s_waitcnt lgkmcnt(5)
Mva 228,204,112
TR 194,19,9216
Mva 236,204,116
Mva 244,204,120
s_mov_b32 s67, 886784
buffer_load_dwordx4 a[236:239], v188, s[36:39], s67 offen sc1
Mva 252,204,124
TR 196,20,1024
s_waitcnt lgkmcnt(5)
Mva 224,208,128
TR 198,20,9216
Mva 232,208,132
Mva 240,208,136
s_mov_b32 s67, 1181696
buffer_load_dwordx4 a[240:243], v188, s[36:39], s67 offen sc1
Mva 248,208,140
TR 200,19,2048
s_waitcnt lgkmcnt(5)
Mva 228,212,128
TR 202,19,10240
Mva 236,212,132
Mva 244,212,136
s_mov_b32 s67, 1476608
buffer_load_dwordx4 a[244:247], v188, s[36:39], s67 offen sc1
Mva 252,212,140
TR 204,20,2048
s_waitcnt lgkmcnt(5)
Mva 224,192,144
TR 206,20,10240
Mva 232,192,148
Mva 240,192,152
s_mov_b32 s67, 1771520
buffer_load_dwordx4 a[248:251], v188, s[36:39], s67 offen sc1
Mva 248,192,156
TR 208,19,3072
s_waitcnt lgkmcnt(5)
Mva 228,196,144
TR 210,19,11264
Mva 236,196,148
Mva 244,196,152
s_mov_b32 s67, 2066432
buffer_load_dwordx4 a[252:255], v188, s[36:39], s67 offen sc1
Mva 252,196,156
TR 212,20,3072
s_waitcnt lgkmcnt(5)
Mva 224,200,160
TR 214,20,11264
Mva 232,200,164
Mva 240,200,168
Mva 248,200,172
s_waitcnt lgkmcnt(4)
Mva 228,204,160
Mva 236,204,164
Mva 244,204,168
Mva 252,204,172
s_waitcnt lgkmcnt(2)
Mva 224,208,176
Mva 232,208,180
Mva 240,208,184
Mva 248,208,188
s_waitcnt lgkmcnt(0)
Mva 228,212,176
Mva 236,212,180
Mva 244,212,184
Mva 252,212,188
VC 224,224,225
VC 225,226,227
s_nop 2
VC 226,228,229
VC 227,230,231
s_mov_b32 s67, 8192
buffer_store_dwordx4 v[224:227], v21, s[52:55], s67 offen sc1
VC 232,232,233
VC 233,234,235
VC 234,236,237
VC 235,238,239
s_mov_b32 s67, 17408
buffer_store_dwordx4 v[232:235], v21, s[52:55], s67 offen sc1
VC 240,240,241
VC 241,242,243
VC 242,244,245
VC 243,246,247
s_mov_b32 s67, 26624
buffer_store_dwordx4 v[240:243], v21, s[52:55], s67 offen sc1
VC 248,248,249
VC 249,250,251
VC 250,252,253
VC 251,254,255
s_mov_b32 s67, 35840
buffer_store_dwordx4 v[248:251], v21, s[52:55], s67 offen sc1
s_nop 0
s_waitcnt vmcnt(19)
buffer_store_dwordx2 v[186:187], v191, s[56:59], 0 offen offset:256 sc1
s_cmp_eq_u32 s38, 0
s_cbranch_scc1 LEB6
LFB6:
LCB61:
s_waitcnt vmcnt(11)
v_cmp_eq_u32 vcc, s100, v118
v_cmp_eq_u32 s[76:77], s100, v119
s_nop 0
s_and_b64 s[76:77], s[76:77], vcc
s_andn2_b32 s76, 0xffff0000, s76
s_cbranch_scc1 LRB61
LGB61:
s_mov_b32 s67, 4096
buffer_load_dwordx4 a[0:3], v188, s[52:55], s67 offen sc1
s_mov_b32 s67, 299008
buffer_load_dwordx4 a[4:7], v188, s[52:55], s67 offen sc1
s_mov_b32 s67, 593920
buffer_load_dwordx4 a[8:11], v188, s[52:55], s67 offen sc1
s_mov_b32 s67, 888832
buffer_load_dwordx4 a[12:15], v188, s[52:55], s67 offen sc1
s_mov_b32 s67, 1183744
buffer_load_dwordx4 a[16:19], v188, s[52:55], s67 offen sc1
)"
R"(s_mov_b32 s67, 1478656
buffer_load_dwordx4 a[20:23], v188, s[52:55], s67 offen sc1
s_mov_b32 s67, 1773568
buffer_load_dwordx4 a[24:27], v188, s[52:55], s67 offen sc1
s_mov_b32 s67, 2068480
buffer_load_dwordx4 a[28:31], v188, s[52:55], s67 offen sc1
s_waitcnt vmcnt(9)
buffer_store_dwordx2 v[186:187], v191, s[56:59], 0 offen offset:384 sc1
buffer_load_dwordx2 v[118:119], v190, s[56:59], 0 offen sc1
Mva 160,96,192
Mva 164,96,224
Mva 160,96,196
Mva 164,96,228
Mva 160,96,200
Mva 164,96,232
Mva 160,96,204
Mva 164,96,236
Mva 160,96,208
Mva 164,96,240
Mva 160,96,212
Mva 164,96,244
Mva 160,96,216
Mva 164,96,248
Mva 160,96,220
Mva 164,96,252
s_nop 6
buffer_store_dwordx4 v[160:163], v189, s[16:19], 0 offen offset:0 sc1
buffer_store_dwordx4 v[164:167], v189, s[16:19], 0 offen offset:512 sc1
LCB62:
s_waitcnt vmcnt(2)
v_cmp_eq_u32 vcc, s100, v118
v_cmp_eq_u32 s[76:77], s100, v119
s_nop 0
s_and_b64 s[76:77], s[76:77], vcc
s_andn2_b32 s76, 0xffff, s77
s_cbranch_scc1 LRB62
LGB62:
buffer_load_dwordx2 v[118:119], v190, s[56:59], 0 offen sc1
s_mov_b32 s67, 6144
buffer_load_dwordx4 a[32:35], v188, s[52:55], s67 offen sc1
s_mov_b32 s67, 301056
buffer_load_dwordx4 a[36:39], v188, s[52:55], s67 offen sc1
s_mov_b32 s67, 595968
buffer_load_dwordx4 a[40:43], v188, s[52:55], s67 offen sc1
s_mov_b32 s67, 890880
buffer_load_dwordx4 a[44:47], v188, s[52:55], s67 offen sc1
s_mov_b32 s67, 1185792
buffer_load_dwordx4 a[48:51], v188, s[52:55], s67 offen sc1
s_mov_b32 s67, 1480704
buffer_load_dwordx4 a[52:55], v188, s[52:55], s67 offen sc1
s_mov_b32 s67, 1775616
buffer_load_dwordx4 a[56:59], v188, s[52:55], s67 offen sc1
s_mov_b32 s67, 2070528
buffer_load_dwordx4 a[60:63], v188, s[52:55], s67 offen sc1
Mva 168,96,0
Mva 168,96,4
Mva 168,96,8
Mva 168,96,12
Mva 168,96,16
Mva 168,96,20
Mva 168,96,24
Mva 168,96,28
s_nop 7
buffer_store_dwordx4 v[168:171], v189, s[16:19], 0 offen offset:1024 sc1
LCB63:
s_waitcnt vmcnt(9)
v_cmp_eq_u32 vcc, s100, v118
v_cmp_eq_u32 s[76:77], s100, v119
s_nop 0
s_and_b64 s[76:77], s[76:77], vcc
s_andn2_b32 s76, 0xffff0000, s77
s_cbranch_scc1 LRB63
LGB63:
s_cmp_lg_u32 s65, 0
s_cselect_b32 s54, 0, s54
s_mov_b32 s67, 8192
buffer_load_dwordx4 a[64:67], v188, s[52:55], s67 offen sc1
s_mov_b32 s67, 303104
buffer_load_dwordx4 a[68:71], v188, s[52:55], s67 offen sc1
s_mov_b32 s67, 598016
buffer_load_dwordx4 a[72:75], v188, s[52:55], s67 offen sc1
s_mov_b32 s67, 892928
buffer_load_dwordx4 a[76:79], v188, s[52:55], s67 offen sc1
s_mov_b32 s67, 1187840
buffer_load_dwordx4 a[80:83], v188, s[52:55], s67 offen sc1
s_mov_b32 s67, 1482752
buffer_load_dwordx4 a[84:87], v188, s[52:55], s67 offen sc1
s_mov_b32 s67, 1777664
buffer_load_dwordx4 a[88:91], v188, s[52:55], s67 offen sc1
s_mov_b32 s67, 2072576
buffer_load_dwordx4 a[92:95], v188, s[52:55], s67 offen sc1
s_waitcnt vmcnt(16)
Mva 172,96,32
s_waitcnt vmcnt(15)
Mva 172,96,36
s_waitcnt vmcnt(14)
Mva 172,96,40
s_waitcnt vmcnt(13)
Mva 172,96,44
s_waitcnt vmcnt(12)
Mva 172,96,48
s_waitcnt vmcnt(11)
Mva 172,96,52
s_waitcnt vmcnt(10)
Mva 172,96,56
s_waitcnt vmcnt(9)
Mva 172,96,60
s_nop 7
buffer_store_dwordx4 v[172:175], v189, s[16:19], 0 offen offset:1536 sc1
s_waitcnt vmcnt(8)
Mva 176,96,64
s_waitcnt vmcnt(7)
Mva 176,96,68
s_waitcnt vmcnt(6)
Mva 176,96,72
s_waitcnt vmcnt(5)
Mva 176,96,76
s_waitcnt vmcnt(4)
Mva 176,96,80
s_waitcnt vmcnt(3)
Mva 176,96,84
s_waitcnt vmcnt(2)
Mva 176,96,88
s_waitcnt vmcnt(1)
Mva 176,96,92
s_cmp_lg_u32 s65, 0
s_cselect_b32 s18, 0, s18
s_nop 5
buffer_store_dwordx4 v[176:179], v189, s[16:19], 0 offen offset:2048 sc1
s_lshl_b32 s90, s14, 7
s_add_u32 s90, s90, 196608
s_add_u32 s91, s13, s11
s_cmp_eq_u32 s91, 0
s_cselect_b32 s90, s90, 0x80000000
v_mov_b32 v1, s90
buffer_store_dword v186, v1, s[56:59], 0 offen sc1
s_endpgm
LRB60:
s_sleep 8
buffer_load_dwordx2 v[118:119], v190, s[56:59], 0 offen sc1
s_waitcnt vmcnt(0)
s_sub_u32 s92, s92, 1
s_cbranch_scc1 LGB60
s_branch LCB60
LEB6:
s_waitcnt vmcnt(0)
buffer_store_dwordx2 v[186:187], v191, s[56:59], 0 offen offset:384 sc1
LCB60:
v_cmp_eq_u32 vcc, s100, v118
v_cmp_eq_u32 s[76:77], s100, v119
s_nop 0
s_and_b64 s[76:77], s[76:77], vcc
s_andn2_b32 s76, 0xffff, s76
s_cbranch_scc1 LRB60
LGB60:
s_mov_b32 s67, 0
buffer_load_dwordx4 a[192:195], v188, s[52:55], s67 offen sc1
s_mov_b32 s67, 294912
buffer_load_dwordx4 a[196:199], v188, s[52:55], s67 offen sc1
s_mov_b32 s67, 589824
buffer_load_dwordx4 a[200:203], v188, s[52:55], s67 offen sc1
s_mov_b32 s67, 884736
buffer_load_dwordx4 a[204:207], v188, s[52:55], s67 offen sc1
s_mov_b32 s67, 1179648
buffer_load_dwordx4 a[208:211], v188, s[52:55], s67 offen sc1
s_mov_b32 s67, 1474560
buffer_load_dwordx4 a[212:215], v188, s[52:55], s67 offen sc1
s_mov_b32 s67, 1769472
buffer_load_dwordx4 a[216:219], v188, s[52:55], s67 offen sc1
s_mov_b32 s67, 2064384
buffer_load_dwordx4 a[220:223], v188, s[52:55], s67 offen sc1
s_mov_b32 s67, 2048
buffer_load_dwordx4 a[224:227], v188, s[52:55], s67 offen sc1
s_mov_b32 s67, 296960
buffer_load_dwordx4 a[228:231], v188, s[52:55], s67 offen sc1
s_mov_b32 s67, 591872
buffer_load_dwordx4 a[232:235], v188, s[52:55], s67 offen sc1
s_mov_b32 s67, 886784
buffer_load_dwordx4 a[236:239], v188, s[52:55], s67 offen sc1
s_mov_b32 s67, 1181696
buffer_load_dwordx4 a[240:243], v188, s[52:55], s67 offen sc1
s_mov_b32 s67, 1476608
buffer_load_dwordx4 a[244:247], v188, s[52:55], s67 offen sc1
s_mov_b32 s67, 1771520
buffer_load_dwordx4 a[248:251], v188, s[52:55], s67 offen sc1
s_mov_b32 s67, 2066432
buffer_load_dwordx4 a[252:255], v188, s[52:55], s67 offen sc1
buffer_load_dwordx2 v[118:119], v190, s[56:59], 0 offen sc1
s_waitcnt vmcnt(0)
s_branch LFB6
LRB61:
s_sleep 4
buffer_load_dwordx2 v[118:119], v190, s[56:59], 0 offen sc1
s_waitcnt vmcnt(0)
s_sub_u32 s92, s92, 1
s_cbranch_scc1 LGB61
s_branch LCB61
LRB62:
s_sleep 2
buffer_load_dwordx2 v[118:119], v190, s[56:59], 0 offen sc1
s_waitcnt vmcnt(0)
s_sub_u32 s92, s92, 1
s_cbranch_scc1 LGB62
s_branch LCB62
LRB63:
s_sleep 1
buffer_load_dwordx2 v[118:119], v190, s[56:59], 0 offen sc1
s_waitcnt vmcnt(0)
s_sub_u32 s92, s92, 1
s_cbranch_scc1 LGB63
s_branch LCB63
LENDB6:

)"
      :
      : "s"(__builtin_amdgcn_kernarg_segment_ptr()), "s"(blockIdx.x), "v"(threadIdx.x), "s"((unsigned)(size_t)smem)
      :
      "v1", "v2", "v3", "v4", "v5", "v6", "v7", "v8", "v9", "v10", "v11", "v12", "v13", "v14", "v15", "v16",
      "v17", "v18", "v19", "v20", "v21", "v22", "v23", "v24", "v25", "v26", "v27", "v28", "v29", "v30", "v31",
      "v32", "v33", "v34", "v35", "v36", "v37", "v38", "v39", "v40", "v41", "v42", "v43", "v44", "v45", "v46",
      "v47", "v48", "v49", "v50", "v51", "v52", "v53", "v54", "v55", "v56", "v57", "v58", "v59", "v60", "v61",
      "v62", "v63", "v64", "v65", "v66", "v67", "v68", "v69", "v70", "v71", "v72", "v73", "v74", "v75", "v76",
      "v77", "v78", "v79", "v80", "v81", "v82", "v83", "v84", "v85", "v86", "v87", "v88", "v89", "v90", "v91",
      "v92", "v93", "v94", "v95", "v96", "v97", "v98", "v99", "v100", "v101", "v102", "v103", "v104", "v105",
      "v106", "v107", "v108", "v109", "v110", "v111", "v112", "v113", "v114", "v115", "v116", "v117", "v118",
      "v119", "v120", "v121", "v122", "v123", "v124", "v125", "v126", "v127", "v128", "v129", "v130", "v131",
      "v132", "v133", "v134", "v135", "v136", "v137", "v138", "v139", "v140", "v141", "v142", "v143", "v144",
      "v145", "v146", "v147", "v148", "v149", "v150", "v151", "v152", "v153", "v154", "v155", "v156", "v157",
      "v158", "v159", "v160", "v161", "v162", "v163", "v164", "v165", "v166", "v167", "v168", "v169", "v170",
      "v171", "v172", "v173", "v174", "v175", "v176", "v177", "v178", "v179", "v180", "v181", "v182", "v183",
      "v184", "v185", "v186", "v187", "v188", "v189", "v190", "v191", "v192", "v193", "v194", "v195", "v196",
      "v197", "v198", "v199", "v200", "v201", "v202", "v203", "v204", "v205", "v206", "v207", "v208", "v209",
      "v210", "v211", "v212", "v213", "v214", "v215", "v216", "v217", "v218", "v219", "v220", "v221", "v222",
      "v223", "v224", "v225", "v226", "v227", "v228", "v229", "v230", "v231", "v232", "v233", "v234", "v235",
      "v236", "v237", "v238", "v239", "v240", "v241", "v242", "v243", "v244", "v245", "v246", "v247", "v248",
      "v249", "v250", "v251", "v252", "v253", "v254", "v255", "a0", "a1", "a2", "a3", "a4", "a5", "a6", "a7",
      "a8", "a9", "a10", "a11", "a12", "a13", "a14", "a15", "a16", "a17", "a18", "a19", "a20", "a21", "a22",
      "a23", "a24", "a25", "a26", "a27", "a28", "a29", "a30", "a31", "a32", "a33", "a34", "a35", "a36", "a37",
      "a38", "a39", "a40", "a41", "a42", "a43", "a44", "a45", "a46", "a47", "a48", "a49", "a50", "a51", "a52",
      "a53", "a54", "a55", "a56", "a57", "a58", "a59", "a60", "a61", "a62", "a63", "a64", "a65", "a66", "a67",
      "a68", "a69", "a70", "a71", "a72", "a73", "a74", "a75", "a76", "a77", "a78", "a79", "a80", "a81", "a82",
      "a83", "a84", "a85", "a86", "a87", "a88", "a89", "a90", "a91", "a92", "a93", "a94", "a95", "a96", "a97",
      "a98", "a99", "a100", "a101", "a102", "a103", "a104", "a105", "a106", "a107", "a108", "a109", "a110",
      "a111", "a112", "a113", "a114", "a115", "a116", "a117", "a118", "a119", "a120", "a121", "a122", "a123",
      "a124", "a125", "a126", "a127", "a128", "a129", "a130", "a131", "a132", "a133", "a134", "a135", "a136",
      "a137", "a138", "a139", "a140", "a141", "a142", "a143", "a144", "a145", "a146", "a147", "a148", "a149",
      "a150", "a151", "a152", "a153", "a154", "a155", "a156", "a157", "a158", "a159", "a160", "a161", "a162",
      "a163", "a164", "a165", "a166", "a167", "a168", "a169", "a170", "a171", "a172", "a173", "a174", "a175",
      "a176", "a177", "a178", "a179", "a180", "a181", "a182", "a183", "a184", "a185", "a186", "a187", "a188",
      "a189", "a190", "a191", "a192", "a193", "a194", "a195", "a196", "a197", "a198", "a199", "a200", "a201",
      "a202", "a203", "a204", "a205", "a206", "a207", "a208", "a209", "a210", "a211", "a212", "a213", "a214",
      "a215", "a216", "a217", "a218", "a219", "a220", "a221", "a222", "a223", "a224", "a225", "a226", "a227",
      "a228", "a229", "a230", "a231", "a232", "a233", "a234", "a235", "a236", "a237", "a238", "a239", "a240",
      "a241", "a242", "a243", "a244", "a245", "a246", "a247", "a248", "a249", "a250", "a251", "a252", "a253",
      "a254", "a255", "s8", "s9", "s10", "s11", "s12", "s13", "s14", "s15", "s16", "s17", "s18", "s19", "s20",
      "s21", "s22", "s23", "s24", "s25", "s26", "s27", "s28", "s29", "s30", "s31", "s33", "s34", "s35",
      "s36", "s37", "s38", "s39", "s40", "s41", "s42", "s43", "s44", "s45", "s46", "s47", "s48", "s49", "s50",
      "s51", "s52", "s53", "s54", "s55", "s56", "s57", "s58", "s59", "s60", "s61", "s62", "s63", "s64", "s65",
      "s66", "s67", "s68", "s69", "s70", "s71", "s72", "s73", "s74", "s75", "s76", "s77", "s78", "s79", "s80",
      "s81", "s82", "s83", "s84", "s85", "s86", "s87", "s88", "s89", "s90", "s91", "s92", "s93", "s94", "s95",
      "s96", "s97", "s98", "s99", "s100", "s101", "vcc", "scc", "memory");
}

extern "C" void kernel_launch(void* const* d_in, const int* in_sizes, int n_in, void* d_out, int out_size, void* d_ws,
                              size_t ws_size, hipStream_t stream) {
  char* ws = (char*)d_ws + 0;
  fused_main<<<256, 256, 0, stream>>>(d_in[0], d_in[1], d_in[2], d_in[7], d_in[3], d_in[4], d_in[5], d_in[6], d_in[10], d_in[11], d_in[12], ws,
      d_in[8], d_out);
}
```
